```python
import torch

def _softmax(x, axis=-1):
    return torch.softmax(x, dim=axis)

def _gelu(x):
    return torch.nn.functional.gelu(x, approximate='tanh')

def _silu(x):
    return torch.nn.functional.silu(x)

def _sigmoid(x):
    return torch.sigmoid(x)

def _layernorm(x, axis=-1, eps=1e-05):
    m = x.mean(dim=axis, keepdim=True)
    v = x.var(dim=axis, keepdim=True, unbiased=False)
    return (x - m) / torch.sqrt(v + eps)

def _scatter_add(tgt, idx, src, axis=0):
    return tgt.clone().index_add_(axis, idx, src)

def _segment_sum(src, seg, n):
    out = torch.zeros((n,) + src.shape[1:], dtype=src.dtype)
    return out.index_add_(0, seg, src)

def _pad(x, pw):
    flat = tuple((v for pair in reversed(pw) for v in pair))
    return torch.nn.functional.pad(x, flat)
kernel_name = 'parallel_sum__memory_aug_recurrent_mem_tr__mixer_token_mlp__prompt'

def setup_inputs():

    _w = lambda a: (lambda t: t.to(torch.bfloat16) if t.is_floating_point() else t)(torch.as_tensor(a))

    def _A_setup():
        B, S, Dm = (1, 2048, 576)
        H, Dh = (32, 128)
        M, seg = (8, 32)
        seg = min(seg, S)
        S = S // seg * seg
        return {'x': _w((lambda *s: torch.randn(*s, dtype=torch.bfloat16) * 0.05)(B, S, Dm)), 'mem0': _w((lambda *s: torch.randn(*s, dtype=torch.bfloat16) * 0.2)(B, M, Dm)), 'Wq': _w((lambda *s: torch.randn(*s, dtype=torch.bfloat16) * 1.6)(Dm, H, Dh) * Dm ** (-0.5)), 'Wk': _w((lambda *s: torch.randn(*s, dtype=torch.bfloat16) * 1.6)(Dm, H, Dh) * Dm ** (-0.5)), 'Wv': _w((lambda *s: torch.randn(*s, dtype=torch.bfloat16) * 0.2)(Dm, H, Dh) * Dm ** (-0.5)), 'Wo': _w((lambda *s: torch.randn(*s, dtype=torch.bfloat16) * 0.2)(H, Dh, Dm) * (H * Dh) ** (-0.5)), 'seg_len': seg}
    _A_in = _A_setup()

    def _B_setup():
        B, S, D, F = (1, 2048, 576, 4608)
        return {'x': _w((lambda *s: torch.randn(*s, dtype=torch.bfloat16) * 0.05)(B, S, D)), 'w1': _w((lambda *s: torch.randn(*s, dtype=torch.bfloat16) * 0.4)(S, F) * S ** (-0.5)), 'b1': _w((lambda *s: torch.randn(*s, dtype=torch.bfloat16) * 3.2)(F) * 0.02), 'w2': _w((lambda *s: torch.randn(*s, dtype=torch.bfloat16) * 0.2)(F, S) * F ** (-0.5)), 'b2': _w((lambda *s: torch.randn(*s, dtype=torch.bfloat16) * 0.4)(S) * 0.02)}
    _B_in = _B_setup()
    return {'A_x': _A_in['x'], 'A_mem0': _A_in['mem0'], 'A_Wq': _A_in['Wq'], 'A_Wk': _A_in['Wk'], 'A_Wv': _A_in['Wv'], 'A_Wo': _A_in['Wo'], 'A_seg_len': _A_in['seg_len'], 'B_x': _B_in['x'], 'B_w1': _B_in['w1'], 'B_b1': _B_in['b1'], 'B_w2': _B_in['w2'], 'B_b2': _B_in['b2']}

def reference(A_x, A_mem0, A_Wq, A_Wk, A_Wv, A_Wo, A_seg_len, B_x, B_w1, B_b1, B_w2, B_b2):

    def _A_ref(x, mem0, Wq, Wk, Wv, Wo, seg_len):
        B, S, Dm = x.shape
        H, Dh = (Wq.shape[1], Wq.shape[2])
        M = mem0.shape[1]
        scale = Dh ** (-0.5)
        n_seg = S // seg_len
        L = M + seg_len
        idx = torch.arange(L)
        mask = (idx[:, None] < M) | (idx[:, None] >= idx[None, :])
        mem = mem0
        outs = []
        for s in range(n_seg):
            xs = x[:, s * seg_len:(s + 1) * seg_len, :].clone()
            h = torch.concatenate([mem, xs], axis=1)
            q = torch.einsum('btd,dhe->bhte', h, Wq)
            k = torch.einsum('btd,dhe->bhte', h, Wk)
            v = torch.einsum('btd,dhe->bhte', h, Wv)
            scores = torch.einsum('bhqe,bhke->bhqk', q, k) * scale
            scores = torch.where(mask[None, None], scores, -1000000000.0)
            p = _softmax(scores, axis=-1)
            o = torch.einsum('bhqk,bhke->bhqe', p, v)
            o = torch.einsum('bhte,hed->btd', o, Wo)
            mem = o[:, :M, :].clone()
            outs.append(o[:, M:, :])
        return torch.concatenate(outs, axis=1)

    def _B_ref(x, w1, b1, w2, b2):
        B, S, D = x.shape
        h = _layernorm(x, axis=-1)
        u = torch.einsum('bsd,sf->bdf', h, w1) + b1
        u = _gelu(u)
        y = torch.einsum('bdf,fs->bds', u, w2) + b2
        return (lambda x, *d: x.permute(*d))(y, 0, 2, 1)
    x = A_x
    xn = _layernorm(x, axis=-1)
    return x + _A_ref(xn, A_mem0, A_Wq, A_Wk, A_Wv, A_Wo, A_seg_len) + _B_ref(xn, B_w1, B_b1, B_w2, B_b2)
```

```cpp
#include <hip/hip_runtime.h>
#include <hip/hip_bf16.h>
typedef int v4i __attribute__((ext_vector_type(4)));
__device__ __forceinline__ int rfl(int x) { return __builtin_amdgcn_readfirstlane(x); }
__device__ __forceinline__ v4i make_srd(const void* p, unsigned bytes) {
    unsigned long long a = (unsigned long long)p;
    v4i r = {rfl((int)(unsigned)a), rfl((int)(unsigned)(a >> 32)) & 0xffff, (int)bytes, 0x00020000};
    return r;
}
__device__ __forceinline__ int hb_off() { return 1146880 + (blockIdx.x & 7) * 128; }
#define EVC(k)
constexpr int HXV = 0, STX = 6, STY = 6, NFIN = 2;
constexpr unsigned OOB = 0x80000000u;
constexpr unsigned WS_QKV = 2u << 20;
constexpr unsigned WS_OT = 52u << 20;
constexpr unsigned WS_BW = 68u << 20;
constexpr unsigned WS_RING = 84u << 20;
constexpr unsigned WS_UF = 128u << 20;
constexpr unsigned WS_YB = 142u << 20;
constexpr unsigned WS_WT = 160u << 20;
constexpr unsigned WS_B = 170u << 20;

__device__ __forceinline__ void role_x(char* ws, const void* wq, const void* wk, const void* wv, int id, int g1, int g2, int rep) {
    int tid = threadIdx.x; asm volatile("" : "+v"(tid));
    const int w = rfl(tid >> 6), l = tid & 63, g = l >> 4, c = l & 15;
    const int h = id >> 1, eh = id & 1;
    v4i sWS = make_srd(ws, WS_B);
    const v4i sWQ = make_srd(wq, 576 * 8192), sWK = make_srd(wk, 576 * 8192), sWV = make_srd(wv, 576 * 8192);
    const int sg = (g & 1) | (w << 1), sv = ((l >> 5) & 1) | ((w & 1) << 1);
    unsigned vPad = l, vGQ = (4 * w + g) * 8192 + h * 256 + ((c ^ (sg << 1)) * 16);
    unsigned vGV = (8 * w + (l >> 3)) * 8192 + h * 256 + eh * 128 + (((l & 7) ^ (sv << 1)) * 16);
    const int x = c >> 2, p = c & 3, rb = 16 * (g >> 1) + 8 * (x >> 1) + 4 * (g & 1) + (x & 1);
    const int st = (x & 1) | ((g & 1) << 1) | ((x >> 1) << 2), stv = (g & 1) | ((x >> 1) << 1);
    unsigned vTQ0 = rb * 256 + ((2 * w) ^ st) * 32 + p * 8;
    unsigned vTV = rb * 128 + (w ^ stv) * 32 + p * 8;
    const unsigned vL16 = l * 16, vIDX = (c & 2) ? 0xEEEEu : 0x4444u;
    const unsigned vXW = 61440 + w * 1056 + (c >> 2) * 256 + (c >> 3) * 16 + g * 64 + (c & 3) * 4;
    const unsigned vXR = 61440 + g * 256 + (g >> 1) * 16 + c * 16;
    const int tau = 4 * eh + w;
    const unsigned vOST = (c & 2) ? (unsigned)(WS_RING + (c >> 3) * 524288 + (h >> 3) * 32768 + (h & 7) * 1024 + tau * 128 + (4 * ((c >> 2) & 1) + 2 * (g >> 1) + (c & 1)) * 16 + (g & 1) * 8) : OOB;
    const unsigned vOT = WS_OT + (w >> 1) * 131072 + (4 * h + 2 * eh + (w & 1)) * 1024 + l * 16;
    const unsigned vTS = (id == 0 && tid < 8) ? (unsigned)(tid * 128) : OOB;
    const unsigned vPQ = (l < 4 ? (l >> 1) * 64 + 2 * h + (l & 1) : 128 + 2 * h + eh) * 128;
    const unsigned vXO = tid == 0 ? (unsigned)(id * 128) : ((tid == 1 && (id & 15) == 0) ? (unsigned)(1144832 - 1097728 + rep * 128) : OOB);
    const int sW1K = w * 1024, sQOFF = rfl(WS_QKV + h * 24576 + w * 4096), sVOFF = rfl(16384 + tau * 1024 - w * 4096);
    const int sREP = rfl(WS_RING + 1048576 + rep * 262144);
    asm volatile(
"LX_top:\n"
"LX_top0:\n"
"s_nop 4\n"
"v_mbcnt_lo_u32_b32 v49, -1, 0\n"
"v_min_u32 v49, 2, v49\n"
"v_lshlrev_b32 v49, 10, v49\n"
"buffer_load_dword v48, v49, s[32:35], %[sHB] offen sc1\n"
"v_mov_b64 v[10:11], -1\n"
"s_mov_b32 s40, 0\n"
"buffer_store_dwordx2 v[10:11], %[vOST], s[32:35], s40 offen sc1\n"
"s_mov_b32 s40, 8192\n"
"buffer_store_dwordx2 v[10:11], %[vOST], s[32:35], s40 offen sc1\n"
"s_mov_b32 s40, 16384\n"
"buffer_store_dwordx2 v[10:11], %[vOST], s[32:35], s40 offen sc1\n"
"s_mov_b32 s40, 24576\n"
"buffer_store_dwordx2 v[10:11], %[vOST], s[32:35], s40 offen sc1\n"
"s_mov_b32 s40, 131072\n"
"buffer_store_dwordx2 v[10:11], %[vOST], s[32:35], s40 offen sc1\n"
"s_mov_b32 s40, 139264\n"
"buffer_store_dwordx2 v[10:11], %[vOST], s[32:35], s40 offen sc1\n"
"s_mov_b32 s40, 147456\n"
"buffer_store_dwordx2 v[10:11], %[vOST], s[32:35], s40 offen sc1\n"
"s_mov_b32 s40, 155648\n"
"buffer_store_dwordx2 v[10:11], %[vOST], s[32:35], s40 offen sc1\n"
"s_mov_b32 s40, 262144\n"
"buffer_store_dwordx2 v[10:11], %[vOST], s[32:35], s40 offen sc1\n"
"s_mov_b32 s40, 270336\n"
"buffer_store_dwordx2 v[10:11], %[vOST], s[32:35], s40 offen sc1\n"
"s_mov_b32 s40, 278528\n"
"buffer_store_dwordx2 v[10:11], %[vOST], s[32:35], s40 offen sc1\n"
"s_mov_b32 s40, 286720\n"
"buffer_store_dwordx2 v[10:11], %[vOST], s[32:35], s40 offen sc1\n"
"s_mov_b32 s40, 0\n"
"s_add_u32 m0, %[sW1K], 0\n"
"s_nop 0\n"
"buffer_load_dwordx4 v44, %[sWQ], s40 offen  lds\n"
"s_add_u32 m0, %[sW1K], 16384\n"
"s_nop 0\n"
"buffer_load_dwordx4 v44, %[sWK], s40 offen  lds\n"
"s_mov_b32 s40, 131072\n"
"s_add_u32 m0, %[sW1K], 4096\n"
"s_nop 0\n"
"buffer_load_dwordx4 v44, %[sWQ], s40 offen  lds\n"
"s_add_u32 m0, %[sW1K], 20480\n"
"s_nop 0\n"
"buffer_load_dwordx4 v44, %[sWK], s40 offen  lds\n"
"s_mov_b32 s40, 262144\n"
"s_add_u32 m0, %[sW1K], 8192\n"
"s_nop 0\n"
"buffer_load_dwordx4 v44, %[sWQ], s40 offen  lds\n"
"s_add_u32 m0, %[sW1K], 24576\n"
"s_nop 0\n"
"buffer_load_dwordx4 v44, %[sWK], s40 offen  lds\n"
"s_mov_b32 s40, 393216\n"
"s_add_u32 m0, %[sW1K], 12288\n"
"s_nop 0\n"
"buffer_load_dwordx4 v44, %[sWQ], s40 offen  lds\n"
"s_add_u32 m0, %[sW1K], 28672\n"
"s_nop 0\n"
"buffer_load_dwordx4 v44, %[sWK], s40 offen  lds\n"
"s_add_u32 m0, %[sW1K], 32768\n"
"s_mov_b32 s40, 0\n"
"buffer_load_dwordx4 v45, %[sWV], s40 offen  lds\n"
"s_add_u32 m0, %[sW1K], 36864\n"
"s_mov_b32 s40, 262144\n"
"buffer_load_dwordx4 v45, %[sWV], s40 offen  lds\n"
"s_add_u32 m0, %[sW1K], 40960\n"
"s_mov_b32 s40, 524288\n"
"buffer_load_dwordx4 v45, %[sWV], s40 offen  lds\n"
"s_add_u32 m0, %[sW1K], 45056\n"
"s_mov_b32 s40, 786432\n"
"buffer_load_dwordx4 v45, %[sWV], s40 offen  lds\n"
"s_mov_b32 s40, 524288\n"
"s_add_u32 m0, %[sW1K], 49152\n"
"s_nop 0\n"
"buffer_load_dwordx4 v44, %[sWQ], s40 offen  lds\n"
"s_add_u32 m0, %[sW1K], 65536\n"
"s_nop 0\n"
"buffer_load_dwordx4 v44, %[sWK], s40 offen  lds\n"
"s_mov_b32 s40, 655360\n"
"s_add_u32 m0, %[sW1K], 53248\n"
"s_nop 0\n"
"buffer_load_dwordx4 v44, %[sWQ], s40 offen  lds\n"
"s_add_u32 m0, %[sW1K], 69632\n"
"s_nop 0\n"
"buffer_load_dwordx4 v44, %[sWK], s40 offen  lds\n"
"s_mov_b32 s40, 786432\n"
"s_add_u32 m0, %[sW1K], 57344\n"
"s_nop 0\n"
"buffer_load_dwordx4 v44, %[sWQ], s40 offen  lds\n"
"s_add_u32 m0, %[sW1K], 73728\n"
"s_nop 0\n"
"buffer_load_dwordx4 v44, %[sWK], s40 offen  lds\n"
"s_mov_b32 s40, 917504\n"
"s_add_u32 m0, %[sW1K], 61440\n"
"s_nop 0\n"
"buffer_load_dwordx4 v44, %[sWQ], s40 offen  lds\n"
"s_add_u32 m0, %[sW1K], 77824\n"
"s_nop 0\n"
"buffer_load_dwordx4 v44, %[sWK], s40 offen  lds\n"
"s_waitcnt vmcnt(8)\n"
"s_barrier\n"
"s_mov_b32 s40, 1048576\n"
"s_add_u32 m0, %[sW1K], 98304\n"
"s_nop 0\n"
"buffer_load_dwordx4 v44, %[sWQ], s40 offen  lds\n"
"s_add_u32 m0, %[sW1K], 114688\n"
"s_nop 0\n"
"buffer_load_dwordx4 v44, %[sWK], s40 offen  lds\n"
"s_mov_b32 s40, 1179648\n"
"s_add_u32 m0, %[sW1K], 102400\n"
"s_nop 0\n"
"buffer_load_dwordx4 v44, %[sWQ], s40 offen  lds\n"
"s_add_u32 m0, %[sW1K], 118784\n"
"s_nop 0\n"
"buffer_load_dwordx4 v44, %[sWK], s40 offen  lds\n"
"s_mov_b32 s40, 1310720\n"
"s_add_u32 m0, %[sW1K], 106496\n"
"s_nop 0\n"
"buffer_load_dwordx4 v44, %[sWQ], s40 offen  lds\n"
"s_add_u32 m0, %[sW1K], 122880\n"
"s_nop 0\n"
"buffer_load_dwordx4 v44, %[sWK], s40 offen  lds\n"
"s_mov_b32 s40, 1441792\n"
"s_add_u32 m0, %[sW1K], 110592\n"
"s_nop 0\n"
"buffer_load_dwordx4 v44, %[sWQ], s40 offen  lds\n"
"s_add_u32 m0, %[sW1K], 126976\n"
"s_nop 0\n"
"buffer_load_dwordx4 v44, %[sWK], s40 offen  lds\n"
"s_add_u32 m0, %[sW1K], 131072\n"
"s_mov_b32 s40, 1048576\n"
"buffer_load_dwordx4 v45, %[sWV], s40 offen  lds\n"
"s_add_u32 m0, %[sW1K], 135168\n"
"s_mov_b32 s40, 1310720\n"
"buffer_load_dwordx4 v45, %[sWV], s40 offen  lds\n"
"s_add_u32 m0, %[sW1K], 139264\n"
"s_mov_b32 s40, 1572864\n"
"buffer_load_dwordx4 v45, %[sWV], s40 offen  lds\n"
"s_add_u32 m0, %[sW1K], 143360\n"
"s_mov_b32 s40, 1835008\n"
"buffer_load_dwordx4 v45, %[sWV], s40 offen  lds\n"
"v_add_u32 v32, 0, v46\n"
"v_xor_b32 v33, 32, v32\n"
"v_add_u32 v34, 0, v47\n"
"ds_read_b64_tr_b16 a[0:1], v32 offset:0\n"
"ds_read_b64_tr_b16 a[2:3], v32 offset:512\n"
"ds_read_b64_tr_b16 a[4:5], v32 offset:8192\n"
"ds_read_b64_tr_b16 a[6:7], v32 offset:8704\n"
"ds_read_b64_tr_b16 a[8:9], v32 offset:16384\n"
"ds_read_b64_tr_b16 a[10:11], v32 offset:16896\n"
"ds_read_b64_tr_b16 a[12:13], v32 offset:24576\n"
"ds_read_b64_tr_b16 a[14:15], v32 offset:25088\n"
"ds_read_b64_tr_b16 a[16:17], v33 offset:0\n"
"ds_read_b64_tr_b16 a[18:19], v33 offset:512\n"
"ds_read_b64_tr_b16 a[20:21], v33 offset:8192\n"
"ds_read_b64_tr_b16 a[22:23], v33 offset:8704\n"
"s_waitcnt lgkmcnt(0)\n"
"ds_read_b64_tr_b16 a[24:25], v33 offset:16384\n"
"ds_read_b64_tr_b16 a[26:27], v33 offset:16896\n"
"ds_read_b64_tr_b16 a[28:29], v33 offset:24576\n"
"ds_read_b64_tr_b16 a[30:31], v33 offset:25088\n"
"ds_read_b64_tr_b16 v[152:153], v34 offset:32768\n"
"ds_read_b64_tr_b16 v[154:155], v34 offset:33024\n"
"ds_read_b64_tr_b16 v[156:157], v34 offset:36864\n"
"ds_read_b64_tr_b16 v[158:159], v34 offset:37120\n"
"ds_read_b64_tr_b16 v[160:161], v34 offset:40960\n"
"ds_read_b64_tr_b16 v[162:163], v34 offset:41216\n"
"ds_read_b64_tr_b16 v[164:165], v34 offset:45056\n"
"ds_read_b64_tr_b16 v[166:167], v34 offset:45312\n"
"s_waitcnt lgkmcnt(0)\n"
"s_waitcnt vmcnt(12)\n"
"s_barrier\n"
"s_mov_b32 s40, 1572864\n"
"s_add_u32 m0, %[sW1K], 0\n"
"s_nop 0\n"
"buffer_load_dwordx4 v44, %[sWQ], s40 offen  lds\n"
"s_add_u32 m0, %[sW1K], 16384\n"
"s_nop 0\n"
"buffer_load_dwordx4 v44, %[sWK], s40 offen  lds\n"
"s_mov_b32 s40, 1703936\n"
"s_add_u32 m0, %[sW1K], 4096\n"
"s_nop 0\n"
"buffer_load_dwordx4 v44, %[sWQ], s40 offen  lds\n"
"s_add_u32 m0, %[sW1K], 20480\n"
"s_nop 0\n"
"buffer_load_dwordx4 v44, %[sWK], s40 offen  lds\n"
"s_mov_b32 s40, 1835008\n"
"s_add_u32 m0, %[sW1K], 8192\n"
"s_nop 0\n"
"buffer_load_dwordx4 v44, %[sWQ], s40 offen  lds\n"
"s_add_u32 m0, %[sW1K], 24576\n"
"s_nop 0\n"
"buffer_load_dwordx4 v44, %[sWK], s40 offen  lds\n"
"s_mov_b32 s40, 1966080\n"
"s_add_u32 m0, %[sW1K], 12288\n"
"s_nop 0\n"
"buffer_load_dwordx4 v44, %[sWQ], s40 offen  lds\n"
"s_add_u32 m0, %[sW1K], 28672\n"
"s_nop 0\n"
"buffer_load_dwordx4 v44, %[sWK], s40 offen  lds\n"
"v_add_u32 v32, 49152, v46\n"
"v_xor_b32 v33, 32, v32\n"
"v_add_u32 v34, 49152, v47\n"
"ds_read_b64_tr_b16 a[32:33], v32 offset:0\n"
"ds_read_b64_tr_b16 a[34:35], v32 offset:512\n"
"ds_read_b64_tr_b16 a[36:37], v32 offset:8192\n"
"ds_read_b64_tr_b16 a[38:39], v32 offset:8704\n"
"ds_read_b64_tr_b16 a[40:41], v32 offset:16384\n"
"ds_read_b64_tr_b16 a[42:43], v32 offset:16896\n"
"ds_read_b64_tr_b16 a[44:45], v32 offset:24576\n"
"ds_read_b64_tr_b16 a[46:47], v32 offset:25088\n"
"ds_read_b64_tr_b16 a[48:49], v33 offset:0\n"
"ds_read_b64_tr_b16 a[50:51], v33 offset:512\n"
"ds_read_b64_tr_b16 a[52:53], v33 offset:8192\n"
"ds_read_b64_tr_b16 a[54:55], v33 offset:8704\n"
"s_waitcnt lgkmcnt(0)\n"
"ds_read_b64_tr_b16 a[56:57], v33 offset:16384\n"
"ds_read_b64_tr_b16 a[58:59], v33 offset:16896\n"
"ds_read_b64_tr_b16 a[60:61], v33 offset:24576\n"
"ds_read_b64_tr_b16 a[62:63], v33 offset:25088\n"
"s_waitcnt lgkmcnt(0)\n"
"s_waitcnt vmcnt(8)\n"
"s_barrier\n"
"s_mov_b32 s40, 2097152\n"
"s_add_u32 m0, %[sW1K], 49152\n"
"s_nop 0\n"
"buffer_load_dwordx4 v44, %[sWQ], s40 offen  lds\n"
"s_add_u32 m0, %[sW1K], 65536\n"
"s_nop 0\n"
"buffer_load_dwordx4 v44, %[sWK], s40 offen  lds\n"
"s_mov_b32 s40, 2228224\n"
"s_add_u32 m0, %[sW1K], 53248\n"
"s_nop 0\n"
"buffer_load_dwordx4 v44, %[sWQ], s40 offen  lds\n"
"s_add_u32 m0, %[sW1K], 69632\n"
"s_nop 0\n"
"buffer_load_dwordx4 v44, %[sWK], s40 offen  lds\n"
"s_mov_b32 s40, 2359296\n"
"s_add_u32 m0, %[sW1K], 57344\n"
"s_nop 0\n"
"buffer_load_dwordx4 v44, %[sWQ], s40 offen  lds\n"
"s_add_u32 m0, %[sW1K], 73728\n"
"s_nop 0\n"
"buffer_load_dwordx4 v44, %[sWK], s40 offen  lds\n"
"s_mov_b32 s40, 2490368\n"
"s_add_u32 m0, %[sW1K], 61440\n"
"s_nop 0\n"
"buffer_load_dwordx4 v44, %[sWQ], s40 offen  lds\n"
"s_add_u32 m0, %[sW1K], 77824\n"
"s_nop 0\n"
"buffer_load_dwordx4 v44, %[sWK], s40 offen  lds\n"
"s_add_u32 m0, %[sW1K], 81920\n"
"s_mov_b32 s40, 2097152\n"
"buffer_load_dwordx4 v45, %[sWV], s40 offen  lds\n"
"s_add_u32 m0, %[sW1K], 86016\n"
"s_mov_b32 s40, 2359296\n"
"buffer_load_dwordx4 v45, %[sWV], s40 offen  lds\n"
"s_add_u32 m0, %[sW1K], 90112\n"
"s_mov_b32 s40, 2621440\n"
"buffer_load_dwordx4 v45, %[sWV], s40 offen  lds\n"
"s_add_u32 m0, %[sW1K], 94208\n"
"s_mov_b32 s40, 2883584\n"
"buffer_load_dwordx4 v45, %[sWV], s40 offen  lds\n"
"v_add_u32 v32, 98304, v46\n"
"v_xor_b32 v33, 32, v32\n"
"v_add_u32 v34, 98304, v47\n"
"ds_read_b64_tr_b16 a[64:65], v32 offset:0\n"
"ds_read_b64_tr_b16 a[66:67], v32 offset:512\n"
"ds_read_b64_tr_b16 a[68:69], v32 offset:8192\n"
"ds_read_b64_tr_b16 a[70:71], v32 offset:8704\n"
"ds_read_b64_tr_b16 a[72:73], v32 offset:16384\n"
"ds_read_b64_tr_b16 a[74:75], v32 offset:16896\n"
"ds_read_b64_tr_b16 a[76:77], v32 offset:24576\n"
"ds_read_b64_tr_b16 a[78:79], v32 offset:25088\n"
"ds_read_b64_tr_b16 a[80:81], v33 offset:0\n"
"ds_read_b64_tr_b16 a[82:83], v33 offset:512\n"
"ds_read_b64_tr_b16 a[84:85], v33 offset:8192\n"
"ds_read_b64_tr_b16 a[86:87], v33 offset:8704\n"
"s_waitcnt lgkmcnt(0)\n"
"ds_read_b64_tr_b16 a[88:89], v33 offset:16384\n"
"ds_read_b64_tr_b16 a[90:91], v33 offset:16896\n"
"ds_read_b64_tr_b16 a[92:93], v33 offset:24576\n"
"ds_read_b64_tr_b16 a[94:95], v33 offset:25088\n"
"ds_read_b64_tr_b16 v[168:169], v34 offset:32768\n"
"ds_read_b64_tr_b16 v[170:171], v34 offset:33024\n"
"ds_read_b64_tr_b16 v[172:173], v34 offset:36864\n"
"ds_read_b64_tr_b16 v[174:175], v34 offset:37120\n"
"ds_read_b64_tr_b16 v[176:177], v34 offset:40960\n"
"ds_read_b64_tr_b16 v[178:179], v34 offset:41216\n"
"ds_read_b64_tr_b16 v[180:181], v34 offset:45056\n"
"ds_read_b64_tr_b16 v[182:183], v34 offset:45312\n"
"s_waitcnt lgkmcnt(0)\n"
"s_waitcnt vmcnt(12)\n"
"s_barrier\n"
"s_mov_b32 s40, 2621440\n"
"s_add_u32 m0, %[sW1K], 98304\n"
"s_nop 0\n"
"buffer_load_dwordx4 v44, %[sWQ], s40 offen  lds\n"
"s_add_u32 m0, %[sW1K], 114688\n"
"s_nop 0\n"
"buffer_load_dwordx4 v44, %[sWK], s40 offen  lds\n"
"s_mov_b32 s40, 2752512\n"
"s_add_u32 m0, %[sW1K], 102400\n"
"s_nop 0\n"
"buffer_load_dwordx4 v44, %[sWQ], s40 offen  lds\n"
"s_add_u32 m0, %[sW1K], 118784\n"
"s_nop 0\n"
"buffer_load_dwordx4 v44, %[sWK], s40 offen  lds\n"
"s_mov_b32 s40, 2883584\n"
"s_add_u32 m0, %[sW1K], 106496\n"
"s_nop 0\n"
"buffer_load_dwordx4 v44, %[sWQ], s40 offen  lds\n"
"s_add_u32 m0, %[sW1K], 122880\n"
"s_nop 0\n"
"buffer_load_dwordx4 v44, %[sWK], s40 offen  lds\n"
"s_mov_b32 s40, 3014656\n"
"s_add_u32 m0, %[sW1K], 110592\n"
"s_nop 0\n"
"buffer_load_dwordx4 v44, %[sWQ], s40 offen  lds\n"
"s_add_u32 m0, %[sW1K], 126976\n"
"s_nop 0\n"
"buffer_load_dwordx4 v44, %[sWK], s40 offen  lds\n"
"v_add_u32 v32, 0, v46\n"
"v_xor_b32 v33, 32, v32\n"
"v_add_u32 v34, 0, v47\n"
"ds_read_b64_tr_b16 a[96:97], v32 offset:0\n"
"ds_read_b64_tr_b16 a[98:99], v32 offset:512\n"
"ds_read_b64_tr_b16 a[100:101], v32 offset:8192\n"
"ds_read_b64_tr_b16 a[102:103], v32 offset:8704\n"
"ds_read_b64_tr_b16 a[104:105], v32 offset:16384\n"
"ds_read_b64_tr_b16 a[106:107], v32 offset:16896\n"
"ds_read_b64_tr_b16 a[108:109], v32 offset:24576\n"
"ds_read_b64_tr_b16 a[110:111], v32 offset:25088\n"
"ds_read_b64_tr_b16 a[112:113], v33 offset:0\n"
"ds_read_b64_tr_b16 a[114:115], v33 offset:512\n"
"ds_read_b64_tr_b16 a[116:117], v33 offset:8192\n"
"ds_read_b64_tr_b16 a[118:119], v33 offset:8704\n"
"s_waitcnt lgkmcnt(0)\n"
"ds_read_b64_tr_b16 a[120:121], v33 offset:16384\n"
"ds_read_b64_tr_b16 a[122:123], v33 offset:16896\n"
"ds_read_b64_tr_b16 a[124:125], v33 offset:24576\n"
"ds_read_b64_tr_b16 a[126:127], v33 offset:25088\n"
"s_waitcnt lgkmcnt(0)\n"
"s_waitcnt vmcnt(8)\n"
"s_barrier\n"
"s_mov_b32 s40, 3145728\n"
"s_add_u32 m0, %[sW1K], 0\n"
"s_nop 0\n"
"buffer_load_dwordx4 v44, %[sWQ], s40 offen  lds\n"
"s_add_u32 m0, %[sW1K], 16384\n"
"s_nop 0\n"
"buffer_load_dwordx4 v44, %[sWK], s40 offen  lds\n"
"s_mov_b32 s40, 3276800\n"
"s_add_u32 m0, %[sW1K], 4096\n"
"s_nop 0\n"
"buffer_load_dwordx4 v44, %[sWQ], s40 offen  lds\n"
"s_add_u32 m0, %[sW1K], 20480\n"
"s_nop 0\n"
"buffer_load_dwordx4 v44, %[sWK], s40 offen  lds\n"
"s_mov_b32 s40, 3407872\n"
"s_add_u32 m0, %[sW1K], 8192\n"
"s_nop 0\n"
"buffer_load_dwordx4 v44, %[sWQ], s40 offen  lds\n"
"s_add_u32 m0, %[sW1K], 24576\n"
"s_nop 0\n"
"buffer_load_dwordx4 v44, %[sWK], s40 offen  lds\n"
"s_mov_b32 s40, 3538944\n"
"s_add_u32 m0, %[sW1K], 12288\n"
"s_nop 0\n"
"buffer_load_dwordx4 v44, %[sWQ], s40 offen  lds\n"
"s_add_u32 m0, %[sW1K], 28672\n"
"s_nop 0\n"
"buffer_load_dwordx4 v44, %[sWK], s40 offen  lds\n"
"s_add_u32 m0, %[sW1K], 32768\n"
"s_mov_b32 s40, 3145728\n"
"buffer_load_dwordx4 v45, %[sWV], s40 offen  lds\n"
"s_add_u32 m0, %[sW1K], 36864\n"
"s_mov_b32 s40, 3407872\n"
"buffer_load_dwordx4 v45, %[sWV], s40 offen  lds\n"
"s_add_u32 m0, %[sW1K], 40960\n"
"s_mov_b32 s40, 3670016\n"
"buffer_load_dwordx4 v45, %[sWV], s40 offen  lds\n"
"s_add_u32 m0, %[sW1K], 45056\n"
"s_mov_b32 s40, 3932160\n"
"buffer_load_dwordx4 v45, %[sWV], s40 offen  lds\n"
"v_add_u32 v32, 49152, v46\n"
"v_xor_b32 v33, 32, v32\n"
"v_add_u32 v34, 49152, v47\n"
"ds_read_b64_tr_b16 a[128:129], v32 offset:0\n"
"ds_read_b64_tr_b16 a[130:131], v32 offset:512\n"
"ds_read_b64_tr_b16 a[132:133], v32 offset:8192\n"
"ds_read_b64_tr_b16 a[134:135], v32 offset:8704\n"
"ds_read_b64_tr_b16 a[136:137], v32 offset:16384\n"
"ds_read_b64_tr_b16 a[138:139], v32 offset:16896\n"
"ds_read_b64_tr_b16 a[140:141], v32 offset:24576\n"
"ds_read_b64_tr_b16 a[142:143], v32 offset:25088\n"
"ds_read_b64_tr_b16 a[144:145], v33 offset:0\n"
"ds_read_b64_tr_b16 a[146:147], v33 offset:512\n"
"ds_read_b64_tr_b16 a[148:149], v33 offset:8192\n"
"ds_read_b64_tr_b16 a[150:151], v33 offset:8704\n"
"s_waitcnt lgkmcnt(0)\n"
"ds_read_b64_tr_b16 a[152:153], v33 offset:16384\n"
"ds_read_b64_tr_b16 a[154:155], v33 offset:16896\n"
"ds_read_b64_tr_b16 a[156:157], v33 offset:24576\n"
"ds_read_b64_tr_b16 a[158:159], v33 offset:25088\n"
"ds_read_b64_tr_b16 v[184:185], v34 offset:32768\n"
"ds_read_b64_tr_b16 v[186:187], v34 offset:33024\n"
"ds_read_b64_tr_b16 v[188:189], v34 offset:36864\n"
"ds_read_b64_tr_b16 v[190:191], v34 offset:37120\n"
"ds_read_b64_tr_b16 v[192:193], v34 offset:40960\n"
"ds_read_b64_tr_b16 v[194:195], v34 offset:41216\n"
"ds_read_b64_tr_b16 v[196:197], v34 offset:45056\n"
"ds_read_b64_tr_b16 v[198:199], v34 offset:45312\n"
"s_waitcnt lgkmcnt(0)\n"
"s_waitcnt vmcnt(12)\n"
"s_barrier\n"
"buffer_load_dword v50, v49, s[32:35], %[sHB] offen sc1\n"
"s_mov_b32 s40, 3670016\n"
"s_add_u32 m0, %[sW1K], 49152\n"
"s_nop 0\n"
"buffer_load_dwordx4 v44, %[sWQ], s40 offen  lds\n"
"s_add_u32 m0, %[sW1K], 65536\n"
"s_nop 0\n"
"buffer_load_dwordx4 v44, %[sWK], s40 offen  lds\n"
"s_mov_b32 s40, 3801088\n"
"s_add_u32 m0, %[sW1K], 53248\n"
"s_nop 0\n"
"buffer_load_dwordx4 v44, %[sWQ], s40 offen  lds\n"
"s_add_u32 m0, %[sW1K], 69632\n"
"s_nop 0\n"
"buffer_load_dwordx4 v44, %[sWK], s40 offen  lds\n"
"s_mov_b32 s40, 3932160\n"
"s_add_u32 m0, %[sW1K], 57344\n"
"s_nop 0\n"
"buffer_load_dwordx4 v44, %[sWQ], s40 offen  lds\n"
"s_add_u32 m0, %[sW1K], 73728\n"
"s_nop 0\n"
"buffer_load_dwordx4 v44, %[sWK], s40 offen  lds\n"
"s_mov_b32 s40, 4063232\n"
"s_add_u32 m0, %[sW1K], 61440\n"
"s_nop 0\n"
"buffer_load_dwordx4 v44, %[sWQ], s40 offen  lds\n"
"s_add_u32 m0, %[sW1K], 77824\n"
"s_nop 0\n"
"buffer_load_dwordx4 v44, %[sWK], s40 offen  lds\n"
"v_add_u32 v32, 98304, v46\n"
"v_xor_b32 v33, 32, v32\n"
"v_add_u32 v34, 98304, v47\n"
"ds_read_b64_tr_b16 a[160:161], v32 offset:0\n"
"ds_read_b64_tr_b16 a[162:163], v32 offset:512\n"
"ds_read_b64_tr_b16 a[164:165], v32 offset:8192\n"
"ds_read_b64_tr_b16 a[166:167], v32 offset:8704\n"
"ds_read_b64_tr_b16 a[168:169], v32 offset:16384\n"
"ds_read_b64_tr_b16 a[170:171], v32 offset:16896\n"
"ds_read_b64_tr_b16 a[172:173], v32 offset:24576\n"
"ds_read_b64_tr_b16 a[174:175], v32 offset:25088\n"
"ds_read_b64_tr_b16 a[176:177], v33 offset:0\n"
"ds_read_b64_tr_b16 a[178:179], v33 offset:512\n"
"ds_read_b64_tr_b16 a[180:181], v33 offset:8192\n"
"ds_read_b64_tr_b16 a[182:183], v33 offset:8704\n"
"s_waitcnt lgkmcnt(0)\n"
"ds_read_b64_tr_b16 a[184:185], v33 offset:16384\n"
"ds_read_b64_tr_b16 a[186:187], v33 offset:16896\n"
"ds_read_b64_tr_b16 a[188:189], v33 offset:24576\n"
"ds_read_b64_tr_b16 a[190:191], v33 offset:25088\n"
"s_waitcnt lgkmcnt(0)\n"
"s_waitcnt vmcnt(8)\n"
"s_barrier\n"
"s_mov_b32 s40, 4194304\n"
"s_add_u32 m0, %[sW1K], 98304\n"
"s_nop 0\n"
"buffer_load_dwordx4 v44, %[sWQ], s40 offen  lds\n"
"s_add_u32 m0, %[sW1K], 114688\n"
"s_nop 0\n"
"buffer_load_dwordx4 v44, %[sWK], s40 offen  lds\n"
"s_mov_b32 s40, 4325376\n"
"s_add_u32 m0, %[sW1K], 102400\n"
"s_nop 0\n"
"buffer_load_dwordx4 v44, %[sWQ], s40 offen  lds\n"
"s_add_u32 m0, %[sW1K], 118784\n"
"s_nop 0\n"
"buffer_load_dwordx4 v44, %[sWK], s40 offen  lds\n"
"s_mov_b32 s40, 4456448\n"
"s_add_u32 m0, %[sW1K], 106496\n"
"s_nop 0\n"
"buffer_load_dwordx4 v44, %[sWQ], s40 offen  lds\n"
"s_add_u32 m0, %[sW1K], 122880\n"
"s_nop 0\n"
"buffer_load_dwordx4 v44, %[sWK], s40 offen  lds\n"
"s_mov_b32 s40, 4587520\n"
"s_add_u32 m0, %[sW1K], 110592\n"
"s_nop 0\n"
"buffer_load_dwordx4 v44, %[sWQ], s40 offen  lds\n"
"s_add_u32 m0, %[sW1K], 126976\n"
"s_nop 0\n"
"buffer_load_dwordx4 v44, %[sWK], s40 offen  lds\n"
"s_add_u32 m0, %[sW1K], 131072\n"
"s_mov_b32 s40, 4194304\n"
"buffer_load_dwordx4 v45, %[sWV], s40 offen  lds\n"
"s_add_u32 m0, %[sW1K], 135168\n"
"s_mov_b32 s40, 4456448\n"
"buffer_load_dwordx4 v45, %[sWV], s40 offen  lds\n"
"v_add_u32 v32, 0, v46\n"
"v_xor_b32 v33, 32, v32\n"
"v_add_u32 v34, 0, v47\n"
"ds_read_b64_tr_b16 a[192:193], v32 offset:0\n"
"ds_read_b64_tr_b16 a[194:195], v32 offset:512\n"
"ds_read_b64_tr_b16 a[196:197], v32 offset:8192\n"
"ds_read_b64_tr_b16 a[198:199], v32 offset:8704\n"
"ds_read_b64_tr_b16 a[200:201], v32 offset:16384\n"
"ds_read_b64_tr_b16 a[202:203], v32 offset:16896\n"
"ds_read_b64_tr_b16 a[204:205], v32 offset:24576\n"
"ds_read_b64_tr_b16 a[206:207], v32 offset:25088\n"
"ds_read_b64_tr_b16 a[208:209], v33 offset:0\n"
"ds_read_b64_tr_b16 a[210:211], v33 offset:512\n"
"ds_read_b64_tr_b16 a[212:213], v33 offset:8192\n"
"ds_read_b64_tr_b16 a[214:215], v33 offset:8704\n"
"s_waitcnt lgkmcnt(0)\n"
"ds_read_b64_tr_b16 a[216:217], v33 offset:16384\n"
"ds_read_b64_tr_b16 a[218:219], v33 offset:16896\n"
"ds_read_b64_tr_b16 a[220:221], v33 offset:24576\n"
"ds_read_b64_tr_b16 a[222:223], v33 offset:25088\n"
"ds_read_b64_tr_b16 v[200:201], v34 offset:32768\n"
"ds_read_b64_tr_b16 v[202:203], v34 offset:33024\n"
"ds_read_b64_tr_b16 v[204:205], v34 offset:36864\n"
"ds_read_b64_tr_b16 v[206:207], v34 offset:37120\n"
"ds_read_b64_tr_b16 v[208:209], v34 offset:40960\n"
"ds_read_b64_tr_b16 v[210:211], v34 offset:41216\n"
"ds_read_b64_tr_b16 v[212:213], v34 offset:45056\n"
"ds_read_b64_tr_b16 v[214:215], v34 offset:45312\n"
"s_waitcnt lgkmcnt(0)\n"
"s_waitcnt vmcnt(10)\n"
"s_barrier\n"
"s_mov_b32 s40, 1064960\n"
"buffer_load_dword v51, off, s[32:35], s40 sc1\n"
"v_add_u32 v32, 49152, v46\n"
"v_xor_b32 v33, 32, v32\n"
"v_add_u32 v34, 49152, v47\n"
"ds_read_b64_tr_b16 a[224:225], v32 offset:0\n"
"ds_read_b64_tr_b16 a[226:227], v32 offset:512\n"
"ds_read_b64_tr_b16 a[228:229], v32 offset:8192\n"
"ds_read_b64_tr_b16 a[230:231], v32 offset:8704\n"
"ds_read_b64_tr_b16 a[232:233], v32 offset:16384\n"
"ds_read_b64_tr_b16 a[234:235], v32 offset:16896\n"
"ds_read_b64_tr_b16 a[236:237], v32 offset:24576\n"
"ds_read_b64_tr_b16 a[238:239], v32 offset:25088\n"
"ds_read_b64_tr_b16 a[240:241], v33 offset:0\n"
"ds_read_b64_tr_b16 a[242:243], v33 offset:512\n"
"ds_read_b64_tr_b16 a[244:245], v33 offset:8192\n"
"ds_read_b64_tr_b16 a[246:247], v33 offset:8704\n"
"s_waitcnt lgkmcnt(0)\n"
"ds_read_b64_tr_b16 a[248:249], v33 offset:16384\n"
"ds_read_b64_tr_b16 a[250:251], v33 offset:16896\n"
"ds_read_b64_tr_b16 a[252:253], v33 offset:24576\n"
"ds_read_b64_tr_b16 a[254:255], v33 offset:25088\n"
"s_waitcnt lgkmcnt(0)\n"
"s_waitcnt vmcnt(0)\n"
"s_barrier\n"
"s_mov_b32 s40, 1073152\n"
"buffer_load_dword v22, %[vPQ], s[32:35], s40 offen sc1\n"
"v_add_u32 v32, 98304, v46\n"
"v_xor_b32 v33, 32, v32\n"
"v_add_u32 v34, 98304, v47\n"
"ds_read_b64_tr_b16 v[224:225], v32 offset:0\n"
"ds_read_b64_tr_b16 v[226:227], v32 offset:512\n"
"ds_read_b64_tr_b16 v[228:229], v32 offset:8192\n"
"ds_read_b64_tr_b16 v[230:231], v32 offset:8704\n"
"ds_read_b64_tr_b16 v[232:233], v32 offset:16384\n"
"ds_read_b64_tr_b16 v[234:235], v32 offset:16896\n"
"ds_read_b64_tr_b16 v[236:237], v32 offset:24576\n"
"ds_read_b64_tr_b16 v[238:239], v32 offset:25088\n"
"ds_read_b64_tr_b16 v[240:241], v33 offset:0\n"
"ds_read_b64_tr_b16 v[242:243], v33 offset:512\n"
"ds_read_b64_tr_b16 v[244:245], v33 offset:8192\n"
"ds_read_b64_tr_b16 v[246:247], v33 offset:8704\n"
"s_waitcnt lgkmcnt(0)\n"
"ds_read_b64_tr_b16 v[248:249], v33 offset:16384\n"
"ds_read_b64_tr_b16 v[250:251], v33 offset:16896\n"
"ds_read_b64_tr_b16 v[252:253], v33 offset:24576\n"
"ds_read_b64_tr_b16 v[254:255], v33 offset:25088\n"
"ds_read_b64_tr_b16 v[216:217], v34 offset:32768\n"
"ds_read_b64_tr_b16 v[218:219], v34 offset:33024\n"
"ds_read_b64_tr_b16 v[220:221], v34 offset:36864\n"
"ds_read_b64_tr_b16 v[222:223], v34 offset:37120\n"
"s_waitcnt lgkmcnt(0)\n"
"LX_w_end:\n"
"s_barrier\n"
"v_cmp_eq_u32 vcc, v50, v48\n"
"v_readfirstlane_b32 s42, v51\n"
"s_cbranch_vccnz LX_slowhs\n"
"s_cmp_ge_i32 s42, 0\n"
"s_cbranch_scc1 LX_hsok\n"
"LX_slowhs:\n"
"s_mov_b32 s41, 0\n"
"LX_hb:\n"
"buffer_load_dword v20, v49, s[32:35], %[sHB] offen sc1\n"
"s_add_u32 s41, s41, 1\n"
"s_cmp_gt_u32 s41, 60000\n"
"s_cbranch_scc1 LX_tmo\n"
"s_waitcnt vmcnt(0)\n"
"v_cmp_eq_u32 vcc, v20, v48\n"
"s_cbranch_vccnz LX_hb\n"
"v_mov_b32 v22, -1\n"
"s_mov_b32 s40, 1064960\n"
"v_mov_b32 v21, 0\n"
"s_mov_b64 exec, 1\n"
"s_mov_b32 s41, 0\n"
"LX_par:\n"
"buffer_load_dword v20, v21, s[32:35], s40 offen sc1\n"
"s_add_u32 s41, s41, 1\n"
"s_cmp_gt_u32 s41, 60000\n"
"s_cbranch_scc1 LX_tmo\n"
"s_waitcnt vmcnt(0)\n"
"v_cmp_gt_i32 vcc, 0, v20\n"
"s_cbranch_vccnz LX_par\n"
"LX_par_ok:\n"
"s_mov_b64 exec, -1\n"
"v_readfirstlane_b32 s42, v20\n"
"s_nop 3\n"
"LX_hsok:\n"
"v_mov_b32 v20, 0\n"
"s_mov_b32 s40, 1097728\n"
"buffer_store_dword v20, %[vXO], s[32:35], s40 offen sc1\n"
"s_mov_b32 s44, 0x3e0293ee\n"
"s_mov_b32 s45, 0x3e0293ee\n"
"v_mov_b32 v25, 0\n"
"v_mov_b32 v27, 0\n"
"s_mov_b32 s43, 0\n"
"v_mov_b32 v28, 0x3f803f80\n"
"v_mov_b32 v29, 0x3f803f80\n"
"v_mov_b32 v30, 0x3f803f80\n"
"v_mov_b32 v31, 0x3f803f80\n"
"s_mov_b32 s56, 0xfff0ffff\n"
"s_mov_b32 s57, 0xf000ff00\n"
"s_mov_b32 s58, 0xffe0fffe\n"
"s_mov_b32 s59, 0xe000fe00\n"
"s_mov_b32 s60, 0xffc0fffc\n"
"s_mov_b32 s61, 0xc000fc00\n"
"s_mov_b32 s62, 0xff80fff8\n"
"s_mov_b32 s63, 0x8000f800\n"
"s_and_b32 s55, s42, 8192\n"
"s_add_u32 s55, s55, 89165824\n"
"s_and_b32 s54, %[sHB], 0x180\n"
"s_lshr_b32 s40, s54, 6\n"
"s_add_u32 s54, s54, s55\n"
"v_bfe_u32 v20, %[vL16], 7, 1\n"
"v_lshl_add_u32 v20, v20, 1, 24\n"
"v_bfe_u32 v20, s42, v20, 2\n"
"v_lshl_add_u32 %[vOST], v20, 13, %[vOST]\n"
"s_add_u32 s40, s40, 16\n"
"s_lshr_b32 s42, s42, s40\n"
"s_and_b32 s42, s42, 3\n"
"s_lshl_b32 s42, s42, 13\n"
"s_add_u32 s42, s42, %[sREP]\n"
"s_cmp_lt_u32 %[sW1K], 2048\n"
"s_cselect_b32 s39, 0xf149f2ca, 0\n"
"s_mov_b32 s64, 1073152\n"
"s_mov_b32 s65, 1097728\n"
"s_mov_b32 s84, 1600\n"
"s_mov_b32 s85, 0\n"
"s_mov_b32 s86, -10\n"
"s_mov_b32 s36, 130\n"
"s_mov_b32 s37, 224\n"
"s_waitcnt vmcnt(1)\n"
"s_mov_b64 exec, -1\n"
"s_mov_b32 s41, 0\n"
"v_cmp_gt_i32 vcc, 1, v22\n"
"s_cbranch_vccz LX_pq0_ok\n"
"s_branch LX_pq0_go\n"
"LX_pq0:\n"
"s_sleep 1\n"
"LX_pq0_go:\n"
"buffer_load_dword v22, %[vPQ], s[32:35], s64 offen sc1\n"
"s_add_u32 s41, s41, 1\n"
"s_cmp_gt_u32 s41, 60000\n"
"s_cbranch_scc1 LX_tmo\n"
"s_waitcnt vmcnt(0)\n"
"v_cmp_gt_i32 vcc, 1, v22\n"
"s_cbranch_vccnz LX_pq0\n"
"LX_pq0_ok:\n"
"s_mov_b64 exec, -1\n"
"s_mov_b32 s52, %[sQOFF]\n"
"s_mov_b32 s53, 0\n"
"s_add_u32 s31, %[sW1K], s53\n"
"s_add_u32 s31, s31, 16384\n"
"s_add_u32 s30, s52, %[sVOFF]\n"
"s_lshl2_add_u32 s29, %[sW1K], s53\n"
"s_mov_b32 m0, s29\n"
"s_nop 0\n"
"buffer_load_dwordx4 %[vL16], s[32:35], s52 offen offset:0 sc1 lds\n"
"buffer_load_dwordx4 %[vL16], s[32:35], s52 offen offset:1024 sc1 lds\n"
"buffer_load_dwordx4 %[vL16], s[32:35], s52 offen offset:2048 sc1 lds\n"
"buffer_load_dwordx4 %[vL16], s[32:35], s52 offen offset:3072 sc1 lds\n"
"s_mov_b32 m0, s31\n"
"s_nop 0\n"
"buffer_load_dwordx4 %[vL16], s[32:35], s30 offen sc1 lds\n"
"v_mov_b32 v23, %[vL16]\n"
"s_and_b32 s40, s43, 3\n"
"s_lshl_b32 s49, s40, 17\n"
"s_lshl_b32 s46, s40, 16\n"
"s_add_u32 s46, s46, s42\n"
"s_add_u32 s47, s46, 4096\n"
"s_add_u32 s48, s46, 32768\n"
"s_xor_b32 s50, s49, 262144\n"
"s_add_u32 s51, s43, 1\n"
"s_mov_b32 s38, 0\n"
"s_add_u32 s52, s52, 786432\n"
"s_xor_b32 s53, s53, 20480\n"
"s_sub_u32 s40, s43, 1\n"
"s_max_i32 s40, s40, 0\n"
"v_mov_b32 v18, s40\n"
"v_writelane_b32 v18, s84, 1\n"
"s_add_u32 s40, s43, 2\n"
"s_min_u32 s87, s40, 64\n"
"s_add_u32 s31, %[sW1K], s53\n"
"s_add_u32 s31, s31, 16384\n"
"s_add_u32 s30, s52, %[sVOFF]\n"
"s_lshl2_add_u32 s29, %[sW1K], s53\n"
"s_mov_b32 s41, 0\n"
"s_mov_b64 exec, 1\n"
"LX_ts0:\n"
"buffer_load_dwordx2 v[12:13], off, s[32:35], s54 sc1\n"
"s_add_u32 s41, s41, 1\n"
"s_cmp_gt_u32 s41, 60000\n"
"s_cbranch_scc1 LX_tmo\n"
"s_waitcnt vmcnt(0)\n"
"v_cmp_gt_i32 vcc, s43, v12\n"
"s_cbranch_vccnz LX_ts0\n"
"v_readfirstlane_b32 s92, v13\n"
"v_readfirstlane_b32 s93, v12\n"
"s_mov_b64 exec, -1\n"
"s_nop 1\n"
"s_cmp_eq_u32 s93, s43\n"
"s_cselect_b32 s93, 0, 0x10000\n"
"s_sub_u32 s92, s92, s93\n"
"s_barrier\n"
"ds_read_b128 v[64:67], v23 offset:8192\n"
"ds_read_b128 v[68:71], v23 offset:9216\n"
"ds_read_b128 v[72:75], v23 offset:10240\n"
"ds_read_b128 v[76:79], v23 offset:11264\n"
"ds_read_b128 v[80:83], v23 offset:12288\n"
"ds_read_b128 v[84:87], v23 offset:13312\n"
"ds_read_b128 v[88:91], v23 offset:14336\n"
"ds_read_b128 v[92:95], v23 offset:15360\n"
"v_add_u32 v20, %[sW1K], v23\n"
"ds_read_b128 v[60:63], v20 offset:16384\n"
"v_mov_b64 v[96:97], 0\n"
"v_mov_b64 v[98:99], 0\n"
"v_mov_b64 v[100:101], 0\n"
"v_mov_b64 v[102:103], 0\n"
"v_mov_b64 v[104:105], 0\n"
"v_mov_b64 v[106:107], 0\n"
"v_mov_b64 v[108:109], 0\n"
"v_mov_b64 v[110:111], 0\n"
"v_mov_b64 v[112:113], 0\n"
"v_mov_b64 v[114:115], 0\n"
"v_mov_b32 v20, 0\n"
"LX_w0:\n"
"s_memrealtime s[90:91]\n"
"s_waitcnt lgkmcnt(0)\n"
"s_sub_u32 s40, s92, s90\n"
"s_sub_u32 s40, s40, 24\n"
"s_cmp_lt_u32 s40, 2000\n"
"s_cbranch_scc1 LX_w0\n"
"buffer_load_dword v22, %[vPQ], s[32:35], s64 offen sc1\n"
"s_branch LX_fast\n"
"LX_late:\n"
"s_add_u32 s92, s90, 8\n"
"s_add_u32 s40, s92, s37\n"
"v_mov_b32 v13, s40\n"
"s_add_u32 s40, s92, s36\n"
"s_mov_b64 exec, 0xf0\n"
"v_mov_b32 v13, s40\n"
"s_mov_b64 exec, -1\n"
"v_mov_b32 v12, s51\n"
"s_mov_b32 s41, 1\n"
"s_branch LX_load\n"
".p2align 8\n"
"LX_step:\n"
"s_mov_b32 s41, 0\n"
"s_mov_b64 exec, 1\n"
"LX_ts:\n"
"buffer_load_dwordx2 v[12:13], off, s[32:35], s54 sc1\n"
"s_add_u32 s41, s41, 1\n"
"s_cmp_gt_u32 s41, 60000\n"
"s_cbranch_scc1 LX_tmo\n"
"s_waitcnt vmcnt(0)\n"
"v_cmp_gt_i32 vcc, s43, v12\n"
"s_cbranch_vccnz LX_ts\n"
"v_readfirstlane_b32 s92, v13\n"
"v_readfirstlane_b32 s93, v12\n"
"s_mov_b64 exec, -1\n"
"s_nop 1\n"
"s_cmp_eq_u32 s93, s43\n"
"s_cselect_b32 s93, 0, 0x10000\n"
"s_sub_u32 s92, s92, s93\n"
"LX_fast:\n"
"s_add_u32 s92, s92, %[sROT]\n"
"s_add_u32 s40, s92, s37\n"
"v_mov_b32 v13, s40\n"
"s_add_u32 s40, s92, s36\n"
"s_mov_b64 exec, 0xf0\n"
"v_mov_b32 v13, s40\n"
"s_mov_b64 exec, -1\n"
"v_mov_b32 v12, s51\n"
"LX_tw:\n"
"s_memrealtime s[90:91]\n"
"s_waitcnt lgkmcnt(0)\n"
"s_sub_u32 s40, s92, s90\n"
"s_cmp_gt_u32 s40, 2000\n"
"s_cbranch_scc1 LX_late\n"
"s_cmp_gt_u32 s40, 20\n"
"s_cbranch_scc1 LX_tw\n"
"s_sub_u32 s40, s40, 5\n"
"s_cmp_lt_i32 s40, 8\n"
"s_cbranch_scc1 LX_tf0\n"
"s_memrealtime s[94:95]\n"
"s_nop 4\n"
"s_memrealtime s[96:97]\n"
"s_nop 4\n"
"s_memrealtime s[98:99]\n"
"s_nop 4\n"
"s_memrealtime s[90:91]\n"
"s_lshl_b32 s40, s92, 2\n"
"s_waitcnt lgkmcnt(0)\n"
"s_sub_u32 s40, s40, s94\n"
"s_sub_u32 s40, s40, s96\n"
"s_sub_u32 s40, s40, s98\n"
"s_sub_u32 s40, s40, s90\n"
"s_ashr_i32 s40, s40, 2\n"
"s_sub_u32 s40, s40, 7\n"
"LX_tf0:\n"
"s_max_i32 s40, s40, 0\n"
"LX_tf:\n"
"s_sub_u32 s40, s40, 1\n"
"s_cbranch_scc0 LX_tf\n"
"LX_go:\n"
"s_mov_b32 s41, 0\n"
"LX_load:\n"
"buffer_load_dwordx4 v[116:119], %[vL16], s[32:35], s46 offen offset:0 sc1\n"
"s_nop 8\n"
"LX_load1:\n"
"buffer_load_dwordx4 v[120:123], %[vL16], s[32:35], s46 offen offset:1024 sc1\n"
"s_nop 8\n"
"LX_load2:\n"
"buffer_load_dwordx4 v[124:127], %[vL16], s[32:35], s46 offen offset:2048 sc1\n"
"s_nop 8\n"
"LX_load3:\n"
"buffer_load_dwordx4 v[128:131], %[vL16], s[32:35], s46 offen offset:3072 sc1\n"
"s_nop 8\n"
"LX_load4:\n"
"buffer_load_dwordx4 v[132:135], %[vL16], s[32:35], s47 offen offset:0 sc1\n"
"s_nop 8\n"
"LX_load5:\n"
"buffer_load_dwordx4 v[136:139], %[vL16], s[32:35], s47 offen offset:1024 sc1\n"
"s_nop 8\n"
"LX_load6:\n"
"buffer_load_dwordx4 v[140:143], %[vL16], s[32:35], s47 offen offset:2048 sc1\n"
"s_nop 8\n"
"LX_load7:\n"
"buffer_load_dwordx4 v[144:147], %[vL16], s[32:35], s47 offen offset:3072 sc1\n"
"s_nop 8\n"
"LX_load8:\n"
"buffer_load_dwordx4 v[148:151], %[vL16], s[32:35], s48 offen offset:0 sc1\n"
"LX_stream:\n"
"s_mov_b32 m0, s29\n"
"s_waitcnt vmcnt(8)\n"
"v_smfmac_f32_16x16x64_bf16 v[96:99], v[116:119], a[0:7], %[vIDX]\n"
"v_smfmac_f32_16x16x64_bf16 v[104:107], v[116:119], a[16:23], %[vIDX]\n"
"v_smfmac_f32_16x16x64_bf16 v[100:103], v[116:119], a[8:15], %[vIDX]\n"
"v_smfmac_f32_16x16x64_bf16 v[108:111], v[116:119], a[24:31], %[vIDX]\n"
"v_smfmac_f32_16x16x64_bf16 v[112:115], v[116:119], v[152:159], %[vIDX]\n"
"s_waitcnt vmcnt(7)\n"
"v_smfmac_f32_16x16x64_bf16 v[96:99], v[120:123], a[32:39], %[vIDX]\n"
"v_smfmac_f32_16x16x64_bf16 v[104:107], v[120:123], a[48:55], %[vIDX]\n"
"v_smfmac_f32_16x16x64_bf16 v[100:103], v[120:123], a[40:47], %[vIDX]\n"
"v_smfmac_f32_16x16x64_bf16 v[108:111], v[120:123], a[56:63], %[vIDX]\n"
"s_waitcnt vmcnt(6)\n"
"v_smfmac_f32_16x16x64_bf16 v[96:99], v[124:127], a[64:71], %[vIDX]\n"
"v_max3_u32 v20, v20, v116, v117\n"
"v_smfmac_f32_16x16x64_bf16 v[104:107], v[124:127], a[80:87], %[vIDX]\n"
"v_max3_u32 v20, v20, v118, v119\n"
"v_smfmac_f32_16x16x64_bf16 v[100:103], v[124:127], a[72:79], %[vIDX]\n"
"v_max3_u32 v20, v20, v120, v121\n"
"v_smfmac_f32_16x16x64_bf16 v[108:111], v[124:127], a[88:95], %[vIDX]\n"
"s_waitcnt vmcnt(5)\n"
"v_max3_u32 v20, v20, v122, v123\n"
"v_smfmac_f32_16x16x64_bf16 v[96:99], v[128:131], a[96:103], %[vIDX]\n"
"v_cmp_eq_u32 vcc, -1, v20\n"
"v_smfmac_f32_16x16x64_bf16 v[104:107], v[128:131], a[112:119], %[vIDX]\n"
"v_max3_u32 v20, v20, v124, v125\n"
"v_smfmac_f32_16x16x64_bf16 v[100:103], v[128:131], a[104:111], %[vIDX]\n"
"s_cbranch_vccnz LX_again0\n"
"v_max3_u32 v20, v20, v126, v127\n"
"v_smfmac_f32_16x16x64_bf16 v[108:111], v[128:131], a[120:127], %[vIDX]\n"
"buffer_store_dwordx2 v[12:13], %[vTS], s[32:35], s55 offen sc1\n"
"s_waitcnt vmcnt(5)\n"
"v_max3_u32 v20, v20, v128, v129\n"
"v_smfmac_f32_16x16x64_bf16 v[96:99], v[132:135], a[128:135], %[vIDX]\n"
"v_max3_u32 v20, v20, v130, v131\n"
"v_smfmac_f32_16x16x64_bf16 v[104:107], v[132:135], a[144:151], %[vIDX]\n"
"v_max3_u32 v20, v20, v132, v133\n"
"v_smfmac_f32_16x16x64_bf16 v[100:103], v[132:135], a[136:143], %[vIDX]\n"
"v_max3_u32 v20, v20, v134, v135\n"
"v_smfmac_f32_16x16x64_bf16 v[108:111], v[132:135], a[152:159], %[vIDX]\n"
"s_waitcnt vmcnt(4)\n"
"v_smfmac_f32_16x16x64_bf16 v[96:99], v[136:139], a[160:167], %[vIDX]\n"
"v_max3_u32 v20, v20, v136, v137\n"
"v_smfmac_f32_16x16x64_bf16 v[104:107], v[136:139], a[176:183], %[vIDX]\n"
"v_max3_u32 v20, v20, v138, v139\n"
"v_smfmac_f32_16x16x64_bf16 v[100:103], v[136:139], a[168:175], %[vIDX]\n"
"v_smfmac_f32_16x16x64_bf16 v[108:111], v[136:139], a[184:191], %[vIDX]\n"
"buffer_load_dwordx4 %[vL16], s[32:35], s52 offen offset:0 sc1 lds\n"
"s_waitcnt vmcnt(4)\n"
"v_smfmac_f32_16x16x64_bf16 v[96:99], v[140:143], a[192:199], %[vIDX]\n"
"v_max3_u32 v20, v20, v140, v141\n"
"v_smfmac_f32_16x16x64_bf16 v[104:107], v[140:143], a[208:215], %[vIDX]\n"
"v_max3_u32 v20, v20, v142, v143\n"
"v_smfmac_f32_16x16x64_bf16 v[100:103], v[140:143], a[200:207], %[vIDX]\n"
"v_smfmac_f32_16x16x64_bf16 v[108:111], v[140:143], a[216:223], %[vIDX]\n"
"buffer_load_dwordx4 %[vL16], s[32:35], s52 offen offset:1024 sc1 lds\n"
"s_waitcnt vmcnt(4)\n"
"v_smfmac_f32_16x16x64_bf16 v[96:99], v[144:147], a[224:231], %[vIDX]\n"
"v_max3_u32 v20, v20, v144, v145\n"
"v_smfmac_f32_16x16x64_bf16 v[104:107], v[144:147], a[240:247], %[vIDX]\n"
"v_max3_u32 v20, v20, v146, v147\n"
"v_smfmac_f32_16x16x64_bf16 v[100:103], v[144:147], a[232:239], %[vIDX]\n"
"v_smfmac_f32_16x16x64_bf16 v[108:111], v[144:147], a[248:255], %[vIDX]\n"
"buffer_load_dwordx4 %[vL16], s[32:35], s52 offen offset:2048 sc1 lds\n"
"s_waitcnt vmcnt(4)\n"
"v_smfmac_f32_16x16x64_bf16 v[96:99], v[148:151], v[224:231], %[vIDX]\n"
"v_max3_u32 v20, v20, v148, v149\n"
"v_smfmac_f32_16x16x64_bf16 v[104:107], v[148:151], v[240:247], %[vIDX]\n"
"s_nop 1\n"
"v_max3_u32 v20, v20, v150, v151\n"
"v_smfmac_f32_16x16x64_bf16 v[100:103], v[148:151], v[232:239], %[vIDX]\n"
"s_nop 1\n"
"v_cmp_eq_u32 vcc, -1, v20\n"
"v_smfmac_f32_16x16x64_bf16 v[108:111], v[148:151], v[248:255], %[vIDX]\n"
"buffer_load_dwordx4 %[vL16], s[32:35], s52 offen offset:3072 sc1 lds\n"
"s_mov_b32 m0, s31\n"
"v_pk_add_f32 v[96:97], v[96:97], v[98:99]\n"
"v_pk_add_f32 v[104:105], v[104:105], v[106:107]\n"
"v_cvt_pk_bf16_f32 v98, v96, v104\n"
"v_cvt_pk_bf16_f32 v99, v97, v105\n"
"s_cbranch_vccnz LX_again2\n"
"v_pk_add_f32 v[100:101], v[100:101], v[102:103]\n"
"ds_write2_b32 %[vXW], v98, v99 offset0:8 offset1:12\n"
"v_pk_add_f32 v[108:109], v[108:109], v[110:111]\n"
"v_cvt_pk_bf16_f32 v102, v100, v108\n"
"v_cvt_pk_bf16_f32 v103, v101, v109\n"
"ds_write2_b32 %[vXW], v102, v103 offset0:0 offset1:4\n"
"buffer_load_dwordx4 %[vL16], s[32:35], s30 offen sc1 lds\n"
"v_smfmac_f32_16x16x64_bf16 v[112:115], v[120:123], v[160:167], %[vIDX]\n"
"v_smfmac_f32_16x16x64_bf16 v[112:115], v[124:127], v[168:175], %[vIDX]\n"
"v_smfmac_f32_16x16x64_bf16 v[112:115], v[128:131], v[176:183], %[vIDX]\n"
"v_smfmac_f32_16x16x64_bf16 v[112:115], v[132:135], v[184:191], %[vIDX]\n"
"v_smfmac_f32_16x16x64_bf16 v[112:115], v[136:139], v[192:199], %[vIDX]\n"
"s_waitcnt lgkmcnt(0)\n"
"s_barrier\n"
"ds_read_b128 v[44:47], %[vXR] offset:0\n"
"ds_read_b128 v[48:51], %[vXR] offset:1056\n"
"ds_read_b128 v[52:55], %[vXR] offset:2112\n"
"ds_read_b128 v[56:59], %[vXR] offset:3168\n"
"v_smfmac_f32_16x16x64_bf16 v[112:115], v[140:143], v[200:207], %[vIDX]\n"
"v_smfmac_f32_16x16x64_bf16 v[112:115], v[144:147], v[208:215], %[vIDX]\n"
"s_cmp_eq_u32 s43, 63\n"
"v_smfmac_f32_16x16x64_bf16 v[112:115], v[148:151], v[216:223], %[vIDX]\n"
"s_cbranch_scc1 LX_lastm\n"
"s_waitcnt lgkmcnt(3)\n"
"v_mfma_f32_16x16x32_bf16 v[32:35], v[44:47], v[44:47], 0\n"
"s_waitcnt lgkmcnt(2)\n"
"v_mfma_f32_16x16x32_bf16 v[32:35], v[48:51], v[48:51], v[32:35]\n"
"s_waitcnt lgkmcnt(1)\n"
"v_mfma_f32_16x16x32_bf16 v[32:35], v[52:55], v[52:55], v[32:35]\n"
"s_waitcnt lgkmcnt(0)\n"
"v_mfma_f32_16x16x32_bf16 v[32:35], v[56:59], v[56:59], v[32:35]\n"
"v_mfma_f32_16x16x32_bf16 v[36:39], v[64:67], v[44:47], 0\n"
"v_mfma_f32_16x16x32_bf16 v[36:39], v[68:71], v[48:51], v[36:39]\n"
"v_pk_add_f32 v[112:113], v[112:113], v[114:115]\n"
"v_mfma_f32_16x16x32_bf16 v[36:39], v[72:75], v[52:55], v[36:39]\n"
"v_cvt_pk_bf16_f32 v24, v112, v113\n"
"v_mfma_f32_16x16x32_bf16 v[36:39], v[76:79], v[56:59], v[36:39]\n"
"v_mfma_f32_16x16x32_bf16 v[40:43], v[80:83], v[44:47], 0\n"
"s_nop 1\n"
"v_mfma_f32_16x16x32_bf16 v[40:43], v[84:87], v[48:51], v[40:43]\n"
"s_nop 1\n"
"v_mfma_f32_16x16x32_bf16 v[40:43], v[88:91], v[52:55], v[40:43]\n"
"s_nop 1\n"
"v_mfma_f32_16x16x32_bf16 v[40:43], v[92:95], v[56:59], v[40:43]\n"
"v_max3_f32 v96, v32, v33, v36\n"
"v_max3_f32 v96, v96, v37, v38\n"
"v_max_f32 v96, v96, v39\n"
"s_nop 2\n"
"v_mov_b32 v21, v21\n"
"s_nop 0\n"
"v_max3_f32 v96, v96, v40, v41\n"
"v_max3_f32 v97, v96, v42, v43\n"
"v_max3_f32 v96, v96, v42, v43\n"
"s_nop 1\n"
"v_permlane32_swap_b32 v96, v97\n"
"v_max_f32 v98, v96, v97\n"
"v_max_f32 v99, v96, v97\n"
"s_nop 1\n"
"v_permlane16_swap_b32 v98, v99\n"
"v_max_f32 v96, v98, v99\n"
"v_mul_f32_e64 v96, v96, -s44\n"
"v_pk_fma_f32 v[32:33], v[32:33], s[44:45], v[96:97] op_sel_hi:[1,1,0]\n"
"v_pk_fma_f32 v[36:37], v[36:37], s[44:45], v[96:97] op_sel_hi:[1,1,0]\n"
"v_exp_f32 v32, v32\n"
"v_exp_f32 v33, v33\n"
"v_exp_f32 v36, v36\n"
"v_cvt_pk_bf16_f32 v26, v32, v33\n"
"v_exp_f32 v37, v37\n"
"v_pk_fma_f32 v[38:39], v[38:39], s[44:45], v[96:97] op_sel_hi:[1,1,0]\n"
"v_mfma_f32_16x16x16_bf16 v[104:107], v[28:29], v[26:27], 0\n"
"v_exp_f32 v38, v38\n"
"v_exp_f32 v39, v39\n"
"v_mfma_f32_16x16x16_bf16 v[108:111], v[24:25], v[26:27], 0\n"
"v_pk_fma_f32 v[40:41], v[40:41], s[44:45], v[96:97] op_sel_hi:[1,1,0]\n"
"v_pk_fma_f32 v[42:43], v[42:43], s[44:45], v[96:97] op_sel_hi:[1,1,0]\n"
"v_cvt_pk_bf16_f32 v100, v36, v37\n"
"v_exp_f32 v40, v40\n"
"v_exp_f32 v41, v41\n"
"v_cvt_pk_bf16_f32 v101, v38, v39\n"
"v_exp_f32 v42, v42\n"
"v_exp_f32 v43, v43\n"
"v_cvt_pk_bf16_f32 v102, v40, v41\n"
"v_cvt_pk_bf16_f32 v103, v42, v43\n"
"s_nop 1\n"
"v_mfma_f32_16x16x32_bf16 v[104:107], v[28:31], v[100:103], v[104:107]\n"
"v_mfma_f32_16x16x32_bf16 v[108:111], v[60:63], v[100:103], v[108:111]\n"
"s_nop 2\n"
"v_mov_b32 v21, v21\n"
"s_nop 2\n"
"v_rcp_f32 v96, v104\n"
"s_nop 0\n"
"v_pk_mul_f32 v[108:109], v[108:109], v[96:97] op_sel_hi:[1,0]\n"
"v_pk_mul_f32 v[110:111], v[110:111], v[96:97] op_sel_hi:[1,0]\n"
"v_cvt_pk_bf16_f32 v34, v108, v109\n"
"v_cvt_pk_bf16_f32 v35, v110, v111\n"
"buffer_store_dwordx2 v[34:35], %[vOST], s[32:35], s49 offen sc1\n"
"LX_idle:\n"
"s_memrealtime s[98:99]\n"
"s_and_b32 s40, %[sW1K], 2048\n"
"s_lshl_b32 s40, s40, 1\n"
"v_add_u32 v36, s40, v23\n"
"s_and_b32 s40, %[sW1K], 1024\n"
"ds_read_b128 v[116:119], v36 offset:0\n"
"ds_read_b128 v[120:123], v36 offset:1024\n"
"ds_read_b128 v[124:127], v36 offset:2048\n"
"ds_read_b128 v[128:131], v36 offset:3072\n"
"v_lshl_add_u32 v37, s40, 1, v23\n"
"v_add_u32 v38, %[sW1K], %[vL16]\n"
"v_lshl_add_u32 v43, s40, 1, %[vL16]\n"
"ds_read_b128 v[132:135], v37 offset:16384\n"
"ds_read_b128 v[136:139], v37 offset:17408\n"
"v_lshrrev_b32 v38, 2, v38\n"
"v_lshrrev_b32 v43, 2, v43\n"
"ds_write_b32 v38, v24 offset:40960\n"
"v_mov_b32 v20, 0xf149f2ca\n"
"v_mov_b32 v39, s39\n"
"s_waitcnt lgkmcnt(0)\n"
"v_mfma_f32_16x16x32_bf16 v[96:99], v[44:47], v[116:119], 0\n"
"v_cndmask_b32 v144, v39, 0, s[56:57]\n"
"v_cndmask_b32 v145, v39, 0, s[58:59]\n"
"buffer_load_dwordx2 v[12:13], off, s[32:35], s54 sc1\n"
"v_mfma_f32_16x16x32_bf16 v[96:99], v[48:51], v[120:123], v[96:99]\n"
"v_cndmask_b32 v146, v39, 0, s[60:61]\n"
"v_cndmask_b32 v147, v39, 0, s[62:63]\n"
"s_mov_b32 s40, 1144832\n"
"buffer_load_dword v42, %[vTS], s[32:35], s40 offen sc1\n"
"v_mfma_f32_16x16x32_bf16 v[96:99], v[52:55], v[124:127], v[96:99]\n"
"v_cndmask_b32 v148, v20, v39, s[56:57]\n"
"v_cndmask_b32 v149, v20, v39, s[58:59]\n"
"v_add_u32 v23, s53, %[vL16]\n"
"v_mfma_f32_16x16x32_bf16 v[96:99], v[56:59], v[128:131], v[96:99]\n"
"v_cndmask_b32 v150, v20, v39, s[60:61]\n"
"v_cndmask_b32 v151, v20, v39, s[62:63]\n"
"v_cmp_gt_i32 vcc, s87, v22\n"
"v_mfma_f32_16x16x32_bf16 v[100:103], v[64:67], v[116:119], v[144:147]\n"
"v_mov_b32 v141, 0\n"
"v_mov_b32 v143, 0\n"
"v_mov_b32 v41, 0\n"
"v_mfma_f32_16x16x32_bf16 v[100:103], v[68:71], v[120:123], v[100:103]\n"
"s_cmp_eq_u32 s43, 0\n"
"s_cselect_b32 s41, 1, s41\n"
"s_sub_u32 s96, s98, s92\n"
"s_lshl_b32 s96, s96, 4\n"
"v_mfma_f32_16x16x32_bf16 v[100:103], v[72:75], v[124:127], v[100:103]\n"
"s_sub_u32 s96, s96, s84\n"
"s_min_i32 s96, s96, 96\n"
"s_max_i32 s96, s96, -256\n"
"v_mfma_f32_16x16x32_bf16 v[100:103], v[76:79], v[128:131], v[100:103]\n"
"s_add_u32 s97, s96, 64\n"
"s_cmp_le_u32 s97, 64\n"
"s_cselect_b32 s97, 4, 1\n"
"s_ashr_i32 s96, s96, s97\n"
"v_mfma_f32_16x16x32_bf16 v[104:107], v[80:83], v[116:119], v[148:151]\n"
"s_cmp_eq_u32 s41, 0\n"
"s_cselect_b32 s96, s96, 0\n"
"s_add_u32 s84, s84, s96\n"
"s_add_u32 s96, s85, 15\n"
"v_mfma_f32_16x16x32_bf16 v[104:107], v[84:87], v[120:123], v[104:107]\n"
"s_lshr_b32 s96, s96, 4\n"
"s_sub_u32 s85, s85, s96\n"
"s_max_i32 s85, s85, 0\n"
"v_mfma_f32_16x16x32_bf16 v[104:107], v[88:91], v[124:127], v[104:107]\n"
"s_lshl_b32 s38, s43, 18\n"
"s_add_u32 s43, s43, 1\n"
"s_and_b32 s40, s43, 3\n"
"v_mfma_f32_16x16x32_bf16 v[104:107], v[92:95], v[128:131], v[104:107]\n"
"s_lshl_b32 s49, s40, 17\n"
"s_lshl_b32 s46, s40, 16\n"
"s_add_u32 s46, s46, s42\n"
"s_add_u32 s47, s46, 4096\n"
"s_add_u32 s48, s46, 32768\n"
"s_xor_b32 s50, s49, 262144\n"
"s_add_u32 s51, s43, 1\n"
"s_add_u32 s52, s52, 786432\n"
"s_xor_b32 s53, s53, 20480\n"
"v_max3_f32 v38, v96, v97, v100\n"
"v_max3_f32 v38, v38, v101, v102\n"
"v_max3_f32 v38, v38, v103, v104\n"
"v_max3_f32 v38, v38, v105, v106\n"
"v_max_f32 v38, v38, v107\n"
"v_mov_b32 v39, v38\n"
"s_sub_u32 s40, s43, 1\n"
"s_max_i32 s40, s40, 0\n"
"v_permlane32_swap_b32 v38, v39\n"
"v_max_f32 v38, v38, v39\n"
"v_mov_b32 v39, v38\n"
"v_mov_b32 v18, s40\n"
"v_writelane_b32 v18, s84, 1\n"
"v_permlane16_swap_b32 v38, v39\n"
"v_max_f32 v38, v38, v39\n"
"v_mul_f32_e64 v38, v38, -s44\n"
"v_pk_fma_f32 v[96:97], v[96:97], s[44:45], v[38:39] op_sel_hi:[1,1,0]\n"
"v_pk_fma_f32 v[100:101], v[100:101], s[44:45], v[38:39] op_sel_hi:[1,1,0]\n"
"v_pk_fma_f32 v[102:103], v[102:103], s[44:45], v[38:39] op_sel_hi:[1,1,0]\n"
"v_pk_fma_f32 v[104:105], v[104:105], s[44:45], v[38:39] op_sel_hi:[1,1,0]\n"
"v_pk_fma_f32 v[106:107], v[106:107], s[44:45], v[38:39] op_sel_hi:[1,1,0]\n"
"v_exp_f32 v96, v96\n"
"s_add_u32 s40, s43, 2\n"
"v_exp_f32 v97, v97\n"
"s_min_u32 s87, s40, 64\n"
"v_exp_f32 v100, v100\n"
"s_add_u32 s31, %[sW1K], s53\n"
"v_exp_f32 v101, v101\n"
"s_add_u32 s31, s31, 16384\n"
"v_exp_f32 v102, v102\n"
"s_add_u32 s30, s52, %[sVOFF]\n"
"v_exp_f32 v103, v103\n"
"s_lshl2_add_u32 s29, %[sW1K], s53\n"
"v_exp_f32 v104, v104\n"
"v_exp_f32 v105, v105\n"
"v_exp_f32 v106, v106\n"
"v_exp_f32 v107, v107\n"
"s_cbranch_vccnz LX_redo\n"
"LX_redone:\n"
"s_waitcnt vmcnt(3)\n"
"s_barrier\n"
"ds_read_b32 v140, v43 offset:40960\n"
"ds_read_b32 v142, v43 offset:41216\n"
"buffer_store_dword v18, %[vXO], s[32:35], s65 offen sc1\n"
"buffer_load_dword v22, %[vPQ], s[32:35], s64 offen sc1\n"
"v_cvt_pk_bf16_f32 v40, v96, v97\n"
"v_cvt_pk_bf16_f32 v116, v100, v101\n"
"v_cvt_pk_bf16_f32 v117, v102, v103\n"
"v_cvt_pk_bf16_f32 v118, v104, v105\n"
"v_cvt_pk_bf16_f32 v119, v106, v107\n"
"v_mfma_f32_16x16x16_bf16 v[120:123], v[28:29], v[40:41], 0\n"
"v_mov_b64 v[96:97], 0\n"
"v_mov_b64 v[98:99], 0\n"
"v_mfma_f32_16x16x32_bf16 v[120:123], v[28:31], v[116:119], v[120:123]\n"
"v_mov_b64 v[100:101], 0\n"
"v_mov_b64 v[102:103], 0\n"
"v_mfma_f32_16x16x32_bf16 v[124:127], v[132:135], v[116:119], 0\n"
"v_mov_b64 v[104:105], 0\n"
"v_mov_b64 v[106:107], 0\n"
"v_mfma_f32_16x16x32_bf16 v[128:131], v[136:139], v[116:119], 0\n"
"v_mov_b64 v[108:109], 0\n"
"v_mov_b64 v[110:111], 0\n"
"s_waitcnt lgkmcnt(0)\n"
"v_mfma_f32_16x16x16_bf16 v[124:127], v[140:141], v[40:41], v[124:127]\n"
"v_mov_b64 v[112:113], 0\n"
"v_mov_b64 v[114:115], 0\n"
"v_mfma_f32_16x16x16_bf16 v[128:131], v[142:143], v[40:41], v[128:131]\n"
"v_rcp_f32 v38, v120\n"
"v_add_u32 v20, %[sW1K], v23\n"
"ds_read_b128 v[60:63], v20 offset:16384\n"
"ds_read_b128 v[64:67], v23 offset:8192\n"
"ds_read_b128 v[68:71], v23 offset:9216\n"
"ds_read_b128 v[72:75], v23 offset:10240\n"
"ds_read_b128 v[76:79], v23 offset:11264\n"
"ds_read_b128 v[80:83], v23 offset:12288\n"
"ds_read_b128 v[84:87], v23 offset:13312\n"
"ds_read_b128 v[88:91], v23 offset:14336\n"
"ds_read_b128 v[92:95], v23 offset:15360\n"
"v_mov_b32 v20, 0\n"
"v_pk_mul_f32 v[124:125], v[124:125], v[38:39] op_sel_hi:[1,0]\n"
"v_pk_mul_f32 v[126:127], v[126:127], v[38:39] op_sel_hi:[1,0]\n"
"v_pk_mul_f32 v[128:129], v[128:129], v[38:39] op_sel_hi:[1,0]\n"
"v_pk_mul_f32 v[130:131], v[130:131], v[38:39] op_sel_hi:[1,0]\n"
"v_cvt_pk_bf16_f32 v14, v124, v125\n"
"v_cvt_pk_bf16_f32 v15, v126, v127\n"
"v_cvt_pk_bf16_f32 v16, v128, v129\n"
"v_cvt_pk_bf16_f32 v17, v130, v131\n"
"s_cmp_eq_u32 s43, 64\n"
"s_cbranch_scc1 LX_done\n"
"buffer_store_dwordx4 v[14:17], %[vOT], s[32:35], s38 offen sc1\n"
"buffer_store_dwordx2 v[10:11], %[vOST], s[32:35], s50 offen sc1\n"
"s_waitcnt vmcnt(4)\n"
"v_max_i32_dpp v42, v42, v42 quad_perm:[1,0,3,2] row_mask:0xf bank_mask:0xf\n"
"v_readfirstlane_b32 s92, v13\n"
"v_readfirstlane_b32 s96, v12\n"
"v_max_i32_dpp v42, v42, v42 quad_perm:[2,3,0,1] row_mask:0xf bank_mask:0xf\n"
"s_nop 0\n"
"v_readlane_b32 s36, v42, 0\n"
"v_readlane_b32 s37, v42, 4\n"
"s_max_i32 s36, s36, s84\n"
"s_cmp_lt_i32 s37, 0\n"
"s_cselect_b32 s37, 1120, s37\n"
"s_add_u32 s36, s36, %[sG1]\n"
"s_add_u32 s37, s37, %[sG2]\n"
"s_add_u32 s36, s36, s85\n"
"s_add_u32 s37, s37, s85\n"
"s_add_u32 s37, s37, s36\n"
"s_lshr_b32 s36, s36, 4\n"
"s_lshr_b32 s37, s37, 4\n"
"s_cmp_lg_u32 s96, s43\n"
"s_cbranch_scc0 LX_fast\n"
"s_branch LX_step\n"
"LX_lastm:\n"
"buffer_store_dwordx2 v[34:35], %[vOST], s[32:35], s49 offen sc1\n"
"s_nop 7\n"
"v_pk_add_f32 v[112:113], v[112:113], v[114:115]\n"
"v_cvt_pk_bf16_f32 v24, v112, v113\n"
"s_branch LX_idle\n"
"LX_redo:\n"
"s_add_u32 s96, s43, 1\n"
"s_min_u32 s96, s96, 64\n"
"s_sub_u32 s52, s52, 786432\n"
"s_xor_b32 s53, s53, 20480\n"
"s_mov_b64 exec, -1\n"
"s_mov_b32 s41, 0\n"
"LX_pq:\n"
"buffer_load_dword v22, %[vPQ], s[32:35], s64 offen sc1\n"
"s_add_u32 s41, s41, 1\n"
"s_cmp_gt_u32 s41, 60000\n"
"s_cbranch_scc1 LX_tmo\n"
"s_waitcnt vmcnt(0)\n"
"v_cmp_gt_i32 vcc, s96, v22\n"
"s_cbranch_vccnz LX_pq\n"
"LX_pq_ok:\n"
"s_mov_b64 exec, -1\n"
"s_add_u32 s31, %[sW1K], s53\n"
"s_add_u32 s31, s31, 16384\n"
"s_add_u32 s30, s52, %[sVOFF]\n"
"s_lshl2_add_u32 s29, %[sW1K], s53\n"
"s_mov_b32 m0, s29\n"
"s_nop 0\n"
"buffer_load_dwordx4 %[vL16], s[32:35], s52 offen offset:0 sc1 lds\n"
"buffer_load_dwordx4 %[vL16], s[32:35], s52 offen offset:1024 sc1 lds\n"
"buffer_load_dwordx4 %[vL16], s[32:35], s52 offen offset:2048 sc1 lds\n"
"buffer_load_dwordx4 %[vL16], s[32:35], s52 offen offset:3072 sc1 lds\n"
"s_mov_b32 m0, s31\n"
"s_nop 0\n"
"buffer_load_dwordx4 %[vL16], s[32:35], s30 offen sc1 lds\n"
"s_add_u32 s52, s52, 786432\n"
"s_xor_b32 s53, s53, 20480\n"
"s_add_u32 s31, %[sW1K], s53\n"
"s_add_u32 s31, s31, 16384\n"
"s_add_u32 s30, s52, %[sVOFF]\n"
"s_lshl2_add_u32 s29, %[sW1K], s53\n"
"s_nop 0\n"
"s_waitcnt vmcnt(0)\n"
"s_branch LX_redone\n"
"LX_again0:\n"
"s_mov_b32 s93, 0\n"
"s_branch LX_again\n"
"LX_again2:\n"
"s_mov_b32 s93, 2\n"
"LX_again:\n"
"s_sub_u32 s40, s43, s86\n"
"s_mov_b32 s86, s43\n"
"s_sub_u32 s40, s40, 1\n"
"s_cmp_lt_u32 s40, 2\n"
"s_cselect_b32 s40, 16, 0\n"
"s_add_u32 s85, s85, s40\n"
"s_min_u32 s85, s85, 480\n"
"s_add_u32 s41, s41, 1\n"
"s_cmp_gt_u32 s41, 0\n"
"s_cbranch_scc1 LX_retry\n"
"v_mov_b64 v[96:97], 0\n"
"v_mov_b64 v[98:99], 0\n"
"v_mov_b64 v[100:101], 0\n"
"v_mov_b64 v[102:103], 0\n"
"v_mov_b64 v[104:105], 0\n"
"v_mov_b64 v[106:107], 0\n"
"v_mov_b64 v[108:109], 0\n"
"v_mov_b64 v[110:111], 0\n"
"v_mov_b64 v[112:113], 0\n"
"v_mov_b64 v[114:115], 0\n"
"v_mov_b32 v20, 0\n"
"v_add_u32 v13, 50, v13\n"
"s_add_u32 s92, s92, 50\n"
"s_cmp_eq_u32 s93, 2\n"
"s_cbranch_scc1 LX_load2\n"
"s_branch LX_load\n"
"LX_retry:\n"
"v_lshrrev_b32 v32, 4, %[vL16]\n"
"v_min_u32 v32, 35, v32\n"
"v_and_b32 v33, 1, v32\n"
"v_lshrrev_b32 v34, 5, v32\n"
"v_lshrrev_b32 v32, 1, v32\n"
"v_lshlrev_b32 v33, 7, v33\n"
"v_mul_u32_u24 v34, 24576, v34\n"
"v_add_u32 v33, v34, v33\n"
"v_lshl_add_u32 v32, v32, 9, v33\n"
"v_mov_b64 v[96:97], 0\n"
"v_mov_b64 v[98:99], 0\n"
"v_mov_b64 v[100:101], 0\n"
"v_mov_b64 v[102:103], 0\n"
"v_mov_b64 v[104:105], 0\n"
"v_mov_b64 v[106:107], 0\n"
"v_mov_b64 v[108:109], 0\n"
"v_mov_b64 v[110:111], 0\n"
"v_mov_b64 v[112:113], 0\n"
"v_mov_b64 v[114:115], 0\n"
"v_mov_b32 v20, 0\n"
"LX_hint:\n"
"buffer_load_dword v33, v32, s[32:35], s46 offen sc1\n"
"s_add_u32 s41, s41, 1\n"
"s_cmp_gt_u32 s41, 60000\n"
"s_cbranch_scc1 LX_tmo\n"
"s_waitcnt vmcnt(0)\n"
"v_cmp_eq_u32 vcc, -1, v33\n"
"s_memrealtime s[90:91]\n"
"s_cbranch_vccnz LX_hint\n"
"s_waitcnt lgkmcnt(0)\n"
"s_add_u32 s92, s90, 22\n"
"s_add_u32 s40, s92, s37\n"
"v_mov_b32 v13, s40\n"
"s_add_u32 s40, s92, s36\n"
"s_mov_b64 exec, 0xf0\n"
"v_mov_b32 v13, s40\n"
"s_mov_b64 exec, -1\n"
"v_mov_b32 v12, s51\n"
"s_branch LX_load\n"
"LX_tmo:\n"
"s_mov_b64 exec, 1\n"
"v_mov_b32 v20, 1\n"
"s_mov_b32 s40, 1065088\n"
"buffer_store_dword v20, off, s[32:35], s40 sc1\n"
"s_endpgm\n"
"LX_done:\n"
"buffer_store_dwordx4 v[14:17], %[vOT], s[32:35], s38 offen sc1\n"
"s_waitcnt vmcnt(0)\n"
"s_barrier\n"
"v_mov_b32 v20, 64\n"
"buffer_store_dword v20, %[vXO], s[32:35], s65 offen sc1\n"
        : "+{s[32:35]}"(sWS), "+{v44}"(vGQ), "+{v45}"(vGV), "+{v46}"(vTQ0), "+{v47}"(vTV), "+{v48}"(vPad)
        : [sWQ] "s"(sWQ), [sWK] "s"(sWK), [sWV] "s"(sWV),
          [sW1K] "s"(sW1K), [sQOFF] "s"(sQOFF), [sVOFF] "s"(sVOFF), [sROT] "s"(rfl(((h & (HXV ? 3 : 7)) * STX) >> 2)), [sREP] "s"(sREP), [sG1] "s"(g1), [sG2] "s"(g2), [sHB] "s"(hb_off()),
          [vL16] "v"(vL16),
          [vIDX] "v"(vIDX), [vXW] "v"(vXW), [vXR] "v"(vXR), [vOST] "v"(vOST), [vOT] "v"(vOT),
          [vTS] "v"(vTS), [vPQ] "v"(vPQ), [vXO] "v"(vXO)
        : "v10", "v11", "v12", "v13", "v14", "v15", "v16", "v17", "v18", "v19", "v20", "v21", "v22", "v23", "v24", "v25", "v26", "v27", "v28", "v29", "v30", "v31", "v32", "v33",
"v34", "v35", "v36", "v37", "v38", "v39", "v40", "v41", "v42", "v43", "v49", "v50", "v51", "v52", "v53", "v54", "v55", "v56", "v57", "v58", "v59", "v60", "v61", "v62",
"v63", "v64", "v65", "v66", "v67", "v68", "v69", "v70", "v71", "v72", "v73", "v74", "v75", "v76", "v77", "v78", "v79", "v80", "v81", "v82", "v83", "v84", "v85", "v86",
"v87", "v88", "v89", "v90", "v91", "v92", "v93", "v94", "v95", "v96", "v97", "v98", "v99", "v100", "v101", "v102", "v103", "v104", "v105", "v106", "v107", "v108", "v109", "v110",
"v111", "v112", "v113", "v114", "v115", "v116", "v117", "v118", "v119", "v120", "v121", "v122", "v123", "v124", "v125", "v126", "v127", "v128", "v129", "v130", "v131", "v132", "v133", "v134",
"v135", "v136", "v137", "v138", "v139", "v140", "v141", "v142", "v143", "v144", "v145", "v146", "v147", "v148", "v149", "v150", "v151", "v152", "v153", "v154", "v155", "v156", "v157", "v158",
"v159", "v160", "v161", "v162", "v163", "v164", "v165", "v166", "v167", "v168", "v169", "v170", "v171", "v172", "v173", "v174", "v175", "v176", "v177", "v178", "v179", "v180", "v181", "v182",
"v183", "v184", "v185", "v186", "v187", "v188", "v189", "v190", "v191", "v192", "v193", "v194", "v195", "v196", "v197", "v198", "v199", "v200", "v201", "v202", "v203", "v204", "v205", "v206",
"v207", "v208", "v209", "v210", "v211", "v212", "v213", "v214", "v215", "v216", "v217", "v218", "v219", "v220", "v221", "v222", "v223", "v224", "v225", "v226", "v227", "v228", "v229", "v230",
"v231", "v232", "v233", "v234", "v235", "v236", "v237", "v238", "v239", "v240", "v241", "v242", "v243", "v244", "v245", "v246", "v247", "v248", "v249", "v250", "v251", "v252", "v253", "v254",
"v255", "a0", "a1", "a2", "a3", "a4", "a5", "a6", "a7", "a8", "a9", "a10", "a11", "a12", "a13", "a14", "a15", "a16", "a17", "a18", "a19", "a20", "a21", "a22",
"a23", "a24", "a25", "a26", "a27", "a28", "a29", "a30", "a31", "a32", "a33", "a34", "a35", "a36", "a37", "a38", "a39", "a40", "a41", "a42", "a43", "a44", "a45", "a46",
"a47", "a48", "a49", "a50", "a51", "a52", "a53", "a54", "a55", "a56", "a57", "a58", "a59", "a60", "a61", "a62", "a63", "a64", "a65", "a66", "a67", "a68", "a69", "a70",
"a71", "a72", "a73", "a74", "a75", "a76", "a77", "a78", "a79", "a80", "a81", "a82", "a83", "a84", "a85", "a86", "a87", "a88", "a89", "a90", "a91", "a92", "a93", "a94",
"a95", "a96", "a97", "a98", "a99", "a100", "a101", "a102", "a103", "a104", "a105", "a106", "a107", "a108", "a109", "a110", "a111", "a112", "a113", "a114", "a115", "a116", "a117", "a118",
"a119", "a120", "a121", "a122", "a123", "a124", "a125", "a126", "a127", "a128", "a129", "a130", "a131", "a132", "a133", "a134", "a135", "a136", "a137", "a138", "a139", "a140", "a141", "a142",
"a143", "a144", "a145", "a146", "a147", "a148", "a149", "a150", "a151", "a152", "a153", "a154", "a155", "a156", "a157", "a158", "a159", "a160", "a161", "a162", "a163", "a164", "a165", "a166",
"a167", "a168", "a169", "a170", "a171", "a172", "a173", "a174", "a175", "a176", "a177", "a178", "a179", "a180", "a181", "a182", "a183", "a184", "a185", "a186", "a187", "a188", "a189", "a190",
"a191", "a192", "a193", "a194", "a195", "a196", "a197", "a198", "a199", "a200", "a201", "a202", "a203", "a204", "a205", "a206", "a207", "a208", "a209", "a210", "a211", "a212", "a213", "a214",
"a215", "a216", "a217", "a218", "a219", "a220", "a221", "a222", "a223", "a224", "a225", "a226", "a227", "a228", "a229", "a230", "a231", "a232", "a233", "a234", "a235", "a236", "a237", "a238",
"a239", "a240", "a241", "a242", "a243", "a244", "a245", "a246", "a247", "a248", "a249", "a250", "a251", "a252", "a253", "a254", "a255", "s36", "s37", "s38", "s39", "s40", "s41", "s42",
"s43", "s44", "s45", "s46", "s47", "s48", "s49", "s50", "s51", "s52", "s53", "s54", "s55", "s56", "s57", "s58", "s59", "s60", "s61", "s62", "s63", "s64", "s65", "s66",
"s67", "s68", "s69", "s70", "s71", "s72", "s73", "s74", "s75", "s76", "s77", "s78", "s79", "s80", "s81", "s82", "s83", "s84", "s85", "s86", "s87", "s88", "s89", "s90",
"s91", "s92", "s93", "s94", "s95", "s96", "s97", "s98", "s99", "vcc", "scc", "memory", "s29", "s30", "s31");
}

__device__ __forceinline__ void role_y(char* ws, const void* wo, const void* mem0, int y) {
    int tid = threadIdx.x; asm volatile("" : "+v"(tid));
    const int w = rfl(tid >> 6), l = tid & 63, g = l >> 4, c = l & 15;
    const int rg = y & 1, J = y >> 1;
    v4i sWS = make_srd(ws, WS_B);
    const v4i sWO = make_srd(wo, 4096 * 1152), sM0 = make_srd(mem0, 9216);
    const int og = (c >> 1) ^ (g & 1) ^ ((g >> 1) << 2);
    unsigned vGO = (w * 1024 + 4 * g + (og >> 1)) * 1152 + 64 * J + (og & 1) * 32 + (l & 1) * 16;
    const int x = c >> 2, p = c & 3;
    unsigned vT[2][2];
#pragma unroll
    for (int u = 0; u < 2; ++u)
#pragma unroll
        for (int T = 0; T < 2; ++T)
            vT[u][T] = w * 32768 + (8 * (g >> 1) + 2 * (x >> 1) + (g & 1)) * 256 + ((((2 * u + (x & 1)) * 2 + T) ^ (g & 1) ^ ((x >> 1) << 2)) * 32) + p * 8;
    const unsigned vIDX = (c & 2) ? 0xEEEEu : 0x4444u;
    const unsigned vOL = WS_RING + rg * 524288 + w * 32768 + l * 16;
    const unsigned vRW = w * 1024 + (g & 1) * 256 + (c >> 1) * 16 + (g >> 1) * 8 + (c & 1) * 4;
    const int T = l >> 5, ihi = (l >> 4) & 1, hf = (l >> 3) & 1, b = (l >> 2) & 1, dw = l & 3;
    const int Tf = NFIN == 1 ? T : (w & 1), cf = 2 * J + Tf;
    const unsigned vRR = Tf * 512 + (2 * ihi + b) * 128 + (4 * hf + dw) * 16;
    const int ct = 2 * J + T, f = ct >> 2;
    const unsigned vMA = WS_RING + 1048576 + w * 262144 + (f >> 3) * 32768 + (f & 7) * 1024 + (ct & 3) * 256 + rg * 128 + (l & 31) * 4;
    const unsigned vMST = w < NFIN ? (unsigned)(WS_RING + 1048576 + T * 262144 + (cf >> 5) * 32768 + (cf & 31) * 256 + rg * 128 + (l & 31) * 4) : OOB;
    const unsigned vRP = (tid == 0 && (STY ? J < 2 : J % 9 == 0)) ? (unsigned)(512 + ((J != 0) * 2 + rg) * 128) : OOB;
    const unsigned vYR = tid == 0 ? (unsigned)(y * 128) : OOB;
    const unsigned vM0 = (4 * rg + 2 * ihi + b) * 1152 + (32 * J + 16 * T + 8 * hf + 2 * dw) * 2;
    const int sW32K = w * 32768;
    asm volatile(
"LY_top:\n"
"s_nop 4\n"
"v_mbcnt_lo_u32_b32 v26, -1, 0\n"
"v_min_u32 v26, 2, v26\n"
"v_lshlrev_b32 v26, 10, v26\n"
"buffer_load_dword v25, v26, s[32:35], %[sHB] offen sc1\n"
"buffer_load_dword v24, %[vM0], %[sM0], 0 offen\n"
"v_mov_b32 v23, -1\n"
"s_mov_b32 s40, 65536\n"
"buffer_store_dword v23, %[vMA], s[32:35], s40 offen sc1\n"
"s_mov_b32 s40, 73728\n"
"buffer_store_dword v23, %[vMA], s[32:35], s40 offen sc1\n"
"s_mov_b32 s40, 81920\n"
"buffer_store_dword v23, %[vMA], s[32:35], s40 offen sc1\n"
"s_mov_b32 s40, 90112\n"
"buffer_store_dword v23, %[vMA], s[32:35], s40 offen sc1\n"
"s_mov_b32 s40, 131072\n"
"buffer_store_dword v23, %[vMA], s[32:35], s40 offen sc1\n"
"s_mov_b32 s40, 139264\n"
"buffer_store_dword v23, %[vMA], s[32:35], s40 offen sc1\n"
"s_mov_b32 s40, 147456\n"
"buffer_store_dword v23, %[vMA], s[32:35], s40 offen sc1\n"
"s_mov_b32 s40, 155648\n"
"buffer_store_dword v23, %[vMA], s[32:35], s40 offen sc1\n"
"s_waitcnt vmcnt(8)\n"
"v_min_u32 v24, -2, v24\n"
"s_mov_b32 s40, 0\n"
"buffer_store_dword v24, %[vMA], s[32:35], s40 offen sc1\n"
"s_mov_b32 s40, 8192\n"
"buffer_store_dword v24, %[vMA], s[32:35], s40 offen sc1\n"
"s_mov_b32 s40, 16384\n"
"buffer_store_dword v24, %[vMA], s[32:35], s40 offen sc1\n"
"s_mov_b32 s40, 24576\n"
"buffer_store_dword v24, %[vMA], s[32:35], s40 offen sc1\n"
"v_mov_b32 v120, v44\n"
"v_mov_b32 v121, v45\n"
"v_mov_b32 v122, v46\n"
"v_mov_b32 v123, v47\n"
"v_mov_b32 v124, v48\n"
"s_mov_b32 s46, %[sROT]\n"
"s_mov_b32 s47, %[sROT]\n"
"s_cmp_ge_u32 s47, 4\n"
"s_cselect_b32 s40, 4, 0\n"
"s_sub_u32 s47, s47, s40\n"
"s_mov_b32 s48, 2\n"
"LY_w_pre:\n"
"s_xor_b32 s49, s46, %[sHX]\n"
"s_mul_i32 s49, s49, 147456\n"
"s_lshl_b32 s50, s47, 13\n"
"s_add_u32 s50, s50, %[sW32K]\n"
"s_add_u32 m0, s50, 0\n"
"s_add_u32 s40, s49, 0\n"
"buffer_load_dwordx4 v120, %[sWO], s40 offen  lds\n"
"s_add_u32 m0, s50, 1024\n"
"s_add_u32 s40, s49, 18432\n"
"buffer_load_dwordx4 v120, %[sWO], s40 offen  lds\n"
"s_add_u32 m0, s50, 2048\n"
"s_add_u32 s40, s49, 36864\n"
"buffer_load_dwordx4 v120, %[sWO], s40 offen  lds\n"
"s_add_u32 m0, s50, 3072\n"
"s_add_u32 s40, s49, 55296\n"
"buffer_load_dwordx4 v120, %[sWO], s40 offen  lds\n"
"s_add_u32 m0, s50, 4096\n"
"s_add_u32 s40, s49, 73728\n"
"buffer_load_dwordx4 v120, %[sWO], s40 offen  lds\n"
"s_add_u32 m0, s50, 5120\n"
"s_add_u32 s40, s49, 92160\n"
"buffer_load_dwordx4 v120, %[sWO], s40 offen  lds\n"
"s_add_u32 m0, s50, 6144\n"
"s_add_u32 s40, s49, 110592\n"
"buffer_load_dwordx4 v120, %[sWO], s40 offen  lds\n"
"s_add_u32 m0, s50, 7168\n"
"s_add_u32 s40, s49, 129024\n"
"buffer_load_dwordx4 v120, %[sWO], s40 offen  lds\n"
"s_add_u32 s46, s46, 1\n"
"s_cmp_eq_u32 s46, 8\n"
"s_cselect_b32 s46, 0, s46\n"
"s_add_u32 s47, s47, 1\n"
"s_cmp_eq_u32 s47, 4\n"
"s_cselect_b32 s47, 0, s47\n"
"s_sub_u32 s48, s48, 1\n"
"s_cmp_lg_u32 s48, 0\n"
"s_cbranch_scc1 LY_w_pre\n"
"s_mov_b32 s48, 8\n"
"s_cmp_eq_u32 %[sROT], 1\n"
"s_cbranch_scc1 LY_w_1\n"
"s_cmp_eq_u32 %[sROT], 2\n"
"s_cbranch_scc1 LY_w_2\n"
"s_cmp_eq_u32 %[sROT], 3\n"
"s_cbranch_scc1 LY_w_3\n"
"s_cmp_eq_u32 %[sROT], 4\n"
"s_cbranch_scc1 LY_w_4\n"
"s_cmp_eq_u32 %[sROT], 5\n"
"s_cbranch_scc1 LY_w_5\n"
"s_cmp_eq_u32 %[sROT], 6\n"
"s_cbranch_scc1 LY_w_6\n"
"s_cmp_eq_u32 %[sROT], 7\n"
"s_cbranch_scc1 LY_w_7\n"
"LY_w_0:\n"
"s_cmp_gt_u32 s48, 2\n"
"s_cbranch_scc1 LY_w_a0\n"
"s_waitcnt vmcnt(0)\n"
"s_branch LY_w_b0\n"
"LY_w_a0:\n"
"s_xor_b32 s49, s46, %[sHX]\n"
"s_mul_i32 s49, s49, 147456\n"
"s_lshl_b32 s50, s47, 13\n"
"s_add_u32 s50, s50, %[sW32K]\n"
"s_add_u32 m0, s50, 0\n"
"s_add_u32 s40, s49, 0\n"
"buffer_load_dwordx4 v120, %[sWO], s40 offen  lds\n"
"s_add_u32 m0, s50, 1024\n"
"s_add_u32 s40, s49, 18432\n"
"buffer_load_dwordx4 v120, %[sWO], s40 offen  lds\n"
"s_add_u32 m0, s50, 2048\n"
"s_add_u32 s40, s49, 36864\n"
"buffer_load_dwordx4 v120, %[sWO], s40 offen  lds\n"
"s_add_u32 m0, s50, 3072\n"
"s_add_u32 s40, s49, 55296\n"
"buffer_load_dwordx4 v120, %[sWO], s40 offen  lds\n"
"s_add_u32 m0, s50, 4096\n"
"s_add_u32 s40, s49, 73728\n"
"buffer_load_dwordx4 v120, %[sWO], s40 offen  lds\n"
"s_add_u32 m0, s50, 5120\n"
"s_add_u32 s40, s49, 92160\n"
"buffer_load_dwordx4 v120, %[sWO], s40 offen  lds\n"
"s_add_u32 m0, s50, 6144\n"
"s_add_u32 s40, s49, 110592\n"
"buffer_load_dwordx4 v120, %[sWO], s40 offen  lds\n"
"s_add_u32 m0, s50, 7168\n"
"s_add_u32 s40, s49, 129024\n"
"buffer_load_dwordx4 v120, %[sWO], s40 offen  lds\n"
"s_add_u32 s46, s46, 1\n"
"s_cmp_eq_u32 s46, 8\n"
"s_cselect_b32 s46, 0, s46\n"
"s_add_u32 s47, s47, 1\n"
"s_cmp_eq_u32 s47, 4\n"
"s_cselect_b32 s47, 0, s47\n"
"s_waitcnt vmcnt(16)\n"
"LY_w_b0:\n"
"ds_read_b64_tr_b16 a[0:1], v121 offset:0\n"
"ds_read_b64_tr_b16 a[2:3], v123 offset:0\n"
"ds_read_b64_tr_b16 a[4:5], v121 offset:4096\n"
"ds_read_b64_tr_b16 a[6:7], v123 offset:4096\n"
"ds_read_b64_tr_b16 a[8:9], v122 offset:0\n"
"ds_read_b64_tr_b16 a[10:11], v124 offset:0\n"
"ds_read_b64_tr_b16 a[12:13], v122 offset:4096\n"
"ds_read_b64_tr_b16 a[14:15], v124 offset:4096\n"
"s_waitcnt lgkmcnt(0)\n"
"ds_read_b64_tr_b16 a[16:17], v121 offset:1024\n"
"ds_read_b64_tr_b16 a[18:19], v123 offset:1024\n"
"ds_read_b64_tr_b16 a[20:21], v121 offset:5120\n"
"ds_read_b64_tr_b16 a[22:23], v123 offset:5120\n"
"ds_read_b64_tr_b16 a[24:25], v122 offset:1024\n"
"ds_read_b64_tr_b16 a[26:27], v124 offset:1024\n"
"ds_read_b64_tr_b16 a[28:29], v122 offset:5120\n"
"ds_read_b64_tr_b16 a[30:31], v124 offset:5120\n"
"s_waitcnt lgkmcnt(0)\n"
"s_mov_b32 s51, 0\n"
"s_cmp_eq_u32 s48, 6\n"
"s_cbranch_scc1 LY_yr\n"
"LY_yr0:\n"
"s_sub_u32 s48, s48, 1\n"
"s_cmp_eq_u32 s48, 0\n"
"s_cbranch_scc1 LY_w_end\n"
"LY_w_1:\n"
"s_cmp_gt_u32 s48, 2\n"
"s_cbranch_scc1 LY_w_a1\n"
"s_waitcnt vmcnt(0)\n"
"s_branch LY_w_b1\n"
"LY_w_a1:\n"
"s_xor_b32 s49, s46, %[sHX]\n"
"s_mul_i32 s49, s49, 147456\n"
"s_lshl_b32 s50, s47, 13\n"
"s_add_u32 s50, s50, %[sW32K]\n"
"s_add_u32 m0, s50, 0\n"
"s_add_u32 s40, s49, 0\n"
"buffer_load_dwordx4 v120, %[sWO], s40 offen  lds\n"
"s_add_u32 m0, s50, 1024\n"
"s_add_u32 s40, s49, 18432\n"
"buffer_load_dwordx4 v120, %[sWO], s40 offen  lds\n"
"s_add_u32 m0, s50, 2048\n"
"s_add_u32 s40, s49, 36864\n"
"buffer_load_dwordx4 v120, %[sWO], s40 offen  lds\n"
"s_add_u32 m0, s50, 3072\n"
"s_add_u32 s40, s49, 55296\n"
"buffer_load_dwordx4 v120, %[sWO], s40 offen  lds\n"
"s_add_u32 m0, s50, 4096\n"
"s_add_u32 s40, s49, 73728\n"
"buffer_load_dwordx4 v120, %[sWO], s40 offen  lds\n"
"s_add_u32 m0, s50, 5120\n"
"s_add_u32 s40, s49, 92160\n"
"buffer_load_dwordx4 v120, %[sWO], s40 offen  lds\n"
"s_add_u32 m0, s50, 6144\n"
"s_add_u32 s40, s49, 110592\n"
"buffer_load_dwordx4 v120, %[sWO], s40 offen  lds\n"
"s_add_u32 m0, s50, 7168\n"
"s_add_u32 s40, s49, 129024\n"
"buffer_load_dwordx4 v120, %[sWO], s40 offen  lds\n"
"s_add_u32 s46, s46, 1\n"
"s_cmp_eq_u32 s46, 8\n"
"s_cselect_b32 s46, 0, s46\n"
"s_add_u32 s47, s47, 1\n"
"s_cmp_eq_u32 s47, 4\n"
"s_cselect_b32 s47, 0, s47\n"
"s_waitcnt vmcnt(16)\n"
"LY_w_b1:\n"
"ds_read_b64_tr_b16 a[32:33], v121 offset:8192\n"
"ds_read_b64_tr_b16 a[34:35], v123 offset:8192\n"
"ds_read_b64_tr_b16 a[36:37], v121 offset:12288\n"
"ds_read_b64_tr_b16 a[38:39], v123 offset:12288\n"
"ds_read_b64_tr_b16 a[40:41], v122 offset:8192\n"
"ds_read_b64_tr_b16 a[42:43], v124 offset:8192\n"
"ds_read_b64_tr_b16 a[44:45], v122 offset:12288\n"
"ds_read_b64_tr_b16 a[46:47], v124 offset:12288\n"
"s_waitcnt lgkmcnt(0)\n"
"ds_read_b64_tr_b16 a[48:49], v121 offset:9216\n"
"ds_read_b64_tr_b16 a[50:51], v123 offset:9216\n"
"ds_read_b64_tr_b16 a[52:53], v121 offset:13312\n"
"ds_read_b64_tr_b16 a[54:55], v123 offset:13312\n"
"ds_read_b64_tr_b16 a[56:57], v122 offset:9216\n"
"ds_read_b64_tr_b16 a[58:59], v124 offset:9216\n"
"ds_read_b64_tr_b16 a[60:61], v122 offset:13312\n"
"ds_read_b64_tr_b16 a[62:63], v124 offset:13312\n"
"s_waitcnt lgkmcnt(0)\n"
"s_mov_b32 s51, 1\n"
"s_cmp_eq_u32 s48, 6\n"
"s_cbranch_scc1 LY_yr\n"
"LY_yr1:\n"
"s_sub_u32 s48, s48, 1\n"
"s_cmp_eq_u32 s48, 0\n"
"s_cbranch_scc1 LY_w_end\n"
"LY_w_2:\n"
"s_cmp_gt_u32 s48, 2\n"
"s_cbranch_scc1 LY_w_a2\n"
"s_waitcnt vmcnt(0)\n"
"s_branch LY_w_b2\n"
"LY_w_a2:\n"
"s_xor_b32 s49, s46, %[sHX]\n"
"s_mul_i32 s49, s49, 147456\n"
"s_lshl_b32 s50, s47, 13\n"
"s_add_u32 s50, s50, %[sW32K]\n"
"s_add_u32 m0, s50, 0\n"
"s_add_u32 s40, s49, 0\n"
"buffer_load_dwordx4 v120, %[sWO], s40 offen  lds\n"
"s_add_u32 m0, s50, 1024\n"
"s_add_u32 s40, s49, 18432\n"
"buffer_load_dwordx4 v120, %[sWO], s40 offen  lds\n"
"s_add_u32 m0, s50, 2048\n"
"s_add_u32 s40, s49, 36864\n"
"buffer_load_dwordx4 v120, %[sWO], s40 offen  lds\n"
"s_add_u32 m0, s50, 3072\n"
"s_add_u32 s40, s49, 55296\n"
"buffer_load_dwordx4 v120, %[sWO], s40 offen  lds\n"
"s_add_u32 m0, s50, 4096\n"
"s_add_u32 s40, s49, 73728\n"
"buffer_load_dwordx4 v120, %[sWO], s40 offen  lds\n"
"s_add_u32 m0, s50, 5120\n"
"s_add_u32 s40, s49, 92160\n"
"buffer_load_dwordx4 v120, %[sWO], s40 offen  lds\n"
"s_add_u32 m0, s50, 6144\n"
"s_add_u32 s40, s49, 110592\n"
"buffer_load_dwordx4 v120, %[sWO], s40 offen  lds\n"
"s_add_u32 m0, s50, 7168\n"
"s_add_u32 s40, s49, 129024\n"
"buffer_load_dwordx4 v120, %[sWO], s40 offen  lds\n"
"s_add_u32 s46, s46, 1\n"
"s_cmp_eq_u32 s46, 8\n"
"s_cselect_b32 s46, 0, s46\n"
"s_add_u32 s47, s47, 1\n"
"s_cmp_eq_u32 s47, 4\n"
"s_cselect_b32 s47, 0, s47\n"
"s_waitcnt vmcnt(16)\n"
"LY_w_b2:\n"
"ds_read_b64_tr_b16 a[64:65], v121 offset:16384\n"
"ds_read_b64_tr_b16 a[66:67], v123 offset:16384\n"
"ds_read_b64_tr_b16 a[68:69], v121 offset:20480\n"
"ds_read_b64_tr_b16 a[70:71], v123 offset:20480\n"
"ds_read_b64_tr_b16 a[72:73], v122 offset:16384\n"
"ds_read_b64_tr_b16 a[74:75], v124 offset:16384\n"
"ds_read_b64_tr_b16 a[76:77], v122 offset:20480\n"
"ds_read_b64_tr_b16 a[78:79], v124 offset:20480\n"
"s_waitcnt lgkmcnt(0)\n"
"ds_read_b64_tr_b16 a[80:81], v121 offset:17408\n"
"ds_read_b64_tr_b16 a[82:83], v123 offset:17408\n"
"ds_read_b64_tr_b16 a[84:85], v121 offset:21504\n"
"ds_read_b64_tr_b16 a[86:87], v123 offset:21504\n"
"ds_read_b64_tr_b16 a[88:89], v122 offset:17408\n"
"ds_read_b64_tr_b16 a[90:91], v124 offset:17408\n"
"ds_read_b64_tr_b16 a[92:93], v122 offset:21504\n"
"ds_read_b64_tr_b16 a[94:95], v124 offset:21504\n"
"s_waitcnt lgkmcnt(0)\n"
"s_mov_b32 s51, 2\n"
"s_cmp_eq_u32 s48, 6\n"
"s_cbranch_scc1 LY_yr\n"
"LY_yr2:\n"
"s_sub_u32 s48, s48, 1\n"
"s_cmp_eq_u32 s48, 0\n"
"s_cbranch_scc1 LY_w_end\n"
"LY_w_3:\n"
"s_cmp_gt_u32 s48, 2\n"
"s_cbranch_scc1 LY_w_a3\n"
"s_waitcnt vmcnt(0)\n"
"s_branch LY_w_b3\n"
"LY_w_a3:\n"
"s_xor_b32 s49, s46, %[sHX]\n"
"s_mul_i32 s49, s49, 147456\n"
"s_lshl_b32 s50, s47, 13\n"
"s_add_u32 s50, s50, %[sW32K]\n"
"s_add_u32 m0, s50, 0\n"
"s_add_u32 s40, s49, 0\n"
"buffer_load_dwordx4 v120, %[sWO], s40 offen  lds\n"
"s_add_u32 m0, s50, 1024\n"
"s_add_u32 s40, s49, 18432\n"
"buffer_load_dwordx4 v120, %[sWO], s40 offen  lds\n"
"s_add_u32 m0, s50, 2048\n"
"s_add_u32 s40, s49, 36864\n"
"buffer_load_dwordx4 v120, %[sWO], s40 offen  lds\n"
"s_add_u32 m0, s50, 3072\n"
"s_add_u32 s40, s49, 55296\n"
"buffer_load_dwordx4 v120, %[sWO], s40 offen  lds\n"
"s_add_u32 m0, s50, 4096\n"
"s_add_u32 s40, s49, 73728\n"
"buffer_load_dwordx4 v120, %[sWO], s40 offen  lds\n"
"s_add_u32 m0, s50, 5120\n"
"s_add_u32 s40, s49, 92160\n"
"buffer_load_dwordx4 v120, %[sWO], s40 offen  lds\n"
"s_add_u32 m0, s50, 6144\n"
"s_add_u32 s40, s49, 110592\n"
"buffer_load_dwordx4 v120, %[sWO], s40 offen  lds\n"
"s_add_u32 m0, s50, 7168\n"
"s_add_u32 s40, s49, 129024\n"
"buffer_load_dwordx4 v120, %[sWO], s40 offen  lds\n"
"s_add_u32 s46, s46, 1\n"
"s_cmp_eq_u32 s46, 8\n"
"s_cselect_b32 s46, 0, s46\n"
"s_add_u32 s47, s47, 1\n"
"s_cmp_eq_u32 s47, 4\n"
"s_cselect_b32 s47, 0, s47\n"
"s_waitcnt vmcnt(16)\n"
"LY_w_b3:\n"
"ds_read_b64_tr_b16 a[96:97], v121 offset:24576\n"
"ds_read_b64_tr_b16 a[98:99], v123 offset:24576\n"
"ds_read_b64_tr_b16 a[100:101], v121 offset:28672\n"
"ds_read_b64_tr_b16 a[102:103], v123 offset:28672\n"
"ds_read_b64_tr_b16 a[104:105], v122 offset:24576\n"
"ds_read_b64_tr_b16 a[106:107], v124 offset:24576\n"
"ds_read_b64_tr_b16 a[108:109], v122 offset:28672\n"
"ds_read_b64_tr_b16 a[110:111], v124 offset:28672\n"
"s_waitcnt lgkmcnt(0)\n"
"ds_read_b64_tr_b16 a[112:113], v121 offset:25600\n"
"ds_read_b64_tr_b16 a[114:115], v123 offset:25600\n"
"ds_read_b64_tr_b16 a[116:117], v121 offset:29696\n"
"ds_read_b64_tr_b16 a[118:119], v123 offset:29696\n"
"ds_read_b64_tr_b16 a[120:121], v122 offset:25600\n"
"ds_read_b64_tr_b16 a[122:123], v124 offset:25600\n"
"ds_read_b64_tr_b16 a[124:125], v122 offset:29696\n"
"ds_read_b64_tr_b16 a[126:127], v124 offset:29696\n"
"s_waitcnt lgkmcnt(0)\n"
"s_mov_b32 s51, 3\n"
"s_cmp_eq_u32 s48, 6\n"
"s_cbranch_scc1 LY_yr\n"
"LY_yr3:\n"
"s_sub_u32 s48, s48, 1\n"
"s_cmp_eq_u32 s48, 0\n"
"s_cbranch_scc1 LY_w_end\n"
"LY_w_4:\n"
"s_cmp_gt_u32 s48, 2\n"
"s_cbranch_scc1 LY_w_a4\n"
"s_waitcnt vmcnt(0)\n"
"s_branch LY_w_b4\n"
"LY_w_a4:\n"
"s_xor_b32 s49, s46, %[sHX]\n"
"s_mul_i32 s49, s49, 147456\n"
"s_lshl_b32 s50, s47, 13\n"
"s_add_u32 s50, s50, %[sW32K]\n"
"s_add_u32 m0, s50, 0\n"
"s_add_u32 s40, s49, 0\n"
"buffer_load_dwordx4 v120, %[sWO], s40 offen  lds\n"
"s_add_u32 m0, s50, 1024\n"
"s_add_u32 s40, s49, 18432\n"
"buffer_load_dwordx4 v120, %[sWO], s40 offen  lds\n"
"s_add_u32 m0, s50, 2048\n"
"s_add_u32 s40, s49, 36864\n"
"buffer_load_dwordx4 v120, %[sWO], s40 offen  lds\n"
"s_add_u32 m0, s50, 3072\n"
"s_add_u32 s40, s49, 55296\n"
"buffer_load_dwordx4 v120, %[sWO], s40 offen  lds\n"
"s_add_u32 m0, s50, 4096\n"
"s_add_u32 s40, s49, 73728\n"
"buffer_load_dwordx4 v120, %[sWO], s40 offen  lds\n"
"s_add_u32 m0, s50, 5120\n"
"s_add_u32 s40, s49, 92160\n"
"buffer_load_dwordx4 v120, %[sWO], s40 offen  lds\n"
"s_add_u32 m0, s50, 6144\n"
"s_add_u32 s40, s49, 110592\n"
"buffer_load_dwordx4 v120, %[sWO], s40 offen  lds\n"
"s_add_u32 m0, s50, 7168\n"
"s_add_u32 s40, s49, 129024\n"
"buffer_load_dwordx4 v120, %[sWO], s40 offen  lds\n"
"s_add_u32 s46, s46, 1\n"
"s_cmp_eq_u32 s46, 8\n"
"s_cselect_b32 s46, 0, s46\n"
"s_add_u32 s47, s47, 1\n"
"s_cmp_eq_u32 s47, 4\n"
"s_cselect_b32 s47, 0, s47\n"
"s_waitcnt vmcnt(16)\n"
"LY_w_b4:\n"
"ds_read_b64_tr_b16 a[128:129], v121 offset:0\n"
"ds_read_b64_tr_b16 a[130:131], v123 offset:0\n"
"ds_read_b64_tr_b16 a[132:133], v121 offset:4096\n"
"ds_read_b64_tr_b16 a[134:135], v123 offset:4096\n"
"ds_read_b64_tr_b16 a[136:137], v122 offset:0\n"
"ds_read_b64_tr_b16 a[138:139], v124 offset:0\n"
"ds_read_b64_tr_b16 a[140:141], v122 offset:4096\n"
"ds_read_b64_tr_b16 a[142:143], v124 offset:4096\n"
"s_waitcnt lgkmcnt(0)\n"
"ds_read_b64_tr_b16 a[144:145], v121 offset:1024\n"
"ds_read_b64_tr_b16 a[146:147], v123 offset:1024\n"
"ds_read_b64_tr_b16 a[148:149], v121 offset:5120\n"
"ds_read_b64_tr_b16 a[150:151], v123 offset:5120\n"
"ds_read_b64_tr_b16 a[152:153], v122 offset:1024\n"
"ds_read_b64_tr_b16 a[154:155], v124 offset:1024\n"
"ds_read_b64_tr_b16 a[156:157], v122 offset:5120\n"
"ds_read_b64_tr_b16 a[158:159], v124 offset:5120\n"
"s_waitcnt lgkmcnt(0)\n"
"s_mov_b32 s51, 4\n"
"s_cmp_eq_u32 s48, 6\n"
"s_cbranch_scc1 LY_yr\n"
"LY_yr4:\n"
"s_sub_u32 s48, s48, 1\n"
"s_cmp_eq_u32 s48, 0\n"
"s_cbranch_scc1 LY_w_end\n"
"LY_w_5:\n"
"s_cmp_gt_u32 s48, 2\n"
"s_cbranch_scc1 LY_w_a5\n"
"s_waitcnt vmcnt(0)\n"
"s_branch LY_w_b5\n"
"LY_w_a5:\n"
"s_xor_b32 s49, s46, %[sHX]\n"
"s_mul_i32 s49, s49, 147456\n"
"s_lshl_b32 s50, s47, 13\n"
"s_add_u32 s50, s50, %[sW32K]\n"
"s_add_u32 m0, s50, 0\n"
"s_add_u32 s40, s49, 0\n"
"buffer_load_dwordx4 v120, %[sWO], s40 offen  lds\n"
"s_add_u32 m0, s50, 1024\n"
"s_add_u32 s40, s49, 18432\n"
"buffer_load_dwordx4 v120, %[sWO], s40 offen  lds\n"
"s_add_u32 m0, s50, 2048\n"
"s_add_u32 s40, s49, 36864\n"
"buffer_load_dwordx4 v120, %[sWO], s40 offen  lds\n"
"s_add_u32 m0, s50, 3072\n"
"s_add_u32 s40, s49, 55296\n"
"buffer_load_dwordx4 v120, %[sWO], s40 offen  lds\n"
"s_add_u32 m0, s50, 4096\n"
"s_add_u32 s40, s49, 73728\n"
"buffer_load_dwordx4 v120, %[sWO], s40 offen  lds\n"
"s_add_u32 m0, s50, 5120\n"
"s_add_u32 s40, s49, 92160\n"
"buffer_load_dwordx4 v120, %[sWO], s40 offen  lds\n"
"s_add_u32 m0, s50, 6144\n"
"s_add_u32 s40, s49, 110592\n"
"buffer_load_dwordx4 v120, %[sWO], s40 offen  lds\n"
"s_add_u32 m0, s50, 7168\n"
"s_add_u32 s40, s49, 129024\n"
"buffer_load_dwordx4 v120, %[sWO], s40 offen  lds\n"
"s_add_u32 s46, s46, 1\n"
"s_cmp_eq_u32 s46, 8\n"
"s_cselect_b32 s46, 0, s46\n"
"s_add_u32 s47, s47, 1\n"
"s_cmp_eq_u32 s47, 4\n"
"s_cselect_b32 s47, 0, s47\n"
"s_waitcnt vmcnt(16)\n"
"LY_w_b5:\n"
"ds_read_b64_tr_b16 a[160:161], v121 offset:8192\n"
"ds_read_b64_tr_b16 a[162:163], v123 offset:8192\n"
"ds_read_b64_tr_b16 a[164:165], v121 offset:12288\n"
"ds_read_b64_tr_b16 a[166:167], v123 offset:12288\n"
"ds_read_b64_tr_b16 a[168:169], v122 offset:8192\n"
"ds_read_b64_tr_b16 a[170:171], v124 offset:8192\n"
"ds_read_b64_tr_b16 a[172:173], v122 offset:12288\n"
"ds_read_b64_tr_b16 a[174:175], v124 offset:12288\n"
"s_waitcnt lgkmcnt(0)\n"
"ds_read_b64_tr_b16 a[176:177], v121 offset:9216\n"
"ds_read_b64_tr_b16 a[178:179], v123 offset:9216\n"
"ds_read_b64_tr_b16 a[180:181], v121 offset:13312\n"
"ds_read_b64_tr_b16 a[182:183], v123 offset:13312\n"
"ds_read_b64_tr_b16 a[184:185], v122 offset:9216\n"
"ds_read_b64_tr_b16 a[186:187], v124 offset:9216\n"
"ds_read_b64_tr_b16 a[188:189], v122 offset:13312\n"
"ds_read_b64_tr_b16 a[190:191], v124 offset:13312\n"
"s_waitcnt lgkmcnt(0)\n"
"s_mov_b32 s51, 5\n"
"s_cmp_eq_u32 s48, 6\n"
"s_cbranch_scc1 LY_yr\n"
"LY_yr5:\n"
"s_sub_u32 s48, s48, 1\n"
"s_cmp_eq_u32 s48, 0\n"
"s_cbranch_scc1 LY_w_end\n"
"LY_w_6:\n"
"s_cmp_gt_u32 s48, 2\n"
"s_cbranch_scc1 LY_w_a6\n"
"s_waitcnt vmcnt(0)\n"
"s_branch LY_w_b6\n"
"LY_w_a6:\n"
"s_xor_b32 s49, s46, %[sHX]\n"
"s_mul_i32 s49, s49, 147456\n"
"s_lshl_b32 s50, s47, 13\n"
"s_add_u32 s50, s50, %[sW32K]\n"
"s_add_u32 m0, s50, 0\n"
"s_add_u32 s40, s49, 0\n"
"buffer_load_dwordx4 v120, %[sWO], s40 offen  lds\n"
"s_add_u32 m0, s50, 1024\n"
"s_add_u32 s40, s49, 18432\n"
"buffer_load_dwordx4 v120, %[sWO], s40 offen  lds\n"
"s_add_u32 m0, s50, 2048\n"
"s_add_u32 s40, s49, 36864\n"
"buffer_load_dwordx4 v120, %[sWO], s40 offen  lds\n"
"s_add_u32 m0, s50, 3072\n"
"s_add_u32 s40, s49, 55296\n"
"buffer_load_dwordx4 v120, %[sWO], s40 offen  lds\n"
"s_add_u32 m0, s50, 4096\n"
"s_add_u32 s40, s49, 73728\n"
"buffer_load_dwordx4 v120, %[sWO], s40 offen  lds\n"
"s_add_u32 m0, s50, 5120\n"
"s_add_u32 s40, s49, 92160\n"
"buffer_load_dwordx4 v120, %[sWO], s40 offen  lds\n"
"s_add_u32 m0, s50, 6144\n"
"s_add_u32 s40, s49, 110592\n"
"buffer_load_dwordx4 v120, %[sWO], s40 offen  lds\n"
"s_add_u32 m0, s50, 7168\n"
"s_add_u32 s40, s49, 129024\n"
"buffer_load_dwordx4 v120, %[sWO], s40 offen  lds\n"
"s_add_u32 s46, s46, 1\n"
"s_cmp_eq_u32 s46, 8\n"
"s_cselect_b32 s46, 0, s46\n"
"s_add_u32 s47, s47, 1\n"
"s_cmp_eq_u32 s47, 4\n"
"s_cselect_b32 s47, 0, s47\n"
"s_waitcnt vmcnt(16)\n"
"LY_w_b6:\n"
"ds_read_b64_tr_b16 a[192:193], v121 offset:16384\n"
"ds_read_b64_tr_b16 a[194:195], v123 offset:16384\n"
"ds_read_b64_tr_b16 a[196:197], v121 offset:20480\n"
"ds_read_b64_tr_b16 a[198:199], v123 offset:20480\n"
"ds_read_b64_tr_b16 a[200:201], v122 offset:16384\n"
"ds_read_b64_tr_b16 a[202:203], v124 offset:16384\n"
"ds_read_b64_tr_b16 a[204:205], v122 offset:20480\n"
"ds_read_b64_tr_b16 a[206:207], v124 offset:20480\n"
"s_waitcnt lgkmcnt(0)\n"
"ds_read_b64_tr_b16 a[208:209], v121 offset:17408\n"
"ds_read_b64_tr_b16 a[210:211], v123 offset:17408\n"
"ds_read_b64_tr_b16 a[212:213], v121 offset:21504\n"
"ds_read_b64_tr_b16 a[214:215], v123 offset:21504\n"
"ds_read_b64_tr_b16 a[216:217], v122 offset:17408\n"
"ds_read_b64_tr_b16 a[218:219], v124 offset:17408\n"
"ds_read_b64_tr_b16 a[220:221], v122 offset:21504\n"
"ds_read_b64_tr_b16 a[222:223], v124 offset:21504\n"
"s_waitcnt lgkmcnt(0)\n"
"s_mov_b32 s51, 6\n"
"s_cmp_eq_u32 s48, 6\n"
"s_cbranch_scc1 LY_yr\n"
"LY_yr6:\n"
"s_sub_u32 s48, s48, 1\n"
"s_cmp_eq_u32 s48, 0\n"
"s_cbranch_scc1 LY_w_end\n"
"LY_w_7:\n"
"s_cmp_gt_u32 s48, 2\n"
"s_cbranch_scc1 LY_w_a7\n"
"s_waitcnt vmcnt(0)\n"
"s_branch LY_w_b7\n"
"LY_w_a7:\n"
"s_xor_b32 s49, s46, %[sHX]\n"
"s_mul_i32 s49, s49, 147456\n"
"s_lshl_b32 s50, s47, 13\n"
"s_add_u32 s50, s50, %[sW32K]\n"
"s_add_u32 m0, s50, 0\n"
"s_add_u32 s40, s49, 0\n"
"buffer_load_dwordx4 v120, %[sWO], s40 offen  lds\n"
"s_add_u32 m0, s50, 1024\n"
"s_add_u32 s40, s49, 18432\n"
"buffer_load_dwordx4 v120, %[sWO], s40 offen  lds\n"
"s_add_u32 m0, s50, 2048\n"
"s_add_u32 s40, s49, 36864\n"
"buffer_load_dwordx4 v120, %[sWO], s40 offen  lds\n"
"s_add_u32 m0, s50, 3072\n"
"s_add_u32 s40, s49, 55296\n"
"buffer_load_dwordx4 v120, %[sWO], s40 offen  lds\n"
"s_add_u32 m0, s50, 4096\n"
"s_add_u32 s40, s49, 73728\n"
"buffer_load_dwordx4 v120, %[sWO], s40 offen  lds\n"
"s_add_u32 m0, s50, 5120\n"
"s_add_u32 s40, s49, 92160\n"
"buffer_load_dwordx4 v120, %[sWO], s40 offen  lds\n"
"s_add_u32 m0, s50, 6144\n"
"s_add_u32 s40, s49, 110592\n"
"buffer_load_dwordx4 v120, %[sWO], s40 offen  lds\n"
"s_add_u32 m0, s50, 7168\n"
"s_add_u32 s40, s49, 129024\n"
"buffer_load_dwordx4 v120, %[sWO], s40 offen  lds\n"
"s_add_u32 s46, s46, 1\n"
"s_cmp_eq_u32 s46, 8\n"
"s_cselect_b32 s46, 0, s46\n"
"s_add_u32 s47, s47, 1\n"
"s_cmp_eq_u32 s47, 4\n"
"s_cselect_b32 s47, 0, s47\n"
"s_waitcnt vmcnt(16)\n"
"LY_w_b7:\n"
"ds_read_b64_tr_b16 a[224:225], v121 offset:24576\n"
"ds_read_b64_tr_b16 a[226:227], v123 offset:24576\n"
"ds_read_b64_tr_b16 a[228:229], v121 offset:28672\n"
"ds_read_b64_tr_b16 a[230:231], v123 offset:28672\n"
"ds_read_b64_tr_b16 a[232:233], v122 offset:24576\n"
"ds_read_b64_tr_b16 a[234:235], v124 offset:24576\n"
"ds_read_b64_tr_b16 a[236:237], v122 offset:28672\n"
"ds_read_b64_tr_b16 a[238:239], v124 offset:28672\n"
"s_waitcnt lgkmcnt(0)\n"
"ds_read_b64_tr_b16 a[240:241], v121 offset:25600\n"
"ds_read_b64_tr_b16 a[242:243], v123 offset:25600\n"
"ds_read_b64_tr_b16 a[244:245], v121 offset:29696\n"
"ds_read_b64_tr_b16 a[246:247], v123 offset:29696\n"
"ds_read_b64_tr_b16 a[248:249], v122 offset:25600\n"
"ds_read_b64_tr_b16 a[250:251], v124 offset:25600\n"
"ds_read_b64_tr_b16 a[252:253], v122 offset:29696\n"
"ds_read_b64_tr_b16 a[254:255], v124 offset:29696\n"
"s_waitcnt lgkmcnt(0)\n"
"s_mov_b32 s51, 7\n"
"s_cmp_eq_u32 s48, 6\n"
"s_cbranch_scc1 LY_yr\n"
"LY_yr7:\n"
"s_sub_u32 s48, s48, 1\n"
"s_cmp_eq_u32 s48, 0\n"
"s_cbranch_scc1 LY_w_end\n"
"s_branch LY_w_0\n"
"LY_w_end:\n"
"s_barrier\n"
"s_mov_b32 s40, 1064960\n"
"v_mov_b32 v21, 0\n"
"s_mov_b64 exec, 1\n"
"s_mov_b32 s41, 0\n"
"LY_par:\n"
"buffer_load_dword v20, v21, s[32:35], s40 offen sc1\n"
"s_add_u32 s41, s41, 1\n"
"s_cmp_gt_u32 s41, 60000\n"
"s_cbranch_scc1 LY_tmo\n"
"s_waitcnt vmcnt(0)\n"
"v_cmp_gt_i32 vcc, 0, v20\n"
"s_cbranch_vccnz LY_par\n"
"LY_par_ok:\n"
"s_mov_b64 exec, -1\n"
"v_readfirstlane_b32 s42, v20\n"
"s_nop 3\n"
"s_and_b32 s54, %[sHB], 0x180\n"
"s_and_b32 s40, s42, 8192\n"
"s_add_u32 s40, s40, 89166336\n"
"s_bfe_u32 s45, s54, 0x10007\n"
"s_add_u32 s54, s54, s40\n"
"s_lshl_b32 s45, s45, 1\n"
"s_add_u32 s45, s45, 24\n"
"s_lshr_b32 s45, s42, s45\n"
"s_and_b32 s45, s45, 3\n"
"s_lshl_b32 s45, s45, 3\n"
"s_add_u32 s45, s45, %[sHX]\n"
"s_lshl_b32 s45, s45, 10\n"
"s_mov_b32 s84, 1120\n"
"s_mov_b32 s85, 1144832\n"
"s_mov_b32 s43, 0\n"
"v_mov_b32 v23, -1\n"
"v_bfe_u32 v20, %[vOL], 9, 1\n"
"v_lshl_add_u32 v20, v20, 1, 16\n"
"v_add_u32 v21, 4, v20\n"
"v_bfe_u32 v20, s42, v20, 2\n"
"v_bfe_u32 v21, s42, v21, 2\n"
"v_lshl_add_u32 v26, v20, 13, %[vMST]\n"
"v_lshl_add_u32 v27, v21, 13, %[vMST]\n"
"v_add_u32 v27, 524288, v27\n"
"s_branch LY_step\n"
".p2align 8\n"
"LY_step:\n"
"s_and_b32 s40, s43, 3\n"
"s_lshl_b32 s47, s40, 17\n"
"s_add_u32 s47, s47, s45\n"
"s_xor_b32 s48, s47, 4096\n"
"s_add_u32 s51, s43, 1\n"
"s_and_b32 s40, s51, 3\n"
"s_lshl_b32 s49, s40, 16\n"
"s_xor_b32 s50, s49, 131072\n"
"v_mov_b64 v[32:33], 0\n"
"v_mov_b64 v[34:35], 0\n"
"v_mov_b64 v[36:37], 0\n"
"v_mov_b64 v[38:39], 0\n"
"v_mov_b64 v[40:41], 0\n"
"v_mov_b64 v[42:43], 0\n"
"v_mov_b64 v[44:45], 0\n"
"v_mov_b64 v[46:47], 0\n"
"v_mov_b32 v20, 0\n"
"s_sleep 20\n"
"s_mov_b32 s41, 0\n"
"s_mov_b64 exec, 1\n"
"LY_ts:\n"
"buffer_load_dwordx2 v[12:13], off, s[32:35], s54 sc1\n"
"s_add_u32 s41, s41, 1\n"
"s_cmp_gt_u32 s41, 60000\n"
"s_cbranch_scc1 LY_tmo\n"
"s_waitcnt vmcnt(0)\n"
"v_cmp_gt_i32 vcc, s51, v12\n"
"s_cbranch_vccnz LY_ts\n"
"v_readfirstlane_b32 s92, v13\n"
"v_readfirstlane_b32 s93, v12\n"
"s_mov_b64 exec, -1\n"
"s_nop 1\n"
"s_cmp_eq_u32 s93, s51\n"
"s_cselect_b32 s93, 0, 0x10000\n"
"s_sub_u32 s92, s92, s93\n"
"s_add_u32 s92, s92, %[sDL]\n"
"LY_tw:\n"
"s_memrealtime s[90:91]\n"
"s_waitcnt lgkmcnt(0)\n"
"s_sub_u32 s40, s92, s90\n"
"s_cmp_gt_u32 s40, 2000\n"
"s_cbranch_scc1 LY_go\n"
"s_cmp_gt_u32 s40, 20\n"
"s_cbranch_scc1 LY_tw\n"
"s_sub_u32 s40, s40, 5\n"
"s_cmp_lt_i32 s40, 8\n"
"s_cbranch_scc1 LY_tf0\n"
"s_memrealtime s[94:95]\n"
"s_nop 4\n"
"s_memrealtime s[96:97]\n"
"s_nop 4\n"
"s_memrealtime s[98:99]\n"
"s_nop 4\n"
"s_memrealtime s[90:91]\n"
"s_lshl_b32 s40, s92, 2\n"
"s_waitcnt lgkmcnt(0)\n"
"s_sub_u32 s40, s40, s94\n"
"s_sub_u32 s40, s40, s96\n"
"s_sub_u32 s40, s40, s98\n"
"s_sub_u32 s40, s40, s90\n"
"s_ashr_i32 s40, s40, 2\n"
"s_sub_u32 s40, s40, 7\n"
"LY_tf0:\n"
"s_max_i32 s40, s40, 0\n"
"LY_tf:\n"
"s_sub_u32 s40, s40, 1\n"
"s_cbranch_scc0 LY_tf\n"
"LY_go:\n"
"s_mov_b32 s41, 0\n"
"LY_load:\n"
"buffer_load_dwordx4 v[64:67], %[vOL], s[32:35], s47 offen offset:0 sc1\n"
"s_nop 9\n"
"LY_load1:\n"
"buffer_load_dwordx4 v[68:71], %[vOL], s[32:35], s47 offen offset:1024 sc1\n"
"s_nop 9\n"
"LY_load2:\n"
"buffer_load_dwordx4 v[72:75], %[vOL], s[32:35], s47 offen offset:2048 sc1\n"
"s_nop 9\n"
"LY_load3:\n"
"buffer_load_dwordx4 v[76:79], %[vOL], s[32:35], s47 offen offset:3072 sc1\n"
"s_nop 9\n"
"LY_load4:\n"
"buffer_load_dwordx4 v[80:83], %[vOL], s[32:35], s48 offen offset:0 sc1\n"
"s_nop 9\n"
"LY_load5:\n"
"buffer_load_dwordx4 v[84:87], %[vOL], s[32:35], s48 offen offset:1024 sc1\n"
"s_nop 9\n"
"LY_load6:\n"
"buffer_load_dwordx4 v[88:91], %[vOL], s[32:35], s48 offen offset:2048 sc1\n"
"s_nop 9\n"
"LY_load7:\n"
"buffer_load_dwordx4 v[92:95], %[vOL], s[32:35], s48 offen offset:3072 sc1\n"
"s_waitcnt vmcnt(7)\n"
"v_smfmac_f32_16x16x64_bf16 v[32:35], v[64:67], a[0:7], %[vIDX]\n"
"v_smfmac_f32_16x16x64_bf16 v[36:39], v[64:67], a[8:15], %[vIDX]\n"
"v_smfmac_f32_16x16x64_bf16 v[40:43], v[64:67], a[16:23], %[vIDX]\n"
"v_smfmac_f32_16x16x64_bf16 v[44:47], v[64:67], a[24:31], %[vIDX]\n"
"s_waitcnt vmcnt(6)\n"
"v_smfmac_f32_16x16x64_bf16 v[32:35], v[68:71], a[32:39], %[vIDX]\n"
"v_smfmac_f32_16x16x64_bf16 v[36:39], v[68:71], a[40:47], %[vIDX]\n"
"v_smfmac_f32_16x16x64_bf16 v[40:43], v[68:71], a[48:55], %[vIDX]\n"
"v_smfmac_f32_16x16x64_bf16 v[44:47], v[68:71], a[56:63], %[vIDX]\n"
"s_waitcnt vmcnt(5)\n"
"v_smfmac_f32_16x16x64_bf16 v[32:35], v[72:75], a[64:71], %[vIDX]\n"
"v_max3_u32 v20, v20, v64, v65\n"
"v_smfmac_f32_16x16x64_bf16 v[36:39], v[72:75], a[72:79], %[vIDX]\n"
"v_max3_u32 v20, v20, v66, v67\n"
"v_smfmac_f32_16x16x64_bf16 v[40:43], v[72:75], a[80:87], %[vIDX]\n"
"v_max3_u32 v20, v20, v68, v69\n"
"v_smfmac_f32_16x16x64_bf16 v[44:47], v[72:75], a[88:95], %[vIDX]\n"
"s_waitcnt vmcnt(4)\n"
"v_max3_u32 v20, v20, v70, v71\n"
"v_smfmac_f32_16x16x64_bf16 v[32:35], v[76:79], a[96:103], %[vIDX]\n"
"v_cmp_eq_u32 vcc, -1, v20\n"
"v_smfmac_f32_16x16x64_bf16 v[36:39], v[76:79], a[104:111], %[vIDX]\n"
"v_max3_u32 v20, v20, v72, v73\n"
"v_smfmac_f32_16x16x64_bf16 v[40:43], v[76:79], a[112:119], %[vIDX]\n"
"s_cbranch_vccnz LY_again0\n"
"v_max3_u32 v20, v20, v74, v75\n"
"v_smfmac_f32_16x16x64_bf16 v[44:47], v[76:79], a[120:127], %[vIDX]\n"
"s_waitcnt vmcnt(3)\n"
"v_max3_u32 v20, v20, v76, v77\n"
"v_smfmac_f32_16x16x64_bf16 v[32:35], v[80:83], a[128:135], %[vIDX]\n"
"v_max3_u32 v20, v20, v78, v79\n"
"v_smfmac_f32_16x16x64_bf16 v[36:39], v[80:83], a[136:143], %[vIDX]\n"
"v_max3_u32 v20, v20, v80, v81\n"
"v_smfmac_f32_16x16x64_bf16 v[40:43], v[80:83], a[144:151], %[vIDX]\n"
"v_max3_u32 v20, v20, v82, v83\n"
"v_smfmac_f32_16x16x64_bf16 v[44:47], v[80:83], a[152:159], %[vIDX]\n"
"s_waitcnt vmcnt(2)\n"
"v_smfmac_f32_16x16x64_bf16 v[32:35], v[84:87], a[160:167], %[vIDX]\n"
"v_max3_u32 v20, v20, v84, v85\n"
"v_smfmac_f32_16x16x64_bf16 v[36:39], v[84:87], a[168:175], %[vIDX]\n"
"v_max3_u32 v20, v20, v86, v87\n"
"v_smfmac_f32_16x16x64_bf16 v[40:43], v[84:87], a[176:183], %[vIDX]\n"
"v_smfmac_f32_16x16x64_bf16 v[44:47], v[84:87], a[184:191], %[vIDX]\n"
"s_waitcnt vmcnt(1)\n"
"v_smfmac_f32_16x16x64_bf16 v[32:35], v[88:91], a[192:199], %[vIDX]\n"
"v_max3_u32 v20, v20, v88, v89\n"
"v_smfmac_f32_16x16x64_bf16 v[36:39], v[88:91], a[200:207], %[vIDX]\n"
"v_max3_u32 v20, v20, v90, v91\n"
"v_smfmac_f32_16x16x64_bf16 v[40:43], v[88:91], a[208:215], %[vIDX]\n"
"v_smfmac_f32_16x16x64_bf16 v[44:47], v[88:91], a[216:223], %[vIDX]\n"
"s_waitcnt vmcnt(0)\n"
"v_smfmac_f32_16x16x64_bf16 v[32:35], v[92:95], a[224:231], %[vIDX]\n"
"v_max3_u32 v20, v20, v92, v93\n"
"v_smfmac_f32_16x16x64_bf16 v[36:39], v[92:95], a[232:239], %[vIDX]\n"
"s_nop 1\n"
"v_max3_u32 v20, v20, v94, v95\n"
"v_smfmac_f32_16x16x64_bf16 v[40:43], v[92:95], a[240:247], %[vIDX]\n"
"s_nop 1\n"
"v_cmp_eq_u32 vcc, -1, v20\n"
"v_smfmac_f32_16x16x64_bf16 v[44:47], v[92:95], a[248:255], %[vIDX]\n"
"s_mov_b32 exec_hi, 0\n"
"v_pk_add_f32 v[48:49], v[32:33], v[34:35]\n"
"v_pk_add_f32 v[50:51], v[36:37], v[38:39]\n"
"s_not_b64 exec, exec\n"
"s_cbranch_vccnz LY_again2\n"
"v_pk_add_f32 v[48:49], v[40:41], v[42:43]\n"
"s_cmp_ge_u32 %[sW32K], 65536\n"
"s_nop 0\n"
"v_pk_add_f32 v[50:51], v[44:45], v[46:47]\n"
"s_mov_b64 exec, -1\n"
"ds_write2_b32 %[vRW], v48, v49 offset0:0 offset1:32\n"
"ds_write2_b32 %[vRW], v50, v51 offset0:128 offset1:160\n"
"s_cbranch_scc1 LY_other\n"
"s_waitcnt lgkmcnt(0)\n"
"s_barrier\n"
"ds_read_b128 v[96:99], %[vRR] offset:0\n"
"ds_read_b128 v[100:103], %[vRR] offset:1024\n"
"ds_read_b128 v[104:107], %[vRR] offset:2048\n"
"ds_read_b128 v[108:111], %[vRR] offset:3072\n"
"s_waitcnt lgkmcnt(2)\n"
"v_pk_add_f32 v[96:97], v[96:97], v[98:99]\n"
"v_pk_add_f32 v[100:101], v[100:101], v[102:103]\n"
"s_waitcnt lgkmcnt(0)\n"
"v_pk_add_f32 v[104:105], v[104:105], v[106:107]\n"
"v_pk_add_f32 v[96:97], v[96:97], v[100:101]\n"
"v_pk_add_f32 v[108:109], v[108:109], v[110:111]\n"
"v_pk_add_f32 v[96:97], v[96:97], v[104:105]\n"
"v_pk_add_f32 v[96:97], v[96:97], v[108:109]\n"
"v_cvt_pk_bf16_f32 v24, v96, v97\n"
"buffer_store_dword v24, v27, s[32:35], s49 offen sc1\n"
"buffer_store_dword v24, v26, s[32:35], s49 offen sc1\n"
"s_memrealtime s[98:99]\n"
"buffer_store_dword v23, v27, s[32:35], s50 offen sc1\n"
"buffer_store_dword v23, v26, s[32:35], s50 offen sc1\n"
"s_waitcnt lgkmcnt(0)\n"
"s_cmp_eq_u32 s43, 0\n"
"s_cselect_b32 s41, 1, s41\n"
"s_sub_u32 s96, s98, s92\n"
"s_lshl_b32 s96, s96, 4\n"
"s_sub_u32 s96, s96, s84\n"
"s_min_i32 s96, s96, 96\n"
"s_max_i32 s96, s96, -256\n"
"s_add_u32 s97, s96, 64\n"
"s_cmp_le_u32 s97, 64\n"
"s_cselect_b32 s97, 4, 1\n"
"s_ashr_i32 s96, s96, s97\n"
"s_cmp_eq_u32 s41, 0\n"
"s_cselect_b32 s96, s96, 0\n"
"s_add_u32 s84, s84, s96\n"
"v_mov_b32 v22, s84\n"
"buffer_store_dword v22, %[vRP], s[32:35], s85 offen sc1\n"
"LY_next:\n"
"s_add_u32 s43, s43, 1\n"
"s_cmp_lt_u32 s43, 63\n"
"s_cbranch_scc1 LY_step\n"
"s_branch LY_done\n"
"LY_other:\n"
"s_waitcnt lgkmcnt(0)\n"
"s_barrier\n"
"s_branch LY_next\n"
"LY_again0:\n"
"s_mov_b32 s93, 0\n"
"s_branch LY_again\n"
"LY_again2:\n"
"s_mov_b32 s93, 2\n"
"LY_again:\n"
"s_mov_b64 exec, -1\n"
"s_nop 2\n"
"s_add_u32 s41, s41, 1\n"
"s_cmp_gt_u32 s41, 0\n"
"s_cbranch_scc1 LY_retry\n"
"v_mov_b64 v[32:33], 0\n"
"v_mov_b64 v[34:35], 0\n"
"v_mov_b64 v[36:37], 0\n"
"v_mov_b64 v[38:39], 0\n"
"v_mov_b64 v[40:41], 0\n"
"v_mov_b64 v[42:43], 0\n"
"v_mov_b64 v[44:45], 0\n"
"v_mov_b64 v[46:47], 0\n"
"v_mov_b32 v20, 0\n"
"s_cmp_eq_u32 s93, 2\n"
"s_cbranch_scc1 LY_load2\n"
"s_branch LY_load\n"
"LY_retry:\n"
"v_and_b32 v52, 0x3f0, %[vOL]\n"
"v_and_b32 v53, 0xfffffc00, %[vOL]\n"
"v_lshl_add_u32 v52, v52, 3, v53\n"
"s_and_b32 s40, s47, 0xffffefff\n"
"s_nop 3\n"
"v_mov_b64 v[32:33], 0\n"
"v_mov_b64 v[34:35], 0\n"
"v_mov_b64 v[36:37], 0\n"
"v_mov_b64 v[38:39], 0\n"
"v_mov_b64 v[40:41], 0\n"
"v_mov_b64 v[42:43], 0\n"
"v_mov_b64 v[44:45], 0\n"
"v_mov_b64 v[46:47], 0\n"
"v_mov_b32 v20, 0\n"
"LY_hint:\n"
"buffer_load_dword v53, v52, s[32:35], s40 offen sc1\n"
"s_add_u32 s41, s41, 1\n"
"s_cmp_gt_u32 s41, 60000\n"
"s_cbranch_scc1 LY_tmo\n"
"s_waitcnt vmcnt(0)\n"
"v_cmp_eq_u32 vcc, -1, v53\n"
"s_memrealtime s[90:91]\n"
"s_cbranch_vccnz LY_hint\n"
"s_waitcnt lgkmcnt(0)\n"
"s_branch LY_load\n"
"LY_yr:\n"
"s_barrier\n"
"s_cmp_lg_u32 %[sW32K], 0\n"
"s_cbranch_scc1 LY_yrb\n"
"s_mov_b32 s41, 0\n"
"LY_hb:\n"
"buffer_load_dword v20, v26, s[32:35], %[sHB] offen sc1\n"
"s_add_u32 s41, s41, 1\n"
"s_cmp_gt_u32 s41, 60000\n"
"s_cbranch_scc1 LY_tmo\n"
"s_waitcnt vmcnt(0)\n"
"v_cmp_eq_u32 vcc, v20, v25\n"
"s_cbranch_vccnz LY_hb\n"
"v_mov_b32 v21, 0\n"
"s_mov_b32 s40, 1105920\n"
"buffer_store_dword v21, %[vYR], s[32:35], s40 offen sc1\n"
"LY_yrb:\n"
"s_cmp_eq_u32 s51, 0\n"
"s_cbranch_scc1 LY_yr0\n"
"s_cmp_eq_u32 s51, 1\n"
"s_cbranch_scc1 LY_yr1\n"
"s_cmp_eq_u32 s51, 2\n"
"s_cbranch_scc1 LY_yr2\n"
"s_cmp_eq_u32 s51, 3\n"
"s_cbranch_scc1 LY_yr3\n"
"s_cmp_eq_u32 s51, 4\n"
"s_cbranch_scc1 LY_yr4\n"
"s_cmp_eq_u32 s51, 5\n"
"s_cbranch_scc1 LY_yr5\n"
"s_cmp_eq_u32 s51, 6\n"
"s_cbranch_scc1 LY_yr6\n"
"s_branch LY_yr7\n"
"LY_tmo:\n"
"s_mov_b64 exec, 1\n"
"v_mov_b32 v20, 2\n"
"s_mov_b32 s40, 1065088\n"
"buffer_store_dword v20, off, s[32:35], s40 sc1\n"
"s_endpgm\n"
"LY_done:\n"
        : "+{s[32:35]}"(sWS), "+{v44}"(vGO), "+{v45}"(vT[0][0]), "+{v46}"(vT[0][1]), "+{v47}"(vT[1][0]), "+{v48}"(vT[1][1])
        : [sWO] "s"(sWO), [sM0] "s"(sM0), [sDL] "s"(rfl(((J >> 1) * STY) >> 2)), [sHX] "s"(rfl(J >= 9 ? HXV : 0)), [sROT] "s"(rfl(((J & 1) * 0 + rg * 4) ^ (J >= 9 ? HXV : 0))), [sW32K] "s"(sW32K), [sHB] "s"(hb_off()),
          [vIDX] "v"(vIDX), [vOL] "v"(vOL), [vRW] "v"(vRW), [vRR] "v"(vRR), [vMST] "v"(vMST), [vMA] "v"(vMA), [vYR] "v"(vYR), [vRP] "v"(vRP), [vM0] "v"(vM0)
        : "v12", "v13", "v19", "v20", "v21", "v22", "v23", "v24", "v25", "v26", "v27", "v28", "v29", "v30", "v31", "v32", "v33", "v34", "v35", "v36", "v37", "v38", "v39", "v40",
"v41", "v42", "v43", "v49", "v50", "v51", "v52", "v53", "v54", "v55", "v56", "v57", "v58", "v59", "v60", "v61", "v62", "v63", "v64", "v65", "v66", "v67", "v68", "v69",
"v70", "v71", "v72", "v73", "v74", "v75", "v76", "v77", "v78", "v79", "v80", "v81", "v82", "v83", "v84", "v85", "v86", "v87", "v88", "v89", "v90", "v91", "v92", "v93",
"v94", "v95", "v96", "v97", "v98", "v99", "v100", "v101", "v102", "v103", "v104", "v105", "v106", "v107", "v108", "v109", "v110", "v111", "v112", "v113", "v114", "v115", "v116", "v117",
"v118", "v119", "v120", "v121", "v122", "v123", "v124", "v125", "v126", "v127", "v128", "v129", "v130", "v131", "v132", "v133", "v134", "v135", "v136", "v137", "v138", "v139", "v140", "v141",
"v142", "v143", "v144", "v145", "v146", "v147", "v148", "v149", "v150", "v151", "v152", "v153", "v154", "v155", "v156", "v157", "v158", "v159", "v160", "v161", "v162", "v163", "v164", "v165",
"v166", "v167", "v168", "v169", "v170", "v171", "v172", "v173", "v174", "v175", "v176", "v177", "v178", "v179", "v180", "v181", "v182", "v183", "v184", "v185", "v186", "v187", "v188", "v189",
"v190", "v191", "v192", "v193", "v194", "v195", "v196", "v197", "v198", "v199", "v200", "v201", "v202", "v203", "v204", "v205", "v206", "v207", "v208", "v209", "v210", "v211", "v212", "v213",
"v214", "v215", "v216", "v217", "v218", "v219", "v220", "v221", "v222", "v223", "v224", "v225", "v226", "v227", "v228", "v229", "v230", "v231", "v232", "v233", "v234", "v235", "v236", "v237",
"v238", "v239", "v240", "v241", "v242", "v243", "v244", "v245", "v246", "v247", "v248", "v249", "v250", "v251", "v252", "v253", "v254", "v255", "a0", "a1", "a2", "a3", "a4", "a5",
"a6", "a7", "a8", "a9", "a10", "a11", "a12", "a13", "a14", "a15", "a16", "a17", "a18", "a19", "a20", "a21", "a22", "a23", "a24", "a25", "a26", "a27", "a28", "a29",
"a30", "a31", "a32", "a33", "a34", "a35", "a36", "a37", "a38", "a39", "a40", "a41", "a42", "a43", "a44", "a45", "a46", "a47", "a48", "a49", "a50", "a51", "a52", "a53",
"a54", "a55", "a56", "a57", "a58", "a59", "a60", "a61", "a62", "a63", "a64", "a65", "a66", "a67", "a68", "a69", "a70", "a71", "a72", "a73", "a74", "a75", "a76", "a77",
"a78", "a79", "a80", "a81", "a82", "a83", "a84", "a85", "a86", "a87", "a88", "a89", "a90", "a91", "a92", "a93", "a94", "a95", "a96", "a97", "a98", "a99", "a100", "a101",
"a102", "a103", "a104", "a105", "a106", "a107", "a108", "a109", "a110", "a111", "a112", "a113", "a114", "a115", "a116", "a117", "a118", "a119", "a120", "a121", "a122", "a123", "a124", "a125",
"a126", "a127", "a128", "a129", "a130", "a131", "a132", "a133", "a134", "a135", "a136", "a137", "a138", "a139", "a140", "a141", "a142", "a143", "a144", "a145", "a146", "a147", "a148", "a149",
"a150", "a151", "a152", "a153", "a154", "a155", "a156", "a157", "a158", "a159", "a160", "a161", "a162", "a163", "a164", "a165", "a166", "a167", "a168", "a169", "a170", "a171", "a172", "a173",
"a174", "a175", "a176", "a177", "a178", "a179", "a180", "a181", "a182", "a183", "a184", "a185", "a186", "a187", "a188", "a189", "a190", "a191", "a192", "a193", "a194", "a195", "a196", "a197",
"a198", "a199", "a200", "a201", "a202", "a203", "a204", "a205", "a206", "a207", "a208", "a209", "a210", "a211", "a212", "a213", "a214", "a215", "a216", "a217", "a218", "a219", "a220", "a221",
"a222", "a223", "a224", "a225", "a226", "a227", "a228", "a229", "a230", "a231", "a232", "a233", "a234", "a235", "a236", "a237", "a238", "a239", "a240", "a241", "a242", "a243", "a244", "a245",
"a246", "a247", "a248", "a249", "a250", "a251", "a252", "a253", "a254", "a255", "s40", "s41", "s42", "s43", "s44", "s45", "s46", "s47", "s48", "s49", "s50", "s51", "s52", "s53",
"s54", "s55", "s56", "s57", "s58", "s59", "s60", "s61", "s62", "s63", "s64", "s65", "s66", "s67", "s68", "s69", "s70", "s71", "s72", "s73", "s74", "s75", "s76", "s77",
"s78", "s79", "s80", "s81", "s82", "s83", "s84", "s85", "s86", "s87", "s88", "s89", "s90", "s91", "s92", "s93", "s94", "s95", "s96", "s97", "s98", "s99", "vcc", "scc",
"memory", "s31");
}

constexpr unsigned BW_XN = 0, BW_HF = 3u << 20, BW_B = 6u << 20;
constexpr int TS0 = 0, TS1 = 1, FSPLIT = 0, K1J = 1, NPRE = 0, K0J = 1, WTX = 8192, PRE_SLEEP = 60, T1_SLEEP = 50, T1_N = 0;
typedef unsigned v4u __attribute__((ext_vector_type(4)));
typedef unsigned v2u __attribute__((ext_vector_type(2)));
__device__ __forceinline__ unsigned pk_bf16(float lo, float hi) { unsigned r; asm("v_cvt_pk_bf16_f32 %0, %1, %2" : "=v"(r) : "v"(lo), "v"(hi)); return r; }
template <int CTRL> __device__ __forceinline__ float dpp_add(float x) {
    return x + __builtin_bit_cast(float, __builtin_amdgcn_update_dpp(0, __builtin_bit_cast(int, x), CTRL, 0xf, 0xf, true));
}
__device__ __forceinline__ float sum32(float x) {
    x = dpp_add<0xB1>(x); x = dpp_add<0x4E>(x); x = dpp_add<0x124>(x); x = dpp_add<0x128>(x);
    const unsigned u = __float_as_uint(x);
    const auto r = __builtin_amdgcn_permlane16_swap(u, u, false, false);
    return __uint_as_float(r[0]) + __uint_as_float(r[1]);
}

__device__ __forceinline__ unsigned ld_dev(const char* p) { return __hip_atomic_load((const unsigned*)p, __ATOMIC_RELAXED, __HIP_MEMORY_SCOPE_AGENT); }
__device__ __forceinline__ void st_dev(char* p, unsigned v) { __hip_atomic_store((unsigned*)p, v, __ATOMIC_RELAXED, __HIP_MEMORY_SCOPE_AGENT); }
__device__ __forceinline__ unsigned hb_read(char* ws) { return ld_dev(ws + hb_off() + min(threadIdx.x & 63, 2u) * 1024); }
__device__ __forceinline__ bool hb_wait(char* ws, unsigned h0) {
    for (int i = 0; i < 60000; ++i) if (__all(hb_read(ws) != h0)) return true;
    return false;
}

__device__ __forceinline__ void ln_groups(char* ws, const void* x, int blk0, int stride, int N) {
    extern __shared__ __attribute__((aligned(16))) char lds[];
    const int tid = threadIdx.x, tok = tid >> 5, p = tid & 31;
    const __amdgpu_buffer_rsrc_t rb = __builtin_amdgcn_make_buffer_rsrc(ws + WS_BW, 0, BW_B, 0x00020000);
    const __amdgpu_buffer_rsrc_t rx = __builtin_amdgcn_make_buffer_rsrc((void*)x, 0, 2359296, 0x00020000);
    unsigned nx[9];
#pragma unroll
    for (int i = 0; i < 9; ++i) nx[i] = __builtin_amdgcn_raw_buffer_load_b32(rx, (blk0 * 8 + tok) * 1152 + p * 4 + 128 * i, 0, 0);
#pragma unroll
    for (int i = 0; i < 9; ++i) asm volatile("" : "+v"(nx[i]));
#pragma unroll 1
    for (int n = 0; n < N; ++n) {
        const int blk = blk0 + n * stride, seg = blk >> 2, oct = blk & 3, lo_ = (n & 1) * 18688;
        char* L = lds + lo_;
        unsigned raw[9];
#pragma unroll
        for (int i = 0; i < 9; ++i) { raw[i] = nx[i]; asm volatile("" : "+v"(raw[i])); }
        const unsigned nxt = n + 1 < N ? (unsigned)(((blk + stride) * 8 + tok) * 1152 + p * 4) : OOB;
#pragma unroll
        for (int i = 0; i < 9; ++i) nx[i] = __builtin_amdgcn_raw_buffer_load_b32(rx, nxt + 128 * i, 0, 0);
#pragma unroll
        for (int pass = 0; pass < 2; ++pass) {
            float v[18], s = 0.f, q = 0.f;
#pragma unroll
            for (int i = 0; i < 9; ++i) { v[2 * i] = __uint_as_float(raw[i] << 16); v[2 * i + 1] = __uint_as_float(raw[i] & 0xffff0000u); s += v[2 * i] + v[2 * i + 1]; }
            const float m = sum32(s) * (1.f / 576.f);
#pragma unroll
            for (int i = 0; i < 18; ++i) { v[i] -= m; q += v[i] * v[i]; }
            const float rs = __builtin_amdgcn_rsqf(sum32(q) * (1.f / 576.f) + 1e-5f);
#pragma unroll
            for (int i = 0; i < 9; ++i) {
                raw[i] = pk_bf16(v[2 * i] * rs, v[2 * i + 1] * rs);
                *(unsigned*)(L + pass * 9344 + tok * 1168 + (p + 32 * i) * 4) = raw[i];
            }
        }
        asm volatile("s_waitcnt vmcnt(0) lgkmcnt(0)\n\ts_barrier" : "+v"(nx[0]), "+v"(nx[1]), "+v"(nx[2]), "+v"(nx[3]), "+v"(nx[4]), "+v"(nx[5]), "+v"(nx[6]), "+v"(nx[7]), "+v"(nx[8]) :: "memory");
        const unsigned xn = BW_XN + seg * 36864 + (oct >> 1) * 18432 + (oct & 1) * 128, hf = BW_HF + seg * 36864 + oct * 256;
        v4u a[3]; v2u lo[3], hi[3]; unsigned ad[3], at[3];
#pragma unroll
        for (int i = 0; i < 3; ++i) {
            const int u = min(tid + 256 * i, 560 + (tid & 15)), c = u & 15, dt = u >> 4;
            ad[i] = lo_ + (u & 7) * 1168 + (u >> 3) * 16;
            at[i] = lo_ + 9344 + (c >> 2) * 1168 + (c & 3) * 16 + (dt >> 1) * 64 + (dt & 1) * 8;
        }
        asm volatile("ds_read_b128 %0, %9\n\tds_read_b128 %1, %10\n\tds_read_b128 %2, %11\n\t"
                     "ds_read_b64_tr_b16 %3, %12\n\tds_read_b64_tr_b16 %6, %12 offset:4672\n\tds_read_b64_tr_b16 %4, %13\n\tds_read_b64_tr_b16 %7, %13 offset:4672\n\t"
                     "ds_read_b64_tr_b16 %5, %14\n\tds_read_b64_tr_b16 %8, %14 offset:4672\n\ts_waitcnt lgkmcnt(0)"
                     : "=&v"(a[0]), "=&v"(a[1]), "=&v"(a[2]), "=&v"(lo[0]), "=&v"(lo[1]), "=&v"(lo[2]), "=&v"(hi[0]), "=&v"(hi[1]), "=&v"(hi[2])
                     : "v"(ad[0]), "v"(ad[1]), "v"(ad[2]), "v"(at[0]), "v"(at[1]), "v"(at[2]) : "memory");
#pragma unroll
        for (int i = 0; i < 3; ++i) {
            const int u = tid + 256 * i;
            if (i < 2 || tid < 64) {
                __builtin_amdgcn_raw_buffer_store_b128(a[i], rb, xn + (u >> 3) * 256 + (u & 7) * 16, 0, 16);
                __builtin_amdgcn_raw_buffer_store_b128((v4u){lo[i].x, lo[i].y, hi[i].x, hi[i].y}, rb, hf + (u >> 4) * 1024 + (u & 15) * 16, 0, 16);
            }
        }
    }
    asm volatile("s_waitcnt vmcnt(0)\n\ts_barrier" ::: "memory");
}

__device__ __forceinline__ int wt_par(char* ws) {
    int par = 0;
    for (int k = 0; k < 60000; ++k) { par = rfl(ld_dev(ws + 1065984)); if (par >= 0) break; __builtin_amdgcn_s_sleep(8); }
    return par;
}

__device__ __forceinline__ void role_qkv(char* ws, const void* wsel, int q) {
    int tid = threadIdx.x; asm volatile("" : "+v"(tid));
    const int w = rfl(tid >> 6), l = tid & 63, g = l >> 4, c = l & 15, x = c >> 2;
    const int mtx = q >> 4, hp = q & 15;
    const v4i sWS = make_srd(ws, WS_B), sW = make_srd(wsel, 576 * 8192);
    const unsigned gb = (l >> 3) * 8192 + 512 * hp + 128 * w;
    const unsigned vG0 = gb + ((l & 7) ^ ((g & 1) << 1)) * 16, vG1 = gb + ((l & 7) ^ (((g & 1) | 2) << 1)) * 16;
    const unsigned vT = 36864 + w * 24576 + (8 * g + x) * 128 + ((x >> 1) | ((g & 1) << 1)) * 32 + (c & 3) * 8;
    const unsigned vL16 = l * 16, vFL = l == 0 ? (unsigned)((q * 4 + w) * 128) : OOB;
    const int sWST = 36864 + w * 24576, sW9K = w * 9216, sISV = rfl(mtx == 2);
    const int sOUT = rfl(WS_QKV + (2 * hp + (w >> 1)) * 24576 + mtx * 8192 + (w & 1) * (mtx == 2 ? 4096 : 2048));
    asm volatile(
"LQ_top:\n"
"s_nop 4\n"
"v_mov_b32 v44, %[vG0]\n"
"v_mov_b32 v45, %[vG1]\n"
"v_mov_b32 v46, %[vT]\n"
"v_mbcnt_lo_u32_b32 v51, -1, 0\n"
"v_min_u32 v51, 2, v51\n"
"v_lshlrev_b32 v51, 10, v51\n"
"buffer_load_dword v50, v51, %[sWS], %[sHB] offen sc1\n"
"v_xor_b32 v47, 32, v46\n"
"v_xor_b32 v48, 64, v46\n"
"v_xor_b32 v49, 96, v46\n"
"v_mbcnt_lo_u32_b32 v54, -1, 0\n"
"v_mbcnt_hi_u32_b32 v54, -1, v54\n"
"v_lshlrev_b32 v54, 7, v54\n"
"v_min_u32 v54, 3968, v54\n"
"s_add_u32 s52, %[sW9K], 71303168\n"
"s_mov_b32 s53, %[sW9K]\n"
"s_mov_b32 s40, 0\n"
"s_add_u32 m0, %[sWST], 0\n"
"s_nop 0\n"
"buffer_load_dwordx4 v44, %[sW], s40 offen lds\n"
"s_mov_b32 s40, 65536\n"
"s_add_u32 m0, %[sWST], 1024\n"
"s_nop 0\n"
"buffer_load_dwordx4 v45, %[sW], s40 offen lds\n"
"s_mov_b32 s40, 131072\n"
"s_add_u32 m0, %[sWST], 2048\n"
"s_nop 0\n"
"buffer_load_dwordx4 v44, %[sW], s40 offen lds\n"
"s_mov_b32 s40, 196608\n"
"s_add_u32 m0, %[sWST], 3072\n"
"s_nop 0\n"
"buffer_load_dwordx4 v45, %[sW], s40 offen lds\n"
"s_mov_b32 s40, 262144\n"
"s_add_u32 m0, %[sWST], 4096\n"
"s_nop 0\n"
"buffer_load_dwordx4 v44, %[sW], s40 offen lds\n"
"s_mov_b32 s40, 327680\n"
"s_add_u32 m0, %[sWST], 5120\n"
"s_nop 0\n"
"buffer_load_dwordx4 v45, %[sW], s40 offen lds\n"
"s_mov_b32 s40, 393216\n"
"s_add_u32 m0, %[sWST], 6144\n"
"s_nop 0\n"
"buffer_load_dwordx4 v44, %[sW], s40 offen lds\n"
"s_mov_b32 s40, 458752\n"
"s_add_u32 m0, %[sWST], 7168\n"
"s_nop 0\n"
"buffer_load_dwordx4 v45, %[sW], s40 offen lds\n"
"s_mov_b32 s40, 524288\n"
"s_add_u32 m0, %[sWST], 8192\n"
"s_nop 0\n"
"buffer_load_dwordx4 v44, %[sW], s40 offen lds\n"
"s_mov_b32 s40, 589824\n"
"s_add_u32 m0, %[sWST], 9216\n"
"s_nop 0\n"
"buffer_load_dwordx4 v45, %[sW], s40 offen lds\n"
"s_mov_b32 s40, 655360\n"
"s_add_u32 m0, %[sWST], 10240\n"
"s_nop 0\n"
"buffer_load_dwordx4 v44, %[sW], s40 offen lds\n"
"s_mov_b32 s40, 720896\n"
"s_add_u32 m0, %[sWST], 11264\n"
"s_nop 0\n"
"buffer_load_dwordx4 v45, %[sW], s40 offen lds\n"
"s_mov_b32 s40, 786432\n"
"s_add_u32 m0, %[sWST], 12288\n"
"s_nop 0\n"
"buffer_load_dwordx4 v44, %[sW], s40 offen lds\n"
"s_mov_b32 s40, 851968\n"
"s_add_u32 m0, %[sWST], 13312\n"
"s_nop 0\n"
"buffer_load_dwordx4 v45, %[sW], s40 offen lds\n"
"s_mov_b32 s40, 917504\n"
"s_add_u32 m0, %[sWST], 14336\n"
"s_nop 0\n"
"buffer_load_dwordx4 v44, %[sW], s40 offen lds\n"
"s_mov_b32 s40, 983040\n"
"s_add_u32 m0, %[sWST], 15360\n"
"s_nop 0\n"
"buffer_load_dwordx4 v45, %[sW], s40 offen lds\n"
"s_mov_b32 s40, 1048576\n"
"s_add_u32 m0, %[sWST], 16384\n"
"s_nop 0\n"
"buffer_load_dwordx4 v44, %[sW], s40 offen lds\n"
"s_mov_b32 s40, 1114112\n"
"s_add_u32 m0, %[sWST], 17408\n"
"s_nop 0\n"
"buffer_load_dwordx4 v45, %[sW], s40 offen lds\n"
"s_mov_b32 s40, 1179648\n"
"s_add_u32 m0, %[sWST], 18432\n"
"s_nop 0\n"
"buffer_load_dwordx4 v44, %[sW], s40 offen lds\n"
"s_mov_b32 s40, 1245184\n"
"s_add_u32 m0, %[sWST], 19456\n"
"s_nop 0\n"
"buffer_load_dwordx4 v45, %[sW], s40 offen lds\n"
"s_mov_b32 s40, 1310720\n"
"s_add_u32 m0, %[sWST], 20480\n"
"s_nop 0\n"
"buffer_load_dwordx4 v44, %[sW], s40 offen lds\n"
"s_mov_b32 s40, 1376256\n"
"s_add_u32 m0, %[sWST], 21504\n"
"s_nop 0\n"
"buffer_load_dwordx4 v45, %[sW], s40 offen lds\n"
"s_mov_b32 s40, 1441792\n"
"s_add_u32 m0, %[sWST], 22528\n"
"s_nop 0\n"
"buffer_load_dwordx4 v44, %[sW], s40 offen lds\n"
"s_mov_b32 s40, 1507328\n"
"s_add_u32 m0, %[sWST], 23552\n"
"s_nop 0\n"
"buffer_load_dwordx4 v45, %[sW], s40 offen lds\n"
"s_waitcnt vmcnt(16)\n"
"ds_read_b64_tr_b16 a[0:1], v46 offset:0\n"
"ds_read_b64_tr_b16 a[2:3], v46 offset:512\n"
"ds_read_b64_tr_b16 a[4:5], v47 offset:0\n"
"ds_read_b64_tr_b16 a[6:7], v47 offset:512\n"
"ds_read_b64_tr_b16 a[8:9], v48 offset:0\n"
"ds_read_b64_tr_b16 a[10:11], v48 offset:512\n"
"ds_read_b64_tr_b16 a[12:13], v49 offset:0\n"
"ds_read_b64_tr_b16 a[14:15], v49 offset:512\n"
"s_waitcnt lgkmcnt(0)\n"
"ds_read_b64_tr_b16 a[16:17], v46 offset:4096\n"
"ds_read_b64_tr_b16 a[18:19], v46 offset:4608\n"
"ds_read_b64_tr_b16 a[20:21], v47 offset:4096\n"
"ds_read_b64_tr_b16 a[22:23], v47 offset:4608\n"
"ds_read_b64_tr_b16 a[24:25], v48 offset:4096\n"
"ds_read_b64_tr_b16 a[26:27], v48 offset:4608\n"
"ds_read_b64_tr_b16 a[28:29], v49 offset:4096\n"
"ds_read_b64_tr_b16 a[30:31], v49 offset:4608\n"
"s_waitcnt lgkmcnt(0)\n"
"s_mov_b32 s40, 1572864\n"
"s_add_u32 m0, %[sWST], 0\n"
"s_nop 0\n"
"buffer_load_dwordx4 v44, %[sW], s40 offen lds\n"
"s_mov_b32 s40, 1638400\n"
"s_add_u32 m0, %[sWST], 1024\n"
"s_nop 0\n"
"buffer_load_dwordx4 v45, %[sW], s40 offen lds\n"
"s_mov_b32 s40, 1703936\n"
"s_add_u32 m0, %[sWST], 2048\n"
"s_nop 0\n"
"buffer_load_dwordx4 v44, %[sW], s40 offen lds\n"
"s_mov_b32 s40, 1769472\n"
"s_add_u32 m0, %[sWST], 3072\n"
"s_nop 0\n"
"buffer_load_dwordx4 v45, %[sW], s40 offen lds\n"
"s_mov_b32 s40, 1835008\n"
"s_add_u32 m0, %[sWST], 4096\n"
"s_nop 0\n"
"buffer_load_dwordx4 v44, %[sW], s40 offen lds\n"
"s_mov_b32 s40, 1900544\n"
"s_add_u32 m0, %[sWST], 5120\n"
"s_nop 0\n"
"buffer_load_dwordx4 v45, %[sW], s40 offen lds\n"
"s_mov_b32 s40, 1966080\n"
"s_add_u32 m0, %[sWST], 6144\n"
"s_nop 0\n"
"buffer_load_dwordx4 v44, %[sW], s40 offen lds\n"
"s_mov_b32 s40, 2031616\n"
"s_add_u32 m0, %[sWST], 7168\n"
"s_nop 0\n"
"buffer_load_dwordx4 v45, %[sW], s40 offen lds\n"
"s_waitcnt vmcnt(16)\n"
"ds_read_b64_tr_b16 a[32:33], v46 offset:8192\n"
"ds_read_b64_tr_b16 a[34:35], v46 offset:8704\n"
"ds_read_b64_tr_b16 a[36:37], v47 offset:8192\n"
"ds_read_b64_tr_b16 a[38:39], v47 offset:8704\n"
"ds_read_b64_tr_b16 a[40:41], v48 offset:8192\n"
"ds_read_b64_tr_b16 a[42:43], v48 offset:8704\n"
"ds_read_b64_tr_b16 a[44:45], v49 offset:8192\n"
"ds_read_b64_tr_b16 a[46:47], v49 offset:8704\n"
"s_waitcnt lgkmcnt(0)\n"
"ds_read_b64_tr_b16 a[48:49], v46 offset:12288\n"
"ds_read_b64_tr_b16 a[50:51], v46 offset:12800\n"
"ds_read_b64_tr_b16 a[52:53], v47 offset:12288\n"
"ds_read_b64_tr_b16 a[54:55], v47 offset:12800\n"
"ds_read_b64_tr_b16 a[56:57], v48 offset:12288\n"
"ds_read_b64_tr_b16 a[58:59], v48 offset:12800\n"
"ds_read_b64_tr_b16 a[60:61], v49 offset:12288\n"
"ds_read_b64_tr_b16 a[62:63], v49 offset:12800\n"
"s_waitcnt lgkmcnt(0)\n"
"s_mov_b32 s40, 2097152\n"
"s_add_u32 m0, %[sWST], 8192\n"
"s_nop 0\n"
"buffer_load_dwordx4 v44, %[sW], s40 offen lds\n"
"s_mov_b32 s40, 2162688\n"
"s_add_u32 m0, %[sWST], 9216\n"
"s_nop 0\n"
"buffer_load_dwordx4 v45, %[sW], s40 offen lds\n"
"s_mov_b32 s40, 2228224\n"
"s_add_u32 m0, %[sWST], 10240\n"
"s_nop 0\n"
"buffer_load_dwordx4 v44, %[sW], s40 offen lds\n"
"s_mov_b32 s40, 2293760\n"
"s_add_u32 m0, %[sWST], 11264\n"
"s_nop 0\n"
"buffer_load_dwordx4 v45, %[sW], s40 offen lds\n"
"s_mov_b32 s40, 2359296\n"
"s_add_u32 m0, %[sWST], 12288\n"
"s_nop 0\n"
"buffer_load_dwordx4 v44, %[sW], s40 offen lds\n"
"s_mov_b32 s40, 2424832\n"
"s_add_u32 m0, %[sWST], 13312\n"
"s_nop 0\n"
"buffer_load_dwordx4 v45, %[sW], s40 offen lds\n"
"s_mov_b32 s40, 2490368\n"
"s_add_u32 m0, %[sWST], 14336\n"
"s_nop 0\n"
"buffer_load_dwordx4 v44, %[sW], s40 offen lds\n"
"s_mov_b32 s40, 2555904\n"
"s_add_u32 m0, %[sWST], 15360\n"
"s_nop 0\n"
"buffer_load_dwordx4 v45, %[sW], s40 offen lds\n"
"s_waitcnt vmcnt(16)\n"
"ds_read_b64_tr_b16 a[64:65], v46 offset:16384\n"
"ds_read_b64_tr_b16 a[66:67], v46 offset:16896\n"
"ds_read_b64_tr_b16 a[68:69], v47 offset:16384\n"
"ds_read_b64_tr_b16 a[70:71], v47 offset:16896\n"
"ds_read_b64_tr_b16 a[72:73], v48 offset:16384\n"
"ds_read_b64_tr_b16 a[74:75], v48 offset:16896\n"
"ds_read_b64_tr_b16 a[76:77], v49 offset:16384\n"
"ds_read_b64_tr_b16 a[78:79], v49 offset:16896\n"
"s_waitcnt lgkmcnt(0)\n"
"ds_read_b64_tr_b16 a[80:81], v46 offset:20480\n"
"ds_read_b64_tr_b16 a[82:83], v46 offset:20992\n"
"ds_read_b64_tr_b16 a[84:85], v47 offset:20480\n"
"ds_read_b64_tr_b16 a[86:87], v47 offset:20992\n"
"ds_read_b64_tr_b16 a[88:89], v48 offset:20480\n"
"ds_read_b64_tr_b16 a[90:91], v48 offset:20992\n"
"ds_read_b64_tr_b16 a[92:93], v49 offset:20480\n"
"ds_read_b64_tr_b16 a[94:95], v49 offset:20992\n"
"s_waitcnt lgkmcnt(0)\n"
"s_mov_b32 s40, 2621440\n"
"s_add_u32 m0, %[sWST], 16384\n"
"s_nop 0\n"
"buffer_load_dwordx4 v44, %[sW], s40 offen lds\n"
"s_mov_b32 s40, 2686976\n"
"s_add_u32 m0, %[sWST], 17408\n"
"s_nop 0\n"
"buffer_load_dwordx4 v45, %[sW], s40 offen lds\n"
"s_mov_b32 s40, 2752512\n"
"s_add_u32 m0, %[sWST], 18432\n"
"s_nop 0\n"
"buffer_load_dwordx4 v44, %[sW], s40 offen lds\n"
"s_mov_b32 s40, 2818048\n"
"s_add_u32 m0, %[sWST], 19456\n"
"s_nop 0\n"
"buffer_load_dwordx4 v45, %[sW], s40 offen lds\n"
"s_mov_b32 s40, 2883584\n"
"s_add_u32 m0, %[sWST], 20480\n"
"s_nop 0\n"
"buffer_load_dwordx4 v44, %[sW], s40 offen lds\n"
"s_mov_b32 s40, 2949120\n"
"s_add_u32 m0, %[sWST], 21504\n"
"s_nop 0\n"
"buffer_load_dwordx4 v45, %[sW], s40 offen lds\n"
"s_mov_b32 s40, 3014656\n"
"s_add_u32 m0, %[sWST], 22528\n"
"s_nop 0\n"
"buffer_load_dwordx4 v44, %[sW], s40 offen lds\n"
"s_mov_b32 s40, 3080192\n"
"s_add_u32 m0, %[sWST], 23552\n"
"s_nop 0\n"
"buffer_load_dwordx4 v45, %[sW], s40 offen lds\n"
"s_waitcnt vmcnt(16)\n"
"ds_read_b64_tr_b16 a[96:97], v46 offset:0\n"
"ds_read_b64_tr_b16 a[98:99], v46 offset:512\n"
"ds_read_b64_tr_b16 a[100:101], v47 offset:0\n"
"ds_read_b64_tr_b16 a[102:103], v47 offset:512\n"
"ds_read_b64_tr_b16 a[104:105], v48 offset:0\n"
"ds_read_b64_tr_b16 a[106:107], v48 offset:512\n"
"ds_read_b64_tr_b16 a[108:109], v49 offset:0\n"
"ds_read_b64_tr_b16 a[110:111], v49 offset:512\n"
"s_waitcnt lgkmcnt(0)\n"
"ds_read_b64_tr_b16 a[112:113], v46 offset:4096\n"
"ds_read_b64_tr_b16 a[114:115], v46 offset:4608\n"
"ds_read_b64_tr_b16 a[116:117], v47 offset:4096\n"
"ds_read_b64_tr_b16 a[118:119], v47 offset:4608\n"
"ds_read_b64_tr_b16 a[120:121], v48 offset:4096\n"
"ds_read_b64_tr_b16 a[122:123], v48 offset:4608\n"
"ds_read_b64_tr_b16 a[124:125], v49 offset:4096\n"
"ds_read_b64_tr_b16 a[126:127], v49 offset:4608\n"
"s_waitcnt lgkmcnt(0)\n"
"buffer_load_dword v52, v51, %[sWS], %[sHB] offen sc1\n"
"s_mov_b32 s40, 3145728\n"
"s_add_u32 m0, %[sWST], 0\n"
"s_nop 0\n"
"buffer_load_dwordx4 v44, %[sW], s40 offen lds\n"
"s_mov_b32 s40, 3211264\n"
"s_add_u32 m0, %[sWST], 1024\n"
"s_nop 0\n"
"buffer_load_dwordx4 v45, %[sW], s40 offen lds\n"
"s_mov_b32 s40, 3276800\n"
"s_add_u32 m0, %[sWST], 2048\n"
"s_nop 0\n"
"buffer_load_dwordx4 v44, %[sW], s40 offen lds\n"
"s_mov_b32 s40, 3342336\n"
"s_add_u32 m0, %[sWST], 3072\n"
"s_nop 0\n"
"buffer_load_dwordx4 v45, %[sW], s40 offen lds\n"
"s_mov_b32 s40, 3407872\n"
"s_add_u32 m0, %[sWST], 4096\n"
"s_nop 0\n"
"buffer_load_dwordx4 v44, %[sW], s40 offen lds\n"
"s_mov_b32 s40, 3473408\n"
"s_add_u32 m0, %[sWST], 5120\n"
"s_nop 0\n"
"buffer_load_dwordx4 v45, %[sW], s40 offen lds\n"
"s_mov_b32 s40, 3538944\n"
"s_add_u32 m0, %[sWST], 6144\n"
"s_nop 0\n"
"buffer_load_dwordx4 v44, %[sW], s40 offen lds\n"
"s_mov_b32 s40, 3604480\n"
"s_add_u32 m0, %[sWST], 7168\n"
"s_nop 0\n"
"buffer_load_dwordx4 v45, %[sW], s40 offen lds\n"
"s_waitcnt vmcnt(16)\n"
"ds_read_b64_tr_b16 a[128:129], v46 offset:8192\n"
"ds_read_b64_tr_b16 a[130:131], v46 offset:8704\n"
"ds_read_b64_tr_b16 a[132:133], v47 offset:8192\n"
"ds_read_b64_tr_b16 a[134:135], v47 offset:8704\n"
"ds_read_b64_tr_b16 a[136:137], v48 offset:8192\n"
"ds_read_b64_tr_b16 a[138:139], v48 offset:8704\n"
"ds_read_b64_tr_b16 a[140:141], v49 offset:8192\n"
"ds_read_b64_tr_b16 a[142:143], v49 offset:8704\n"
"s_waitcnt lgkmcnt(0)\n"
"ds_read_b64_tr_b16 a[144:145], v46 offset:12288\n"
"ds_read_b64_tr_b16 a[146:147], v46 offset:12800\n"
"ds_read_b64_tr_b16 a[148:149], v47 offset:12288\n"
"ds_read_b64_tr_b16 a[150:151], v47 offset:12800\n"
"ds_read_b64_tr_b16 a[152:153], v48 offset:12288\n"
"ds_read_b64_tr_b16 a[154:155], v48 offset:12800\n"
"ds_read_b64_tr_b16 a[156:157], v49 offset:12288\n"
"ds_read_b64_tr_b16 a[158:159], v49 offset:12800\n"
"s_waitcnt lgkmcnt(0)\n"
"s_mov_b32 s40, 3670016\n"
"s_add_u32 m0, %[sWST], 8192\n"
"s_nop 0\n"
"buffer_load_dwordx4 v44, %[sW], s40 offen lds\n"
"s_mov_b32 s40, 3735552\n"
"s_add_u32 m0, %[sWST], 9216\n"
"s_nop 0\n"
"buffer_load_dwordx4 v45, %[sW], s40 offen lds\n"
"s_mov_b32 s40, 3801088\n"
"s_add_u32 m0, %[sWST], 10240\n"
"s_nop 0\n"
"buffer_load_dwordx4 v44, %[sW], s40 offen lds\n"
"s_mov_b32 s40, 3866624\n"
"s_add_u32 m0, %[sWST], 11264\n"
"s_nop 0\n"
"buffer_load_dwordx4 v45, %[sW], s40 offen lds\n"
"s_mov_b32 s40, 3932160\n"
"s_add_u32 m0, %[sWST], 12288\n"
"s_nop 0\n"
"buffer_load_dwordx4 v44, %[sW], s40 offen lds\n"
"s_mov_b32 s40, 3997696\n"
"s_add_u32 m0, %[sWST], 13312\n"
"s_nop 0\n"
"buffer_load_dwordx4 v45, %[sW], s40 offen lds\n"
"s_mov_b32 s40, 4063232\n"
"s_add_u32 m0, %[sWST], 14336\n"
"s_nop 0\n"
"buffer_load_dwordx4 v44, %[sW], s40 offen lds\n"
"s_mov_b32 s40, 4128768\n"
"s_add_u32 m0, %[sWST], 15360\n"
"s_nop 0\n"
"buffer_load_dwordx4 v45, %[sW], s40 offen lds\n"
"s_waitcnt vmcnt(16)\n"
"ds_read_b64_tr_b16 a[160:161], v46 offset:16384\n"
"ds_read_b64_tr_b16 a[162:163], v46 offset:16896\n"
"ds_read_b64_tr_b16 a[164:165], v47 offset:16384\n"
"ds_read_b64_tr_b16 a[166:167], v47 offset:16896\n"
"ds_read_b64_tr_b16 a[168:169], v48 offset:16384\n"
"ds_read_b64_tr_b16 a[170:171], v48 offset:16896\n"
"ds_read_b64_tr_b16 a[172:173], v49 offset:16384\n"
"ds_read_b64_tr_b16 a[174:175], v49 offset:16896\n"
"s_waitcnt lgkmcnt(0)\n"
"ds_read_b64_tr_b16 a[176:177], v46 offset:20480\n"
"ds_read_b64_tr_b16 a[178:179], v46 offset:20992\n"
"ds_read_b64_tr_b16 a[180:181], v47 offset:20480\n"
"ds_read_b64_tr_b16 a[182:183], v47 offset:20992\n"
"ds_read_b64_tr_b16 a[184:185], v48 offset:20480\n"
"ds_read_b64_tr_b16 a[186:187], v48 offset:20992\n"
"ds_read_b64_tr_b16 a[188:189], v49 offset:20480\n"
"ds_read_b64_tr_b16 a[190:191], v49 offset:20992\n"
"s_waitcnt lgkmcnt(0)\n"
"s_mov_b32 s40, 1111040\n"
"buffer_load_dword v53, v54, %[sWS], s40 offen sc1\n"
"s_mov_b32 s40, 4194304\n"
"s_add_u32 m0, %[sWST], 16384\n"
"s_nop 0\n"
"buffer_load_dwordx4 v44, %[sW], s40 offen lds\n"
"s_mov_b32 s40, 4259840\n"
"s_add_u32 m0, %[sWST], 17408\n"
"s_nop 0\n"
"buffer_load_dwordx4 v45, %[sW], s40 offen lds\n"
"s_mov_b32 s40, 4325376\n"
"s_add_u32 m0, %[sWST], 18432\n"
"s_nop 0\n"
"buffer_load_dwordx4 v44, %[sW], s40 offen lds\n"
"s_mov_b32 s40, 4390912\n"
"s_add_u32 m0, %[sWST], 19456\n"
"s_nop 0\n"
"buffer_load_dwordx4 v45, %[sW], s40 offen lds\n"
"s_mov_b32 s40, 4456448\n"
"s_add_u32 m0, %[sWST], 20480\n"
"s_nop 0\n"
"buffer_load_dwordx4 v44, %[sW], s40 offen lds\n"
"s_mov_b32 s40, 4521984\n"
"s_add_u32 m0, %[sWST], 21504\n"
"s_nop 0\n"
"buffer_load_dwordx4 v45, %[sW], s40 offen lds\n"
"s_mov_b32 s40, 4587520\n"
"s_add_u32 m0, %[sWST], 22528\n"
"s_nop 0\n"
"buffer_load_dwordx4 v44, %[sW], s40 offen lds\n"
"s_mov_b32 s40, 4653056\n"
"s_add_u32 m0, %[sWST], 23552\n"
"s_nop 0\n"
"buffer_load_dwordx4 v45, %[sW], s40 offen lds\n"
"s_waitcnt vmcnt(16)\n"
"ds_read_b64_tr_b16 a[192:193], v46 offset:0\n"
"ds_read_b64_tr_b16 a[194:195], v46 offset:512\n"
"ds_read_b64_tr_b16 a[196:197], v47 offset:0\n"
"ds_read_b64_tr_b16 a[198:199], v47 offset:512\n"
"ds_read_b64_tr_b16 a[200:201], v48 offset:0\n"
"ds_read_b64_tr_b16 a[202:203], v48 offset:512\n"
"ds_read_b64_tr_b16 a[204:205], v49 offset:0\n"
"ds_read_b64_tr_b16 a[206:207], v49 offset:512\n"
"s_waitcnt lgkmcnt(0)\n"
"ds_read_b64_tr_b16 a[208:209], v46 offset:4096\n"
"ds_read_b64_tr_b16 a[210:211], v46 offset:4608\n"
"ds_read_b64_tr_b16 a[212:213], v47 offset:4096\n"
"ds_read_b64_tr_b16 a[214:215], v47 offset:4608\n"
"ds_read_b64_tr_b16 a[216:217], v48 offset:4096\n"
"ds_read_b64_tr_b16 a[218:219], v48 offset:4608\n"
"ds_read_b64_tr_b16 a[220:221], v49 offset:4096\n"
"ds_read_b64_tr_b16 a[222:223], v49 offset:4608\n"
"s_waitcnt lgkmcnt(0)\n"
"s_waitcnt vmcnt(8)\n"
"ds_read_b64_tr_b16 a[224:225], v46 offset:8192\n"
"ds_read_b64_tr_b16 a[226:227], v46 offset:8704\n"
"ds_read_b64_tr_b16 a[228:229], v47 offset:8192\n"
"ds_read_b64_tr_b16 a[230:231], v47 offset:8704\n"
"ds_read_b64_tr_b16 a[232:233], v48 offset:8192\n"
"ds_read_b64_tr_b16 a[234:235], v48 offset:8704\n"
"ds_read_b64_tr_b16 a[236:237], v49 offset:8192\n"
"ds_read_b64_tr_b16 a[238:239], v49 offset:8704\n"
"s_waitcnt lgkmcnt(0)\n"
"ds_read_b64_tr_b16 a[240:241], v46 offset:12288\n"
"ds_read_b64_tr_b16 a[242:243], v46 offset:12800\n"
"ds_read_b64_tr_b16 a[244:245], v47 offset:12288\n"
"ds_read_b64_tr_b16 a[246:247], v47 offset:12800\n"
"ds_read_b64_tr_b16 a[248:249], v48 offset:12288\n"
"ds_read_b64_tr_b16 a[250:251], v48 offset:12800\n"
"ds_read_b64_tr_b16 a[252:253], v49 offset:12288\n"
"ds_read_b64_tr_b16 a[254:255], v49 offset:12800\n"
"s_waitcnt lgkmcnt(0)\n"
"v_cmp_eq_u32 vcc, v52, v50\n"
"v_cmp_gt_i32 s[46:47], 0, v53\n"
"s_or_b64 vcc, vcc, s[46:47]\n"
"s_cbranch_vccz LQ_hsok\n"
"s_mov_b32 s41, 0\n"
"LQ_hb:\n"
"buffer_load_dword v20, v51, %[sWS], %[sHB] offen sc1\n"
"s_add_u32 s41, s41, 1\n"
"s_cmp_gt_u32 s41, 60000\n"
"s_cbranch_scc1 LQ_tmo\n"
"s_waitcnt vmcnt(0)\n"
"v_cmp_eq_u32 vcc, v20, v50\n"
"s_cbranch_vccnz LQ_hb\n"
"s_mov_b32 s40, 1111040\n"
"s_mov_b64 exec, -1\n"
"s_mov_b32 s41, 0\n"
"LQ_ln:\n"
"buffer_load_dword v20, v54, %[sWS], s40 offen sc1\n"
"s_add_u32 s41, s41, 1\n"
"s_cmp_gt_u32 s41, 60000\n"
"s_cbranch_scc1 LQ_tmo\n"
"s_waitcnt vmcnt(0)\n"
"v_cmp_gt_i32 vcc, 0, v20\n"
"s_cbranch_vccnz LQ_ln\n"
"LQ_ln_ok:\n"
"s_mov_b64 exec, -1\n"
"LQ_hsok:\n"
"s_add_u32 m0, s53, 0\n"
"s_add_u32 s40, s52, 0\n"
"buffer_load_dwordx4 %[vL16], %[sWS], s40 offen offset:0 sc1 lds\n"
"buffer_load_dwordx4 %[vL16], %[sWS], s40 offen offset:1024 sc1 lds\n"
"buffer_load_dwordx4 %[vL16], %[sWS], s40 offen offset:2048 sc1 lds\n"
"buffer_load_dwordx4 %[vL16], %[sWS], s40 offen offset:3072 sc1 lds\n"
"s_add_u32 m0, s53, 4096\n"
"s_add_u32 s40, s52, 4096\n"
"buffer_load_dwordx4 %[vL16], %[sWS], s40 offen offset:0 sc1 lds\n"
"buffer_load_dwordx4 %[vL16], %[sWS], s40 offen offset:1024 sc1 lds\n"
"buffer_load_dwordx4 %[vL16], %[sWS], s40 offen offset:2048 sc1 lds\n"
"buffer_load_dwordx4 %[vL16], %[sWS], s40 offen offset:3072 sc1 lds\n"
"s_add_u32 m0, s53, 8192\n"
"s_add_u32 s40, s52, 8192\n"
"buffer_load_dwordx4 %[vL16], %[sWS], s40 offen offset:0 sc1 lds\n"
"s_add_u32 s52, s52, 36864\n"
"s_add_u32 s53, s53, 36864\n"
"s_cmp_ge_u32 s53, 110592\n"
"s_cselect_b32 s40, 110592, 0\n"
"s_sub_u32 s53, s53, s40\n"
"s_waitcnt vmcnt(9)\n"
"ds_read_b64_tr_b16 v[224:225], v46 offset:16384\n"
"ds_read_b64_tr_b16 v[226:227], v46 offset:16896\n"
"ds_read_b64_tr_b16 v[228:229], v47 offset:16384\n"
"ds_read_b64_tr_b16 v[230:231], v47 offset:16896\n"
"ds_read_b64_tr_b16 v[232:233], v48 offset:16384\n"
"ds_read_b64_tr_b16 v[234:235], v48 offset:16896\n"
"ds_read_b64_tr_b16 v[236:237], v49 offset:16384\n"
"ds_read_b64_tr_b16 v[238:239], v49 offset:16896\n"
"s_waitcnt lgkmcnt(0)\n"
"ds_read_b64_tr_b16 v[240:241], v46 offset:20480\n"
"ds_read_b64_tr_b16 v[242:243], v46 offset:20992\n"
"ds_read_b64_tr_b16 v[244:245], v47 offset:20480\n"
"ds_read_b64_tr_b16 v[246:247], v47 offset:20992\n"
"ds_read_b64_tr_b16 v[248:249], v48 offset:20480\n"
"ds_read_b64_tr_b16 v[250:251], v48 offset:20992\n"
"ds_read_b64_tr_b16 v[252:253], v49 offset:20480\n"
"ds_read_b64_tr_b16 v[254:255], v49 offset:20992\n"
"s_waitcnt lgkmcnt(0)\n"
"s_barrier\n"
"s_add_u32 m0, s53, 0\n"
"s_add_u32 s40, s52, 0\n"
"buffer_load_dwordx4 %[vL16], %[sWS], s40 offen offset:0 sc1 lds\n"
"buffer_load_dwordx4 %[vL16], %[sWS], s40 offen offset:1024 sc1 lds\n"
"buffer_load_dwordx4 %[vL16], %[sWS], s40 offen offset:2048 sc1 lds\n"
"buffer_load_dwordx4 %[vL16], %[sWS], s40 offen offset:3072 sc1 lds\n"
"s_add_u32 m0, s53, 4096\n"
"s_add_u32 s40, s52, 4096\n"
"buffer_load_dwordx4 %[vL16], %[sWS], s40 offen offset:0 sc1 lds\n"
"buffer_load_dwordx4 %[vL16], %[sWS], s40 offen offset:1024 sc1 lds\n"
"buffer_load_dwordx4 %[vL16], %[sWS], s40 offen offset:2048 sc1 lds\n"
"buffer_load_dwordx4 %[vL16], %[sWS], s40 offen offset:3072 sc1 lds\n"
"s_add_u32 m0, s53, 8192\n"
"s_add_u32 s40, s52, 8192\n"
"buffer_load_dwordx4 %[vL16], %[sWS], s40 offen offset:0 sc1 lds\n"
"s_add_u32 s52, s52, 36864\n"
"s_add_u32 s53, s53, 36864\n"
"s_cmp_ge_u32 s53, 110592\n"
"s_cselect_b32 s40, 110592, 0\n"
"s_sub_u32 s53, s53, s40\n"
"s_mov_b32 s43, 0\n"
"s_mov_b32 s44, 0\n"
"s_mov_b32 s45, %[sOUT]\n"
"s_mov_b32 s46, 1073152\n"
"v_mov_b32 v23, %[vL16]\n"
"s_waitcnt vmcnt(9)\n"
"s_barrier\n"
"ds_read_b128 v[128:131], v23 offset:0\n"
"ds_read_b128 v[132:135], v23 offset:18432\n"
"ds_read_b128 v[136:139], v23 offset:1024\n"
"ds_read_b128 v[140:143], v23 offset:19456\n"
"s_cmp_eq_u32 %[sISV], 0\n"
"s_cbranch_scc0 LQ_v_loop\n"
"LQ_q_loop:\n"
"s_waitcnt lgkmcnt(2)\n"
"v_mfma_f32_16x16x32_bf16 v[64:67], a[0:3], v[128:131], 0\n"
"ds_read_b128 v[144:147], v23 offset:2048\n"
"v_mfma_f32_16x16x32_bf16 v[68:71], a[4:7], v[128:131], 0\n"
"v_mfma_f32_16x16x32_bf16 v[72:75], a[8:11], v[128:131], 0\n"
"ds_read_b128 v[148:151], v23 offset:20480\n"
"v_mfma_f32_16x16x32_bf16 v[76:79], a[12:15], v[128:131], 0\n"
"v_mfma_f32_16x16x32_bf16 v[80:83], a[0:3], v[132:135], 0\n"
"s_add_u32 s47, s45, 4096\n"
"v_mfma_f32_16x16x32_bf16 v[84:87], a[4:7], v[132:135], 0\n"
"v_mfma_f32_16x16x32_bf16 v[88:91], a[8:11], v[132:135], 0\n"
"v_mov_b32 v22, s43\n"
"v_mfma_f32_16x16x32_bf16 v[92:95], a[12:15], v[132:135], 0\n"
"s_waitcnt lgkmcnt(2)\n"
"v_mfma_f32_16x16x32_bf16 v[64:67], a[16:19], v[136:139], v[64:67]\n"
"ds_read_b128 v[128:131], v23 offset:3072\n"
"v_mfma_f32_16x16x32_bf16 v[68:71], a[20:23], v[136:139], v[68:71]\n"
"ds_read_b128 v[132:135], v23 offset:21504\n"
"v_mfma_f32_16x16x32_bf16 v[72:75], a[24:27], v[136:139], v[72:75]\n"
"v_cvt_pk_bf16_f32 v160, v96, v100\n"
"v_mfma_f32_16x16x32_bf16 v[76:79], a[28:31], v[136:139], v[76:79]\n"
"v_cvt_pk_bf16_f32 v161, v97, v101\n"
"v_mfma_f32_16x16x32_bf16 v[80:83], a[16:19], v[140:143], v[80:83]\n"
"v_cvt_pk_bf16_f32 v162, v98, v102\n"
"v_mfma_f32_16x16x32_bf16 v[84:87], a[20:23], v[140:143], v[84:87]\n"
"v_cvt_pk_bf16_f32 v163, v99, v103\n"
"v_mfma_f32_16x16x32_bf16 v[88:91], a[24:27], v[140:143], v[88:91]\n"
"buffer_store_dwordx4 v[160:163], %[vL16], %[sWS], s45 offen offset:0 sc1\n"
"v_mfma_f32_16x16x32_bf16 v[92:95], a[28:31], v[140:143], v[92:95]\n"
"s_waitcnt lgkmcnt(2)\n"
"v_mfma_f32_16x16x32_bf16 v[64:67], a[32:35], v[144:147], v[64:67]\n"
"ds_read_b128 v[136:139], v23 offset:4096\n"
"v_mfma_f32_16x16x32_bf16 v[68:71], a[36:39], v[144:147], v[68:71]\n"
"ds_read_b128 v[140:143], v23 offset:22528\n"
"v_mfma_f32_16x16x32_bf16 v[72:75], a[40:43], v[144:147], v[72:75]\n"
"v_cvt_pk_bf16_f32 v164, v104, v108\n"
"v_mfma_f32_16x16x32_bf16 v[76:79], a[44:47], v[144:147], v[76:79]\n"
"v_cvt_pk_bf16_f32 v165, v105, v109\n"
"v_mfma_f32_16x16x32_bf16 v[80:83], a[32:35], v[148:151], v[80:83]\n"
"v_cvt_pk_bf16_f32 v166, v106, v110\n"
"v_mfma_f32_16x16x32_bf16 v[84:87], a[36:39], v[148:151], v[84:87]\n"
"v_cvt_pk_bf16_f32 v167, v107, v111\n"
"v_mfma_f32_16x16x32_bf16 v[88:91], a[40:43], v[148:151], v[88:91]\n"
"buffer_store_dwordx4 v[164:167], %[vL16], %[sWS], s45 offen offset:1024 sc1\n"
"v_mfma_f32_16x16x32_bf16 v[92:95], a[44:47], v[148:151], v[92:95]\n"
"s_waitcnt lgkmcnt(2)\n"
"v_mfma_f32_16x16x32_bf16 v[64:67], a[48:51], v[128:131], v[64:67]\n"
"ds_read_b128 v[144:147], v23 offset:5120\n"
"v_mfma_f32_16x16x32_bf16 v[68:71], a[52:55], v[128:131], v[68:71]\n"
"ds_read_b128 v[148:151], v23 offset:23552\n"
"v_mfma_f32_16x16x32_bf16 v[72:75], a[56:59], v[128:131], v[72:75]\n"
"v_cvt_pk_bf16_f32 v168, v112, v116\n"
"v_mfma_f32_16x16x32_bf16 v[76:79], a[60:63], v[128:131], v[76:79]\n"
"v_cvt_pk_bf16_f32 v169, v113, v117\n"
"v_mfma_f32_16x16x32_bf16 v[80:83], a[48:51], v[132:135], v[80:83]\n"
"v_cvt_pk_bf16_f32 v170, v114, v118\n"
"v_mfma_f32_16x16x32_bf16 v[84:87], a[52:55], v[132:135], v[84:87]\n"
"v_cvt_pk_bf16_f32 v171, v115, v119\n"
"v_mfma_f32_16x16x32_bf16 v[88:91], a[56:59], v[132:135], v[88:91]\n"
"buffer_store_dwordx4 v[168:171], %[vL16], %[sWS], s47 offen offset:0 sc1\n"
"v_mfma_f32_16x16x32_bf16 v[92:95], a[60:63], v[132:135], v[92:95]\n"
"s_waitcnt lgkmcnt(2)\n"
"v_mfma_f32_16x16x32_bf16 v[64:67], a[64:67], v[136:139], v[64:67]\n"
"ds_read_b128 v[128:131], v23 offset:6144\n"
"v_mfma_f32_16x16x32_bf16 v[68:71], a[68:71], v[136:139], v[68:71]\n"
"ds_read_b128 v[132:135], v23 offset:24576\n"
"v_mfma_f32_16x16x32_bf16 v[72:75], a[72:75], v[136:139], v[72:75]\n"
"v_cvt_pk_bf16_f32 v172, v120, v124\n"
"v_mfma_f32_16x16x32_bf16 v[76:79], a[76:79], v[136:139], v[76:79]\n"
"v_cvt_pk_bf16_f32 v173, v121, v125\n"
"v_mfma_f32_16x16x32_bf16 v[80:83], a[64:67], v[140:143], v[80:83]\n"
"v_cvt_pk_bf16_f32 v174, v122, v126\n"
"v_mfma_f32_16x16x32_bf16 v[84:87], a[68:71], v[140:143], v[84:87]\n"
"v_cvt_pk_bf16_f32 v175, v123, v127\n"
"v_mfma_f32_16x16x32_bf16 v[88:91], a[72:75], v[140:143], v[88:91]\n"
"buffer_store_dwordx4 v[172:175], %[vL16], %[sWS], s47 offen offset:1024 sc1\n"
"v_mfma_f32_16x16x32_bf16 v[92:95], a[76:79], v[140:143], v[92:95]\n"
"s_waitcnt lgkmcnt(2)\n"
"v_mfma_f32_16x16x32_bf16 v[64:67], a[80:83], v[144:147], v[64:67]\n"
"ds_read_b128 v[136:139], v23 offset:7168\n"
"v_mfma_f32_16x16x32_bf16 v[68:71], a[84:87], v[144:147], v[68:71]\n"
"v_mfma_f32_16x16x32_bf16 v[72:75], a[88:91], v[144:147], v[72:75]\n"
"ds_read_b128 v[140:143], v23 offset:25600\n"
"v_mfma_f32_16x16x32_bf16 v[76:79], a[92:95], v[144:147], v[76:79]\n"
"v_mfma_f32_16x16x32_bf16 v[80:83], a[80:83], v[148:151], v[80:83]\n"
"v_mfma_f32_16x16x32_bf16 v[84:87], a[84:87], v[148:151], v[84:87]\n"
"s_cmp_lg_u32 s43, 0\n"
"s_cselect_b32 s40, 786432, 0\n"
"s_add_u32 s45, s45, s40\n"
"v_mfma_f32_16x16x32_bf16 v[88:91], a[88:91], v[148:151], v[88:91]\n"
"v_mfma_f32_16x16x32_bf16 v[92:95], a[92:95], v[148:151], v[92:95]\n"
"s_waitcnt lgkmcnt(2)\n"
"v_mfma_f32_16x16x32_bf16 v[64:67], a[96:99], v[128:131], v[64:67]\n"
"ds_read_b128 v[144:147], v23 offset:8192\n"
"v_mfma_f32_16x16x32_bf16 v[68:71], a[100:103], v[128:131], v[68:71]\n"
"v_mfma_f32_16x16x32_bf16 v[72:75], a[104:107], v[128:131], v[72:75]\n"
"ds_read_b128 v[148:151], v23 offset:26624\n"
"v_mfma_f32_16x16x32_bf16 v[76:79], a[108:111], v[128:131], v[76:79]\n"
"v_mfma_f32_16x16x32_bf16 v[80:83], a[96:99], v[132:135], v[80:83]\n"
"v_mfma_f32_16x16x32_bf16 v[84:87], a[100:103], v[132:135], v[84:87]\n"
"s_add_u32 m0, s53, 0\n"
"s_add_u32 s40, s52, 0\n"
"buffer_load_dwordx4 %[vL16], %[sWS], s40 offen offset:0 sc1 lds\n"
"v_mfma_f32_16x16x32_bf16 v[88:91], a[104:107], v[132:135], v[88:91]\n"
"v_mfma_f32_16x16x32_bf16 v[92:95], a[108:111], v[132:135], v[92:95]\n"
"s_waitcnt lgkmcnt(2)\n"
"v_mfma_f32_16x16x32_bf16 v[64:67], a[112:115], v[136:139], v[64:67]\n"
"ds_read_b128 v[128:131], v23 offset:9216\n"
"v_mfma_f32_16x16x32_bf16 v[68:71], a[116:119], v[136:139], v[68:71]\n"
"v_mfma_f32_16x16x32_bf16 v[72:75], a[120:123], v[136:139], v[72:75]\n"
"ds_read_b128 v[132:135], v23 offset:27648\n"
"v_mfma_f32_16x16x32_bf16 v[76:79], a[124:127], v[136:139], v[76:79]\n"
"v_mfma_f32_16x16x32_bf16 v[80:83], a[112:115], v[140:143], v[80:83]\n"
"v_mfma_f32_16x16x32_bf16 v[84:87], a[116:119], v[140:143], v[84:87]\n"
"buffer_load_dwordx4 %[vL16], %[sWS], s40 offen offset:1024 sc1 lds\n"
"v_mfma_f32_16x16x32_bf16 v[88:91], a[120:123], v[140:143], v[88:91]\n"
"v_mfma_f32_16x16x32_bf16 v[92:95], a[124:127], v[140:143], v[92:95]\n"
"s_waitcnt lgkmcnt(2)\n"
"v_mfma_f32_16x16x32_bf16 v[64:67], a[128:131], v[144:147], v[64:67]\n"
"ds_read_b128 v[136:139], v23 offset:10240\n"
"v_mfma_f32_16x16x32_bf16 v[68:71], a[132:135], v[144:147], v[68:71]\n"
"v_mfma_f32_16x16x32_bf16 v[72:75], a[136:139], v[144:147], v[72:75]\n"
"ds_read_b128 v[140:143], v23 offset:28672\n"
"v_mfma_f32_16x16x32_bf16 v[76:79], a[140:143], v[144:147], v[76:79]\n"
"v_mfma_f32_16x16x32_bf16 v[80:83], a[128:131], v[148:151], v[80:83]\n"
"v_mfma_f32_16x16x32_bf16 v[84:87], a[132:135], v[148:151], v[84:87]\n"
"buffer_load_dwordx4 %[vL16], %[sWS], s40 offen offset:2048 sc1 lds\n"
"v_mfma_f32_16x16x32_bf16 v[88:91], a[136:139], v[148:151], v[88:91]\n"
"v_mfma_f32_16x16x32_bf16 v[92:95], a[140:143], v[148:151], v[92:95]\n"
"s_waitcnt lgkmcnt(2)\n"
"v_mfma_f32_16x16x32_bf16 v[64:67], a[144:147], v[128:131], v[64:67]\n"
"ds_read_b128 v[144:147], v23 offset:11264\n"
"v_mfma_f32_16x16x32_bf16 v[68:71], a[148:151], v[128:131], v[68:71]\n"
"v_mfma_f32_16x16x32_bf16 v[72:75], a[152:155], v[128:131], v[72:75]\n"
"ds_read_b128 v[148:151], v23 offset:29696\n"
"v_mfma_f32_16x16x32_bf16 v[76:79], a[156:159], v[128:131], v[76:79]\n"
"v_mfma_f32_16x16x32_bf16 v[80:83], a[144:147], v[132:135], v[80:83]\n"
"v_mfma_f32_16x16x32_bf16 v[84:87], a[148:151], v[132:135], v[84:87]\n"
"buffer_load_dwordx4 %[vL16], %[sWS], s40 offen offset:3072 sc1 lds\n"
"v_mfma_f32_16x16x32_bf16 v[88:91], a[152:155], v[132:135], v[88:91]\n"
"v_mfma_f32_16x16x32_bf16 v[92:95], a[156:159], v[132:135], v[92:95]\n"
"s_waitcnt lgkmcnt(2)\n"
"v_mfma_f32_16x16x32_bf16 v[64:67], a[160:163], v[136:139], v[64:67]\n"
"ds_read_b128 v[128:131], v23 offset:12288\n"
"v_mfma_f32_16x16x32_bf16 v[68:71], a[164:167], v[136:139], v[68:71]\n"
"v_mfma_f32_16x16x32_bf16 v[72:75], a[168:171], v[136:139], v[72:75]\n"
"ds_read_b128 v[132:135], v23 offset:30720\n"
"v_mfma_f32_16x16x32_bf16 v[76:79], a[172:175], v[136:139], v[76:79]\n"
"v_mfma_f32_16x16x32_bf16 v[80:83], a[160:163], v[140:143], v[80:83]\n"
"v_mfma_f32_16x16x32_bf16 v[84:87], a[164:167], v[140:143], v[84:87]\n"
"s_add_u32 m0, s53, 4096\n"
"s_add_u32 s40, s52, 4096\n"
"buffer_load_dwordx4 %[vL16], %[sWS], s40 offen offset:0 sc1 lds\n"
"v_mfma_f32_16x16x32_bf16 v[88:91], a[168:171], v[140:143], v[88:91]\n"
"v_mfma_f32_16x16x32_bf16 v[92:95], a[172:175], v[140:143], v[92:95]\n"
"s_waitcnt lgkmcnt(2)\n"
"v_mfma_f32_16x16x32_bf16 v[64:67], a[176:179], v[144:147], v[64:67]\n"
"s_waitcnt vmcnt(5)\n"
"buffer_store_dword v22, %[vFL], %[sWS], s46 offen sc1\n"
"v_mfma_f32_16x16x32_bf16 v[68:71], a[180:183], v[144:147], v[68:71]\n"
"v_mfma_f32_16x16x32_bf16 v[72:75], a[184:187], v[144:147], v[72:75]\n"
"ds_read_b128 v[136:139], v23 offset:13312\n"
"v_mfma_f32_16x16x32_bf16 v[76:79], a[188:191], v[144:147], v[76:79]\n"
"v_mfma_f32_16x16x32_bf16 v[80:83], a[176:179], v[148:151], v[80:83]\n"
"ds_read_b128 v[140:143], v23 offset:31744\n"
"v_mfma_f32_16x16x32_bf16 v[84:87], a[180:183], v[148:151], v[84:87]\n"
"v_mfma_f32_16x16x32_bf16 v[88:91], a[184:187], v[148:151], v[88:91]\n"
"buffer_load_dwordx4 %[vL16], %[sWS], s40 offen offset:1024 sc1 lds\n"
"v_mfma_f32_16x16x32_bf16 v[92:95], a[188:191], v[148:151], v[92:95]\n"
"s_waitcnt lgkmcnt(2)\n"
"v_mfma_f32_16x16x32_bf16 v[64:67], a[192:195], v[128:131], v[64:67]\n"
"ds_read_b128 v[144:147], v23 offset:14336\n"
"v_mfma_f32_16x16x32_bf16 v[68:71], a[196:199], v[128:131], v[68:71]\n"
"v_mfma_f32_16x16x32_bf16 v[72:75], a[200:203], v[128:131], v[72:75]\n"
"ds_read_b128 v[148:151], v23 offset:32768\n"
"v_mfma_f32_16x16x32_bf16 v[76:79], a[204:207], v[128:131], v[76:79]\n"
"v_mfma_f32_16x16x32_bf16 v[80:83], a[192:195], v[132:135], v[80:83]\n"
"v_mfma_f32_16x16x32_bf16 v[84:87], a[196:199], v[132:135], v[84:87]\n"
"buffer_load_dwordx4 %[vL16], %[sWS], s40 offen offset:2048 sc1 lds\n"
"v_mfma_f32_16x16x32_bf16 v[88:91], a[200:203], v[132:135], v[88:91]\n"
"v_mfma_f32_16x16x32_bf16 v[92:95], a[204:207], v[132:135], v[92:95]\n"
"s_waitcnt lgkmcnt(2)\n"
"v_mfma_f32_16x16x32_bf16 v[64:67], a[208:211], v[136:139], v[64:67]\n"
"ds_read_b128 v[128:131], v23 offset:15360\n"
"v_mfma_f32_16x16x32_bf16 v[68:71], a[212:215], v[136:139], v[68:71]\n"
"v_mfma_f32_16x16x32_bf16 v[72:75], a[216:219], v[136:139], v[72:75]\n"
"ds_read_b128 v[132:135], v23 offset:33792\n"
"v_mfma_f32_16x16x32_bf16 v[76:79], a[220:223], v[136:139], v[76:79]\n"
"v_mfma_f32_16x16x32_bf16 v[80:83], a[208:211], v[140:143], v[80:83]\n"
"v_mfma_f32_16x16x32_bf16 v[84:87], a[212:215], v[140:143], v[84:87]\n"
"buffer_load_dwordx4 %[vL16], %[sWS], s40 offen offset:3072 sc1 lds\n"
"v_mfma_f32_16x16x32_bf16 v[88:91], a[216:219], v[140:143], v[88:91]\n"
"v_mfma_f32_16x16x32_bf16 v[92:95], a[220:223], v[140:143], v[92:95]\n"
"s_waitcnt lgkmcnt(2)\n"
"v_mfma_f32_16x16x32_bf16 v[64:67], a[224:227], v[144:147], v[64:67]\n"
"ds_read_b128 v[136:139], v23 offset:16384\n"
"v_mfma_f32_16x16x32_bf16 v[68:71], a[228:231], v[144:147], v[68:71]\n"
"v_mfma_f32_16x16x32_bf16 v[72:75], a[232:235], v[144:147], v[72:75]\n"
"ds_read_b128 v[140:143], v23 offset:34816\n"
"v_mfma_f32_16x16x32_bf16 v[76:79], a[236:239], v[144:147], v[76:79]\n"
"v_mfma_f32_16x16x32_bf16 v[80:83], a[224:227], v[148:151], v[80:83]\n"
"v_mfma_f32_16x16x32_bf16 v[84:87], a[228:231], v[148:151], v[84:87]\n"
"s_add_u32 m0, s53, 8192\n"
"s_add_u32 s40, s52, 8192\n"
"buffer_load_dwordx4 %[vL16], %[sWS], s40 offen offset:0 sc1 lds\n"
"v_mfma_f32_16x16x32_bf16 v[88:91], a[232:235], v[148:151], v[88:91]\n"
"v_mfma_f32_16x16x32_bf16 v[92:95], a[236:239], v[148:151], v[92:95]\n"
"s_waitcnt lgkmcnt(2)\n"
"v_mfma_f32_16x16x32_bf16 v[64:67], a[240:243], v[128:131], v[64:67]\n"
"ds_read_b128 v[144:147], v23 offset:17408\n"
"v_mfma_f32_16x16x32_bf16 v[68:71], a[244:247], v[128:131], v[68:71]\n"
"ds_read_b128 v[148:151], v23 offset:35840\n"
"v_mfma_f32_16x16x32_bf16 v[72:75], a[248:251], v[128:131], v[72:75]\n"
"v_mfma_f32_16x16x32_bf16 v[76:79], a[252:255], v[128:131], v[76:79]\n"
"s_add_u32 s52, s52, 36864\n"
"s_add_u32 s53, s53, 36864\n"
"s_cmp_ge_u32 s53, 110592\n"
"s_cselect_b32 s40, 110592, 0\n"
"s_sub_u32 s53, s53, s40\n"
"v_mfma_f32_16x16x32_bf16 v[80:83], a[240:243], v[132:135], v[80:83]\n"
"s_add_u32 s44, s44, 36864\n"
"s_cmp_ge_u32 s44, 110592\n"
"s_cselect_b32 s44, 0, s44\n"
"v_mfma_f32_16x16x32_bf16 v[84:87], a[244:247], v[132:135], v[84:87]\n"
"v_mfma_f32_16x16x32_bf16 v[88:91], a[248:251], v[132:135], v[88:91]\n"
"v_add_u32 v23, s44, %[vL16]\n"
"s_waitcnt lgkmcnt(0)\n"
"s_barrier\n"
"v_mfma_f32_16x16x32_bf16 v[92:95], a[252:255], v[132:135], v[92:95]\n"
"s_waitcnt lgkmcnt(2)\n"
"v_mfma_f32_16x16x32_bf16 v[64:67], v[224:227], v[136:139], v[64:67]\n"
"ds_read_b128 v[128:131], v23 offset:0\n"
"v_mfma_f32_16x16x32_bf16 v[68:71], v[228:231], v[136:139], v[68:71]\n"
"v_mfma_f32_16x16x32_bf16 v[72:75], v[232:235], v[136:139], v[72:75]\n"
"v_mfma_f32_16x16x32_bf16 v[76:79], v[236:239], v[136:139], v[76:79]\n"
"v_mfma_f32_16x16x32_bf16 v[80:83], v[224:227], v[140:143], v[80:83]\n"
"ds_read_b128 v[132:135], v23 offset:18432\n"
"v_mfma_f32_16x16x32_bf16 v[84:87], v[228:231], v[140:143], v[84:87]\n"
"v_mfma_f32_16x16x32_bf16 v[88:91], v[232:235], v[140:143], v[88:91]\n"
"v_mfma_f32_16x16x32_bf16 v[92:95], v[236:239], v[140:143], v[92:95]\n"
"s_waitcnt lgkmcnt(2)\n"
"v_mfma_f32_16x16x32_bf16 v[64:67], v[240:243], v[144:147], v[64:67]\n"
"ds_read_b128 v[136:139], v23 offset:1024\n"
"v_mfma_f32_16x16x32_bf16 v[68:71], v[244:247], v[144:147], v[68:71]\n"
"v_mfma_f32_16x16x32_bf16 v[72:75], v[248:251], v[144:147], v[72:75]\n"
"ds_read_b128 v[140:143], v23 offset:19456\n"
"v_mfma_f32_16x16x32_bf16 v[76:79], v[252:255], v[144:147], v[76:79]\n"
"v_mfma_f32_16x16x32_bf16 v[80:83], v[240:243], v[148:151], v[80:83]\n"
"v_mfma_f32_16x16x32_bf16 v[84:87], v[244:247], v[148:151], v[84:87]\n"
"s_add_u32 s43, s43, 1\n"
"s_cmp_lt_u32 s43, 64\n"
"v_mfma_f32_16x16x32_bf16 v[88:91], v[248:251], v[148:151], v[88:91]\n"
"v_mfma_f32_16x16x32_bf16 v[92:95], v[252:255], v[148:151], v[92:95]\n"
"s_waitcnt lgkmcnt(2)\n"
"v_mfma_f32_16x16x32_bf16 v[96:99], a[0:3], v[128:131], 0\n"
"ds_read_b128 v[144:147], v23 offset:2048\n"
"v_mfma_f32_16x16x32_bf16 v[100:103], a[4:7], v[128:131], 0\n"
"v_mfma_f32_16x16x32_bf16 v[104:107], a[8:11], v[128:131], 0\n"
"ds_read_b128 v[148:151], v23 offset:20480\n"
"v_mfma_f32_16x16x32_bf16 v[108:111], a[12:15], v[128:131], 0\n"
"v_mfma_f32_16x16x32_bf16 v[112:115], a[0:3], v[132:135], 0\n"
"s_add_u32 s47, s45, 4096\n"
"v_mfma_f32_16x16x32_bf16 v[116:119], a[4:7], v[132:135], 0\n"
"v_mfma_f32_16x16x32_bf16 v[120:123], a[8:11], v[132:135], 0\n"
"v_mov_b32 v22, s43\n"
"v_mfma_f32_16x16x32_bf16 v[124:127], a[12:15], v[132:135], 0\n"
"s_waitcnt lgkmcnt(2)\n"
"v_mfma_f32_16x16x32_bf16 v[96:99], a[16:19], v[136:139], v[96:99]\n"
"ds_read_b128 v[128:131], v23 offset:3072\n"
"v_mfma_f32_16x16x32_bf16 v[100:103], a[20:23], v[136:139], v[100:103]\n"
"ds_read_b128 v[132:135], v23 offset:21504\n"
"v_mfma_f32_16x16x32_bf16 v[104:107], a[24:27], v[136:139], v[104:107]\n"
"v_cvt_pk_bf16_f32 v160, v64, v68\n"
"v_mfma_f32_16x16x32_bf16 v[108:111], a[28:31], v[136:139], v[108:111]\n"
"v_cvt_pk_bf16_f32 v161, v65, v69\n"
"v_mfma_f32_16x16x32_bf16 v[112:115], a[16:19], v[140:143], v[112:115]\n"
"v_cvt_pk_bf16_f32 v162, v66, v70\n"
"v_mfma_f32_16x16x32_bf16 v[116:119], a[20:23], v[140:143], v[116:119]\n"
"v_cvt_pk_bf16_f32 v163, v67, v71\n"
"v_mfma_f32_16x16x32_bf16 v[120:123], a[24:27], v[140:143], v[120:123]\n"
"buffer_store_dwordx4 v[160:163], %[vL16], %[sWS], s45 offen offset:0 sc1\n"
"v_mfma_f32_16x16x32_bf16 v[124:127], a[28:31], v[140:143], v[124:127]\n"
"s_waitcnt lgkmcnt(2)\n"
"v_mfma_f32_16x16x32_bf16 v[96:99], a[32:35], v[144:147], v[96:99]\n"
"ds_read_b128 v[136:139], v23 offset:4096\n"
"v_mfma_f32_16x16x32_bf16 v[100:103], a[36:39], v[144:147], v[100:103]\n"
"ds_read_b128 v[140:143], v23 offset:22528\n"
"v_mfma_f32_16x16x32_bf16 v[104:107], a[40:43], v[144:147], v[104:107]\n"
"v_cvt_pk_bf16_f32 v164, v72, v76\n"
"v_mfma_f32_16x16x32_bf16 v[108:111], a[44:47], v[144:147], v[108:111]\n"
"v_cvt_pk_bf16_f32 v165, v73, v77\n"
"v_mfma_f32_16x16x32_bf16 v[112:115], a[32:35], v[148:151], v[112:115]\n"
"v_cvt_pk_bf16_f32 v166, v74, v78\n"
"v_mfma_f32_16x16x32_bf16 v[116:119], a[36:39], v[148:151], v[116:119]\n"
"v_cvt_pk_bf16_f32 v167, v75, v79\n"
"v_mfma_f32_16x16x32_bf16 v[120:123], a[40:43], v[148:151], v[120:123]\n"
"buffer_store_dwordx4 v[164:167], %[vL16], %[sWS], s45 offen offset:1024 sc1\n"
"v_mfma_f32_16x16x32_bf16 v[124:127], a[44:47], v[148:151], v[124:127]\n"
"s_waitcnt lgkmcnt(2)\n"
"v_mfma_f32_16x16x32_bf16 v[96:99], a[48:51], v[128:131], v[96:99]\n"
"ds_read_b128 v[144:147], v23 offset:5120\n"
"v_mfma_f32_16x16x32_bf16 v[100:103], a[52:55], v[128:131], v[100:103]\n"
"ds_read_b128 v[148:151], v23 offset:23552\n"
"v_mfma_f32_16x16x32_bf16 v[104:107], a[56:59], v[128:131], v[104:107]\n"
"v_cvt_pk_bf16_f32 v168, v80, v84\n"
"v_mfma_f32_16x16x32_bf16 v[108:111], a[60:63], v[128:131], v[108:111]\n"
"v_cvt_pk_bf16_f32 v169, v81, v85\n"
"v_mfma_f32_16x16x32_bf16 v[112:115], a[48:51], v[132:135], v[112:115]\n"
"v_cvt_pk_bf16_f32 v170, v82, v86\n"
"v_mfma_f32_16x16x32_bf16 v[116:119], a[52:55], v[132:135], v[116:119]\n"
"v_cvt_pk_bf16_f32 v171, v83, v87\n"
"v_mfma_f32_16x16x32_bf16 v[120:123], a[56:59], v[132:135], v[120:123]\n"
"buffer_store_dwordx4 v[168:171], %[vL16], %[sWS], s47 offen offset:0 sc1\n"
"v_mfma_f32_16x16x32_bf16 v[124:127], a[60:63], v[132:135], v[124:127]\n"
"s_waitcnt lgkmcnt(2)\n"
"v_mfma_f32_16x16x32_bf16 v[96:99], a[64:67], v[136:139], v[96:99]\n"
"ds_read_b128 v[128:131], v23 offset:6144\n"
"v_mfma_f32_16x16x32_bf16 v[100:103], a[68:71], v[136:139], v[100:103]\n"
"ds_read_b128 v[132:135], v23 offset:24576\n"
"v_mfma_f32_16x16x32_bf16 v[104:107], a[72:75], v[136:139], v[104:107]\n"
"v_cvt_pk_bf16_f32 v172, v88, v92\n"
"v_mfma_f32_16x16x32_bf16 v[108:111], a[76:79], v[136:139], v[108:111]\n"
"v_cvt_pk_bf16_f32 v173, v89, v93\n"
"v_mfma_f32_16x16x32_bf16 v[112:115], a[64:67], v[140:143], v[112:115]\n"
"v_cvt_pk_bf16_f32 v174, v90, v94\n"
"v_mfma_f32_16x16x32_bf16 v[116:119], a[68:71], v[140:143], v[116:119]\n"
"v_cvt_pk_bf16_f32 v175, v91, v95\n"
"v_mfma_f32_16x16x32_bf16 v[120:123], a[72:75], v[140:143], v[120:123]\n"
"buffer_store_dwordx4 v[172:175], %[vL16], %[sWS], s47 offen offset:1024 sc1\n"
"v_mfma_f32_16x16x32_bf16 v[124:127], a[76:79], v[140:143], v[124:127]\n"
"s_waitcnt lgkmcnt(2)\n"
"v_mfma_f32_16x16x32_bf16 v[96:99], a[80:83], v[144:147], v[96:99]\n"
"ds_read_b128 v[136:139], v23 offset:7168\n"
"v_mfma_f32_16x16x32_bf16 v[100:103], a[84:87], v[144:147], v[100:103]\n"
"v_mfma_f32_16x16x32_bf16 v[104:107], a[88:91], v[144:147], v[104:107]\n"
"ds_read_b128 v[140:143], v23 offset:25600\n"
"v_mfma_f32_16x16x32_bf16 v[108:111], a[92:95], v[144:147], v[108:111]\n"
"v_mfma_f32_16x16x32_bf16 v[112:115], a[80:83], v[148:151], v[112:115]\n"
"s_cmp_lg_u32 s43, 0\n"
"s_cselect_b32 s40, 786432, 0\n"
"s_add_u32 s45, s45, s40\n"
"v_mfma_f32_16x16x32_bf16 v[116:119], a[84:87], v[148:151], v[116:119]\n"
"v_mfma_f32_16x16x32_bf16 v[120:123], a[88:91], v[148:151], v[120:123]\n"
"s_cmp_eq_u32 s43, 5\n"
"s_cbranch_scc1 LQ_q_all\n"
"LQ_q_allok:\n"
"v_mfma_f32_16x16x32_bf16 v[124:127], a[92:95], v[148:151], v[124:127]\n"
"s_waitcnt lgkmcnt(2)\n"
"v_mfma_f32_16x16x32_bf16 v[96:99], a[96:99], v[128:131], v[96:99]\n"
"ds_read_b128 v[144:147], v23 offset:8192\n"
"v_mfma_f32_16x16x32_bf16 v[100:103], a[100:103], v[128:131], v[100:103]\n"
"v_mfma_f32_16x16x32_bf16 v[104:107], a[104:107], v[128:131], v[104:107]\n"
"ds_read_b128 v[148:151], v23 offset:26624\n"
"v_mfma_f32_16x16x32_bf16 v[108:111], a[108:111], v[128:131], v[108:111]\n"
"v_mfma_f32_16x16x32_bf16 v[112:115], a[96:99], v[132:135], v[112:115]\n"
"v_mfma_f32_16x16x32_bf16 v[116:119], a[100:103], v[132:135], v[116:119]\n"
"s_add_u32 m0, s53, 0\n"
"s_add_u32 s40, s52, 0\n"
"buffer_load_dwordx4 %[vL16], %[sWS], s40 offen offset:0 sc1 lds\n"
"v_mfma_f32_16x16x32_bf16 v[120:123], a[104:107], v[132:135], v[120:123]\n"
"v_mfma_f32_16x16x32_bf16 v[124:127], a[108:111], v[132:135], v[124:127]\n"
"s_waitcnt lgkmcnt(2)\n"
"v_mfma_f32_16x16x32_bf16 v[96:99], a[112:115], v[136:139], v[96:99]\n"
"ds_read_b128 v[128:131], v23 offset:9216\n"
"v_mfma_f32_16x16x32_bf16 v[100:103], a[116:119], v[136:139], v[100:103]\n"
"v_mfma_f32_16x16x32_bf16 v[104:107], a[120:123], v[136:139], v[104:107]\n"
"ds_read_b128 v[132:135], v23 offset:27648\n"
"v_mfma_f32_16x16x32_bf16 v[108:111], a[124:127], v[136:139], v[108:111]\n"
"v_mfma_f32_16x16x32_bf16 v[112:115], a[112:115], v[140:143], v[112:115]\n"
"v_mfma_f32_16x16x32_bf16 v[116:119], a[116:119], v[140:143], v[116:119]\n"
"buffer_load_dwordx4 %[vL16], %[sWS], s40 offen offset:1024 sc1 lds\n"
"v_mfma_f32_16x16x32_bf16 v[120:123], a[120:123], v[140:143], v[120:123]\n"
"v_mfma_f32_16x16x32_bf16 v[124:127], a[124:127], v[140:143], v[124:127]\n"
"s_waitcnt lgkmcnt(2)\n"
"v_mfma_f32_16x16x32_bf16 v[96:99], a[128:131], v[144:147], v[96:99]\n"
"ds_read_b128 v[136:139], v23 offset:10240\n"
"v_mfma_f32_16x16x32_bf16 v[100:103], a[132:135], v[144:147], v[100:103]\n"
"v_mfma_f32_16x16x32_bf16 v[104:107], a[136:139], v[144:147], v[104:107]\n"
"ds_read_b128 v[140:143], v23 offset:28672\n"
"v_mfma_f32_16x16x32_bf16 v[108:111], a[140:143], v[144:147], v[108:111]\n"
"v_mfma_f32_16x16x32_bf16 v[112:115], a[128:131], v[148:151], v[112:115]\n"
"v_mfma_f32_16x16x32_bf16 v[116:119], a[132:135], v[148:151], v[116:119]\n"
"buffer_load_dwordx4 %[vL16], %[sWS], s40 offen offset:2048 sc1 lds\n"
"v_mfma_f32_16x16x32_bf16 v[120:123], a[136:139], v[148:151], v[120:123]\n"
"v_mfma_f32_16x16x32_bf16 v[124:127], a[140:143], v[148:151], v[124:127]\n"
"s_waitcnt lgkmcnt(2)\n"
"v_mfma_f32_16x16x32_bf16 v[96:99], a[144:147], v[128:131], v[96:99]\n"
"ds_read_b128 v[144:147], v23 offset:11264\n"
"v_mfma_f32_16x16x32_bf16 v[100:103], a[148:151], v[128:131], v[100:103]\n"
"v_mfma_f32_16x16x32_bf16 v[104:107], a[152:155], v[128:131], v[104:107]\n"
"ds_read_b128 v[148:151], v23 offset:29696\n"
"v_mfma_f32_16x16x32_bf16 v[108:111], a[156:159], v[128:131], v[108:111]\n"
"v_mfma_f32_16x16x32_bf16 v[112:115], a[144:147], v[132:135], v[112:115]\n"
"v_mfma_f32_16x16x32_bf16 v[116:119], a[148:151], v[132:135], v[116:119]\n"
"buffer_load_dwordx4 %[vL16], %[sWS], s40 offen offset:3072 sc1 lds\n"
"v_mfma_f32_16x16x32_bf16 v[120:123], a[152:155], v[132:135], v[120:123]\n"
"v_mfma_f32_16x16x32_bf16 v[124:127], a[156:159], v[132:135], v[124:127]\n"
"s_waitcnt lgkmcnt(2)\n"
"v_mfma_f32_16x16x32_bf16 v[96:99], a[160:163], v[136:139], v[96:99]\n"
"ds_read_b128 v[128:131], v23 offset:12288\n"
"v_mfma_f32_16x16x32_bf16 v[100:103], a[164:167], v[136:139], v[100:103]\n"
"v_mfma_f32_16x16x32_bf16 v[104:107], a[168:171], v[136:139], v[104:107]\n"
"ds_read_b128 v[132:135], v23 offset:30720\n"
"v_mfma_f32_16x16x32_bf16 v[108:111], a[172:175], v[136:139], v[108:111]\n"
"v_mfma_f32_16x16x32_bf16 v[112:115], a[160:163], v[140:143], v[112:115]\n"
"v_mfma_f32_16x16x32_bf16 v[116:119], a[164:167], v[140:143], v[116:119]\n"
"s_add_u32 m0, s53, 4096\n"
"s_add_u32 s40, s52, 4096\n"
"buffer_load_dwordx4 %[vL16], %[sWS], s40 offen offset:0 sc1 lds\n"
"v_mfma_f32_16x16x32_bf16 v[120:123], a[168:171], v[140:143], v[120:123]\n"
"v_mfma_f32_16x16x32_bf16 v[124:127], a[172:175], v[140:143], v[124:127]\n"
"s_waitcnt lgkmcnt(2)\n"
"v_mfma_f32_16x16x32_bf16 v[96:99], a[176:179], v[144:147], v[96:99]\n"
"s_waitcnt vmcnt(5)\n"
"buffer_store_dword v22, %[vFL], %[sWS], s46 offen sc1\n"
"v_mfma_f32_16x16x32_bf16 v[100:103], a[180:183], v[144:147], v[100:103]\n"
"v_mfma_f32_16x16x32_bf16 v[104:107], a[184:187], v[144:147], v[104:107]\n"
"ds_read_b128 v[136:139], v23 offset:13312\n"
"v_mfma_f32_16x16x32_bf16 v[108:111], a[188:191], v[144:147], v[108:111]\n"
"v_mfma_f32_16x16x32_bf16 v[112:115], a[176:179], v[148:151], v[112:115]\n"
"ds_read_b128 v[140:143], v23 offset:31744\n"
"v_mfma_f32_16x16x32_bf16 v[116:119], a[180:183], v[148:151], v[116:119]\n"
"v_mfma_f32_16x16x32_bf16 v[120:123], a[184:187], v[148:151], v[120:123]\n"
"buffer_load_dwordx4 %[vL16], %[sWS], s40 offen offset:1024 sc1 lds\n"
"v_mfma_f32_16x16x32_bf16 v[124:127], a[188:191], v[148:151], v[124:127]\n"
"s_waitcnt lgkmcnt(2)\n"
"v_mfma_f32_16x16x32_bf16 v[96:99], a[192:195], v[128:131], v[96:99]\n"
"ds_read_b128 v[144:147], v23 offset:14336\n"
"v_mfma_f32_16x16x32_bf16 v[100:103], a[196:199], v[128:131], v[100:103]\n"
"v_mfma_f32_16x16x32_bf16 v[104:107], a[200:203], v[128:131], v[104:107]\n"
"ds_read_b128 v[148:151], v23 offset:32768\n"
"v_mfma_f32_16x16x32_bf16 v[108:111], a[204:207], v[128:131], v[108:111]\n"
"v_mfma_f32_16x16x32_bf16 v[112:115], a[192:195], v[132:135], v[112:115]\n"
"v_mfma_f32_16x16x32_bf16 v[116:119], a[196:199], v[132:135], v[116:119]\n"
"buffer_load_dwordx4 %[vL16], %[sWS], s40 offen offset:2048 sc1 lds\n"
"v_mfma_f32_16x16x32_bf16 v[120:123], a[200:203], v[132:135], v[120:123]\n"
"v_mfma_f32_16x16x32_bf16 v[124:127], a[204:207], v[132:135], v[124:127]\n"
"s_waitcnt lgkmcnt(2)\n"
"v_mfma_f32_16x16x32_bf16 v[96:99], a[208:211], v[136:139], v[96:99]\n"
"ds_read_b128 v[128:131], v23 offset:15360\n"
"v_mfma_f32_16x16x32_bf16 v[100:103], a[212:215], v[136:139], v[100:103]\n"
"v_mfma_f32_16x16x32_bf16 v[104:107], a[216:219], v[136:139], v[104:107]\n"
"ds_read_b128 v[132:135], v23 offset:33792\n"
"v_mfma_f32_16x16x32_bf16 v[108:111], a[220:223], v[136:139], v[108:111]\n"
"v_mfma_f32_16x16x32_bf16 v[112:115], a[208:211], v[140:143], v[112:115]\n"
"v_mfma_f32_16x16x32_bf16 v[116:119], a[212:215], v[140:143], v[116:119]\n"
"buffer_load_dwordx4 %[vL16], %[sWS], s40 offen offset:3072 sc1 lds\n"
"v_mfma_f32_16x16x32_bf16 v[120:123], a[216:219], v[140:143], v[120:123]\n"
"v_mfma_f32_16x16x32_bf16 v[124:127], a[220:223], v[140:143], v[124:127]\n"
"s_waitcnt lgkmcnt(2)\n"
"v_mfma_f32_16x16x32_bf16 v[96:99], a[224:227], v[144:147], v[96:99]\n"
"ds_read_b128 v[136:139], v23 offset:16384\n"
"v_mfma_f32_16x16x32_bf16 v[100:103], a[228:231], v[144:147], v[100:103]\n"
"v_mfma_f32_16x16x32_bf16 v[104:107], a[232:235], v[144:147], v[104:107]\n"
"ds_read_b128 v[140:143], v23 offset:34816\n"
"v_mfma_f32_16x16x32_bf16 v[108:111], a[236:239], v[144:147], v[108:111]\n"
"v_mfma_f32_16x16x32_bf16 v[112:115], a[224:227], v[148:151], v[112:115]\n"
"v_mfma_f32_16x16x32_bf16 v[116:119], a[228:231], v[148:151], v[116:119]\n"
"s_add_u32 m0, s53, 8192\n"
"s_add_u32 s40, s52, 8192\n"
"buffer_load_dwordx4 %[vL16], %[sWS], s40 offen offset:0 sc1 lds\n"
"v_mfma_f32_16x16x32_bf16 v[120:123], a[232:235], v[148:151], v[120:123]\n"
"v_mfma_f32_16x16x32_bf16 v[124:127], a[236:239], v[148:151], v[124:127]\n"
"s_waitcnt lgkmcnt(2)\n"
"v_mfma_f32_16x16x32_bf16 v[96:99], a[240:243], v[128:131], v[96:99]\n"
"ds_read_b128 v[144:147], v23 offset:17408\n"
"v_mfma_f32_16x16x32_bf16 v[100:103], a[244:247], v[128:131], v[100:103]\n"
"ds_read_b128 v[148:151], v23 offset:35840\n"
"v_mfma_f32_16x16x32_bf16 v[104:107], a[248:251], v[128:131], v[104:107]\n"
"v_mfma_f32_16x16x32_bf16 v[108:111], a[252:255], v[128:131], v[108:111]\n"
"s_add_u32 s52, s52, 36864\n"
"s_add_u32 s53, s53, 36864\n"
"s_cmp_ge_u32 s53, 110592\n"
"s_cselect_b32 s40, 110592, 0\n"
"s_sub_u32 s53, s53, s40\n"
"v_mfma_f32_16x16x32_bf16 v[112:115], a[240:243], v[132:135], v[112:115]\n"
"s_add_u32 s44, s44, 36864\n"
"s_cmp_ge_u32 s44, 110592\n"
"s_cselect_b32 s44, 0, s44\n"
"v_mfma_f32_16x16x32_bf16 v[116:119], a[244:247], v[132:135], v[116:119]\n"
"v_mfma_f32_16x16x32_bf16 v[120:123], a[248:251], v[132:135], v[120:123]\n"
"v_add_u32 v23, s44, %[vL16]\n"
"s_waitcnt lgkmcnt(0)\n"
"s_barrier\n"
"v_mfma_f32_16x16x32_bf16 v[124:127], a[252:255], v[132:135], v[124:127]\n"
"s_waitcnt lgkmcnt(2)\n"
"v_mfma_f32_16x16x32_bf16 v[96:99], v[224:227], v[136:139], v[96:99]\n"
"ds_read_b128 v[128:131], v23 offset:0\n"
"v_mfma_f32_16x16x32_bf16 v[100:103], v[228:231], v[136:139], v[100:103]\n"
"v_mfma_f32_16x16x32_bf16 v[104:107], v[232:235], v[136:139], v[104:107]\n"
"v_mfma_f32_16x16x32_bf16 v[108:111], v[236:239], v[136:139], v[108:111]\n"
"v_mfma_f32_16x16x32_bf16 v[112:115], v[224:227], v[140:143], v[112:115]\n"
"ds_read_b128 v[132:135], v23 offset:18432\n"
"v_mfma_f32_16x16x32_bf16 v[116:119], v[228:231], v[140:143], v[116:119]\n"
"v_mfma_f32_16x16x32_bf16 v[120:123], v[232:235], v[140:143], v[120:123]\n"
"v_mfma_f32_16x16x32_bf16 v[124:127], v[236:239], v[140:143], v[124:127]\n"
"s_waitcnt lgkmcnt(2)\n"
"v_mfma_f32_16x16x32_bf16 v[96:99], v[240:243], v[144:147], v[96:99]\n"
"ds_read_b128 v[136:139], v23 offset:1024\n"
"v_mfma_f32_16x16x32_bf16 v[100:103], v[244:247], v[144:147], v[100:103]\n"
"v_mfma_f32_16x16x32_bf16 v[104:107], v[248:251], v[144:147], v[104:107]\n"
"ds_read_b128 v[140:143], v23 offset:19456\n"
"v_mfma_f32_16x16x32_bf16 v[108:111], v[252:255], v[144:147], v[108:111]\n"
"v_mfma_f32_16x16x32_bf16 v[112:115], v[240:243], v[148:151], v[112:115]\n"
"v_mfma_f32_16x16x32_bf16 v[116:119], v[244:247], v[148:151], v[116:119]\n"
"s_add_u32 s43, s43, 1\n"
"s_cmp_lt_u32 s43, 64\n"
"v_mfma_f32_16x16x32_bf16 v[120:123], v[248:251], v[148:151], v[120:123]\n"
"v_mfma_f32_16x16x32_bf16 v[124:127], v[252:255], v[148:151], v[124:127]\n"
"s_cbranch_scc1 LQ_q_loop\n"
"s_add_u32 s47, s45, 4096\n"
"v_cvt_pk_bf16_f32 v160, v96, v100\n"
"v_cvt_pk_bf16_f32 v161, v97, v101\n"
"v_cvt_pk_bf16_f32 v162, v98, v102\n"
"v_cvt_pk_bf16_f32 v163, v99, v103\n"
"buffer_store_dwordx4 v[160:163], %[vL16], %[sWS], s45 offen offset:0 sc1\n"
"v_cvt_pk_bf16_f32 v164, v104, v108\n"
"v_cvt_pk_bf16_f32 v165, v105, v109\n"
"v_cvt_pk_bf16_f32 v166, v106, v110\n"
"v_cvt_pk_bf16_f32 v167, v107, v111\n"
"buffer_store_dwordx4 v[164:167], %[vL16], %[sWS], s45 offen offset:1024 sc1\n"
"v_cvt_pk_bf16_f32 v168, v112, v116\n"
"v_cvt_pk_bf16_f32 v169, v113, v117\n"
"v_cvt_pk_bf16_f32 v170, v114, v118\n"
"v_cvt_pk_bf16_f32 v171, v115, v119\n"
"buffer_store_dwordx4 v[168:171], %[vL16], %[sWS], s47 offen offset:0 sc1\n"
"v_cvt_pk_bf16_f32 v172, v120, v124\n"
"v_cvt_pk_bf16_f32 v173, v121, v125\n"
"v_cvt_pk_bf16_f32 v174, v122, v126\n"
"v_cvt_pk_bf16_f32 v175, v123, v127\n"
"buffer_store_dwordx4 v[172:175], %[vL16], %[sWS], s47 offen offset:1024 sc1\n"
"s_branch LQ_done\n"
"LQ_q_all:\n"
"v_and_b32 v21, 0x1f0, %[vL16]\n"
"v_lshlrev_b32 v21, 3, v21\n"
"s_lshr_b32 s40, %[sW9K], 13\n"
"s_lshl_b32 s40, s40, 12\n"
"v_add_u32 v21, s40, v21\n"
"v_min_u32 v21, 13184, v21\n"
"s_mov_b32 s40, 1111040\n"
"s_mov_b64 exec, -1\n"
"s_mov_b32 s41, 0\n"
"s_branch LQ_q_ln_go\n"
"LQ_q_ln:\n"
"s_sleep 2\n"
"LQ_q_ln_go:\n"
"buffer_load_dword v20, v21, %[sWS], s40 offen sc1\n"
"s_add_u32 s41, s41, 1\n"
"s_cmp_gt_u32 s41, 60000\n"
"s_cbranch_scc1 LQ_tmo\n"
"s_waitcnt vmcnt(0)\n"
"v_cmp_gt_i32 vcc, 3, v20\n"
"s_cbranch_vccnz LQ_q_ln\n"
"LQ_q_ln_ok:\n"
"s_mov_b64 exec, -1\n"
"s_branch LQ_q_allok\n"
"LQ_v_loop:\n"
"s_waitcnt lgkmcnt(2)\n"
"v_mfma_f32_16x16x32_bf16 v[64:67], v[128:131], a[0:3], 0\n"
"ds_read_b128 v[144:147], v23 offset:2048\n"
"v_mfma_f32_16x16x32_bf16 v[68:71], v[128:131], a[4:7], 0\n"
"v_mfma_f32_16x16x32_bf16 v[72:75], v[128:131], a[8:11], 0\n"
"ds_read_b128 v[148:151], v23 offset:20480\n"
"v_mfma_f32_16x16x32_bf16 v[76:79], v[128:131], a[12:15], 0\n"
"v_mfma_f32_16x16x32_bf16 v[80:83], v[132:135], a[0:3], 0\n"
"s_add_u32 s47, s45, 4096\n"
"v_mfma_f32_16x16x32_bf16 v[84:87], v[132:135], a[4:7], 0\n"
"v_mfma_f32_16x16x32_bf16 v[88:91], v[132:135], a[8:11], 0\n"
"v_mov_b32 v22, s43\n"
"v_mfma_f32_16x16x32_bf16 v[92:95], v[132:135], a[12:15], 0\n"
"s_waitcnt lgkmcnt(2)\n"
"v_mfma_f32_16x16x32_bf16 v[64:67], v[136:139], a[16:19], v[64:67]\n"
"ds_read_b128 v[128:131], v23 offset:3072\n"
"v_mfma_f32_16x16x32_bf16 v[68:71], v[136:139], a[20:23], v[68:71]\n"
"ds_read_b128 v[132:135], v23 offset:21504\n"
"v_mfma_f32_16x16x32_bf16 v[72:75], v[136:139], a[24:27], v[72:75]\n"
"v_cvt_pk_bf16_f32 v160, v96, v97\n"
"v_mfma_f32_16x16x32_bf16 v[76:79], v[136:139], a[28:31], v[76:79]\n"
"v_cvt_pk_bf16_f32 v161, v98, v99\n"
"v_mfma_f32_16x16x32_bf16 v[80:83], v[140:143], a[16:19], v[80:83]\n"
"v_cvt_pk_bf16_f32 v162, v112, v113\n"
"v_mfma_f32_16x16x32_bf16 v[84:87], v[140:143], a[20:23], v[84:87]\n"
"v_cvt_pk_bf16_f32 v163, v114, v115\n"
"v_mfma_f32_16x16x32_bf16 v[88:91], v[140:143], a[24:27], v[88:91]\n"
"buffer_store_dwordx4 v[160:163], %[vL16], %[sWS], s45 offen offset:0 sc1\n"
"v_mfma_f32_16x16x32_bf16 v[92:95], v[140:143], a[28:31], v[92:95]\n"
"s_waitcnt lgkmcnt(2)\n"
"v_mfma_f32_16x16x32_bf16 v[64:67], v[144:147], a[32:35], v[64:67]\n"
"ds_read_b128 v[136:139], v23 offset:4096\n"
"v_mfma_f32_16x16x32_bf16 v[68:71], v[144:147], a[36:39], v[68:71]\n"
"ds_read_b128 v[140:143], v23 offset:22528\n"
"v_mfma_f32_16x16x32_bf16 v[72:75], v[144:147], a[40:43], v[72:75]\n"
"v_cvt_pk_bf16_f32 v164, v100, v101\n"
"v_mfma_f32_16x16x32_bf16 v[76:79], v[144:147], a[44:47], v[76:79]\n"
"v_cvt_pk_bf16_f32 v165, v102, v103\n"
"v_mfma_f32_16x16x32_bf16 v[80:83], v[148:151], a[32:35], v[80:83]\n"
"v_cvt_pk_bf16_f32 v166, v116, v117\n"
"v_mfma_f32_16x16x32_bf16 v[84:87], v[148:151], a[36:39], v[84:87]\n"
"v_cvt_pk_bf16_f32 v167, v118, v119\n"
"v_mfma_f32_16x16x32_bf16 v[88:91], v[148:151], a[40:43], v[88:91]\n"
"buffer_store_dwordx4 v[164:167], %[vL16], %[sWS], s45 offen offset:1024 sc1\n"
"v_mfma_f32_16x16x32_bf16 v[92:95], v[148:151], a[44:47], v[92:95]\n"
"s_waitcnt lgkmcnt(2)\n"
"v_mfma_f32_16x16x32_bf16 v[64:67], v[128:131], a[48:51], v[64:67]\n"
"ds_read_b128 v[144:147], v23 offset:5120\n"
"v_mfma_f32_16x16x32_bf16 v[68:71], v[128:131], a[52:55], v[68:71]\n"
"ds_read_b128 v[148:151], v23 offset:23552\n"
"v_mfma_f32_16x16x32_bf16 v[72:75], v[128:131], a[56:59], v[72:75]\n"
"v_cvt_pk_bf16_f32 v168, v104, v105\n"
"v_mfma_f32_16x16x32_bf16 v[76:79], v[128:131], a[60:63], v[76:79]\n"
"v_cvt_pk_bf16_f32 v169, v106, v107\n"
"v_mfma_f32_16x16x32_bf16 v[80:83], v[132:135], a[48:51], v[80:83]\n"
"v_cvt_pk_bf16_f32 v170, v120, v121\n"
"v_mfma_f32_16x16x32_bf16 v[84:87], v[132:135], a[52:55], v[84:87]\n"
"v_cvt_pk_bf16_f32 v171, v122, v123\n"
"v_mfma_f32_16x16x32_bf16 v[88:91], v[132:135], a[56:59], v[88:91]\n"
"buffer_store_dwordx4 v[168:171], %[vL16], %[sWS], s45 offen offset:2048 sc1\n"
"v_mfma_f32_16x16x32_bf16 v[92:95], v[132:135], a[60:63], v[92:95]\n"
"s_waitcnt lgkmcnt(2)\n"
"v_mfma_f32_16x16x32_bf16 v[64:67], v[136:139], a[64:67], v[64:67]\n"
"ds_read_b128 v[128:131], v23 offset:6144\n"
"v_mfma_f32_16x16x32_bf16 v[68:71], v[136:139], a[68:71], v[68:71]\n"
"ds_read_b128 v[132:135], v23 offset:24576\n"
"v_mfma_f32_16x16x32_bf16 v[72:75], v[136:139], a[72:75], v[72:75]\n"
"v_cvt_pk_bf16_f32 v172, v108, v109\n"
"v_mfma_f32_16x16x32_bf16 v[76:79], v[136:139], a[76:79], v[76:79]\n"
"v_cvt_pk_bf16_f32 v173, v110, v111\n"
"v_mfma_f32_16x16x32_bf16 v[80:83], v[140:143], a[64:67], v[80:83]\n"
"v_cvt_pk_bf16_f32 v174, v124, v125\n"
"v_mfma_f32_16x16x32_bf16 v[84:87], v[140:143], a[68:71], v[84:87]\n"
"v_cvt_pk_bf16_f32 v175, v126, v127\n"
"v_mfma_f32_16x16x32_bf16 v[88:91], v[140:143], a[72:75], v[88:91]\n"
"buffer_store_dwordx4 v[172:175], %[vL16], %[sWS], s45 offen offset:3072 sc1\n"
"v_mfma_f32_16x16x32_bf16 v[92:95], v[140:143], a[76:79], v[92:95]\n"
"s_waitcnt lgkmcnt(2)\n"
"v_mfma_f32_16x16x32_bf16 v[64:67], v[144:147], a[80:83], v[64:67]\n"
"ds_read_b128 v[136:139], v23 offset:7168\n"
"v_mfma_f32_16x16x32_bf16 v[68:71], v[144:147], a[84:87], v[68:71]\n"
"v_mfma_f32_16x16x32_bf16 v[72:75], v[144:147], a[88:91], v[72:75]\n"
"ds_read_b128 v[140:143], v23 offset:25600\n"
"v_mfma_f32_16x16x32_bf16 v[76:79], v[144:147], a[92:95], v[76:79]\n"
"v_mfma_f32_16x16x32_bf16 v[80:83], v[148:151], a[80:83], v[80:83]\n"
"v_mfma_f32_16x16x32_bf16 v[84:87], v[148:151], a[84:87], v[84:87]\n"
"s_cmp_lg_u32 s43, 0\n"
"s_cselect_b32 s40, 786432, 0\n"
"s_add_u32 s45, s45, s40\n"
"v_mfma_f32_16x16x32_bf16 v[88:91], v[148:151], a[88:91], v[88:91]\n"
"v_mfma_f32_16x16x32_bf16 v[92:95], v[148:151], a[92:95], v[92:95]\n"
"s_waitcnt lgkmcnt(2)\n"
"v_mfma_f32_16x16x32_bf16 v[64:67], v[128:131], a[96:99], v[64:67]\n"
"ds_read_b128 v[144:147], v23 offset:8192\n"
"v_mfma_f32_16x16x32_bf16 v[68:71], v[128:131], a[100:103], v[68:71]\n"
"v_mfma_f32_16x16x32_bf16 v[72:75], v[128:131], a[104:107], v[72:75]\n"
"ds_read_b128 v[148:151], v23 offset:26624\n"
"v_mfma_f32_16x16x32_bf16 v[76:79], v[128:131], a[108:111], v[76:79]\n"
"v_mfma_f32_16x16x32_bf16 v[80:83], v[132:135], a[96:99], v[80:83]\n"
"v_mfma_f32_16x16x32_bf16 v[84:87], v[132:135], a[100:103], v[84:87]\n"
"s_add_u32 m0, s53, 0\n"
"s_add_u32 s40, s52, 0\n"
"buffer_load_dwordx4 %[vL16], %[sWS], s40 offen offset:0 sc1 lds\n"
"v_mfma_f32_16x16x32_bf16 v[88:91], v[132:135], a[104:107], v[88:91]\n"
"v_mfma_f32_16x16x32_bf16 v[92:95], v[132:135], a[108:111], v[92:95]\n"
"s_waitcnt lgkmcnt(2)\n"
"v_mfma_f32_16x16x32_bf16 v[64:67], v[136:139], a[112:115], v[64:67]\n"
"ds_read_b128 v[128:131], v23 offset:9216\n"
"v_mfma_f32_16x16x32_bf16 v[68:71], v[136:139], a[116:119], v[68:71]\n"
"v_mfma_f32_16x16x32_bf16 v[72:75], v[136:139], a[120:123], v[72:75]\n"
"ds_read_b128 v[132:135], v23 offset:27648\n"
"v_mfma_f32_16x16x32_bf16 v[76:79], v[136:139], a[124:127], v[76:79]\n"
"v_mfma_f32_16x16x32_bf16 v[80:83], v[140:143], a[112:115], v[80:83]\n"
"v_mfma_f32_16x16x32_bf16 v[84:87], v[140:143], a[116:119], v[84:87]\n"
"buffer_load_dwordx4 %[vL16], %[sWS], s40 offen offset:1024 sc1 lds\n"
"v_mfma_f32_16x16x32_bf16 v[88:91], v[140:143], a[120:123], v[88:91]\n"
"v_mfma_f32_16x16x32_bf16 v[92:95], v[140:143], a[124:127], v[92:95]\n"
"s_waitcnt lgkmcnt(2)\n"
"v_mfma_f32_16x16x32_bf16 v[64:67], v[144:147], a[128:131], v[64:67]\n"
"ds_read_b128 v[136:139], v23 offset:10240\n"
"v_mfma_f32_16x16x32_bf16 v[68:71], v[144:147], a[132:135], v[68:71]\n"
"v_mfma_f32_16x16x32_bf16 v[72:75], v[144:147], a[136:139], v[72:75]\n"
"ds_read_b128 v[140:143], v23 offset:28672\n"
"v_mfma_f32_16x16x32_bf16 v[76:79], v[144:147], a[140:143], v[76:79]\n"
"v_mfma_f32_16x16x32_bf16 v[80:83], v[148:151], a[128:131], v[80:83]\n"
"v_mfma_f32_16x16x32_bf16 v[84:87], v[148:151], a[132:135], v[84:87]\n"
"buffer_load_dwordx4 %[vL16], %[sWS], s40 offen offset:2048 sc1 lds\n"
"v_mfma_f32_16x16x32_bf16 v[88:91], v[148:151], a[136:139], v[88:91]\n"
"v_mfma_f32_16x16x32_bf16 v[92:95], v[148:151], a[140:143], v[92:95]\n"
"s_waitcnt lgkmcnt(2)\n"
"v_mfma_f32_16x16x32_bf16 v[64:67], v[128:131], a[144:147], v[64:67]\n"
"ds_read_b128 v[144:147], v23 offset:11264\n"
"v_mfma_f32_16x16x32_bf16 v[68:71], v[128:131], a[148:151], v[68:71]\n"
"v_mfma_f32_16x16x32_bf16 v[72:75], v[128:131], a[152:155], v[72:75]\n"
"ds_read_b128 v[148:151], v23 offset:29696\n"
"v_mfma_f32_16x16x32_bf16 v[76:79], v[128:131], a[156:159], v[76:79]\n"
"v_mfma_f32_16x16x32_bf16 v[80:83], v[132:135], a[144:147], v[80:83]\n"
"v_mfma_f32_16x16x32_bf16 v[84:87], v[132:135], a[148:151], v[84:87]\n"
"buffer_load_dwordx4 %[vL16], %[sWS], s40 offen offset:3072 sc1 lds\n"
"v_mfma_f32_16x16x32_bf16 v[88:91], v[132:135], a[152:155], v[88:91]\n"
"v_mfma_f32_16x16x32_bf16 v[92:95], v[132:135], a[156:159], v[92:95]\n"
"s_waitcnt lgkmcnt(2)\n"
"v_mfma_f32_16x16x32_bf16 v[64:67], v[136:139], a[160:163], v[64:67]\n"
"ds_read_b128 v[128:131], v23 offset:12288\n"
"v_mfma_f32_16x16x32_bf16 v[68:71], v[136:139], a[164:167], v[68:71]\n"
"v_mfma_f32_16x16x32_bf16 v[72:75], v[136:139], a[168:171], v[72:75]\n"
"ds_read_b128 v[132:135], v23 offset:30720\n"
"v_mfma_f32_16x16x32_bf16 v[76:79], v[136:139], a[172:175], v[76:79]\n"
"v_mfma_f32_16x16x32_bf16 v[80:83], v[140:143], a[160:163], v[80:83]\n"
"v_mfma_f32_16x16x32_bf16 v[84:87], v[140:143], a[164:167], v[84:87]\n"
"s_add_u32 m0, s53, 4096\n"
"s_add_u32 s40, s52, 4096\n"
"buffer_load_dwordx4 %[vL16], %[sWS], s40 offen offset:0 sc1 lds\n"
"v_mfma_f32_16x16x32_bf16 v[88:91], v[140:143], a[168:171], v[88:91]\n"
"v_mfma_f32_16x16x32_bf16 v[92:95], v[140:143], a[172:175], v[92:95]\n"
"s_waitcnt lgkmcnt(2)\n"
"v_mfma_f32_16x16x32_bf16 v[64:67], v[144:147], a[176:179], v[64:67]\n"
"s_waitcnt vmcnt(5)\n"
"buffer_store_dword v22, %[vFL], %[sWS], s46 offen sc1\n"
"v_mfma_f32_16x16x32_bf16 v[68:71], v[144:147], a[180:183], v[68:71]\n"
"v_mfma_f32_16x16x32_bf16 v[72:75], v[144:147], a[184:187], v[72:75]\n"
"ds_read_b128 v[136:139], v23 offset:13312\n"
"v_mfma_f32_16x16x32_bf16 v[76:79], v[144:147], a[188:191], v[76:79]\n"
"v_mfma_f32_16x16x32_bf16 v[80:83], v[148:151], a[176:179], v[80:83]\n"
"ds_read_b128 v[140:143], v23 offset:31744\n"
"v_mfma_f32_16x16x32_bf16 v[84:87], v[148:151], a[180:183], v[84:87]\n"
"v_mfma_f32_16x16x32_bf16 v[88:91], v[148:151], a[184:187], v[88:91]\n"
"buffer_load_dwordx4 %[vL16], %[sWS], s40 offen offset:1024 sc1 lds\n"
"v_mfma_f32_16x16x32_bf16 v[92:95], v[148:151], a[188:191], v[92:95]\n"
"s_waitcnt lgkmcnt(2)\n"
"v_mfma_f32_16x16x32_bf16 v[64:67], v[128:131], a[192:195], v[64:67]\n"
"ds_read_b128 v[144:147], v23 offset:14336\n"
"v_mfma_f32_16x16x32_bf16 v[68:71], v[128:131], a[196:199], v[68:71]\n"
"v_mfma_f32_16x16x32_bf16 v[72:75], v[128:131], a[200:203], v[72:75]\n"
"ds_read_b128 v[148:151], v23 offset:32768\n"
"v_mfma_f32_16x16x32_bf16 v[76:79], v[128:131], a[204:207], v[76:79]\n"
"v_mfma_f32_16x16x32_bf16 v[80:83], v[132:135], a[192:195], v[80:83]\n"
"v_mfma_f32_16x16x32_bf16 v[84:87], v[132:135], a[196:199], v[84:87]\n"
"buffer_load_dwordx4 %[vL16], %[sWS], s40 offen offset:2048 sc1 lds\n"
"v_mfma_f32_16x16x32_bf16 v[88:91], v[132:135], a[200:203], v[88:91]\n"
"v_mfma_f32_16x16x32_bf16 v[92:95], v[132:135], a[204:207], v[92:95]\n"
"s_waitcnt lgkmcnt(2)\n"
"v_mfma_f32_16x16x32_bf16 v[64:67], v[136:139], a[208:211], v[64:67]\n"
"ds_read_b128 v[128:131], v23 offset:15360\n"
"v_mfma_f32_16x16x32_bf16 v[68:71], v[136:139], a[212:215], v[68:71]\n"
"v_mfma_f32_16x16x32_bf16 v[72:75], v[136:139], a[216:219], v[72:75]\n"
"ds_read_b128 v[132:135], v23 offset:33792\n"
"v_mfma_f32_16x16x32_bf16 v[76:79], v[136:139], a[220:223], v[76:79]\n"
"v_mfma_f32_16x16x32_bf16 v[80:83], v[140:143], a[208:211], v[80:83]\n"
"v_mfma_f32_16x16x32_bf16 v[84:87], v[140:143], a[212:215], v[84:87]\n"
"buffer_load_dwordx4 %[vL16], %[sWS], s40 offen offset:3072 sc1 lds\n"
"v_mfma_f32_16x16x32_bf16 v[88:91], v[140:143], a[216:219], v[88:91]\n"
"v_mfma_f32_16x16x32_bf16 v[92:95], v[140:143], a[220:223], v[92:95]\n"
"s_waitcnt lgkmcnt(2)\n"
"v_mfma_f32_16x16x32_bf16 v[64:67], v[144:147], a[224:227], v[64:67]\n"
"ds_read_b128 v[136:139], v23 offset:16384\n"
"v_mfma_f32_16x16x32_bf16 v[68:71], v[144:147], a[228:231], v[68:71]\n"
"v_mfma_f32_16x16x32_bf16 v[72:75], v[144:147], a[232:235], v[72:75]\n"
"ds_read_b128 v[140:143], v23 offset:34816\n"
"v_mfma_f32_16x16x32_bf16 v[76:79], v[144:147], a[236:239], v[76:79]\n"
"v_mfma_f32_16x16x32_bf16 v[80:83], v[148:151], a[224:227], v[80:83]\n"
"v_mfma_f32_16x16x32_bf16 v[84:87], v[148:151], a[228:231], v[84:87]\n"
"s_add_u32 m0, s53, 8192\n"
"s_add_u32 s40, s52, 8192\n"
"buffer_load_dwordx4 %[vL16], %[sWS], s40 offen offset:0 sc1 lds\n"
"v_mfma_f32_16x16x32_bf16 v[88:91], v[148:151], a[232:235], v[88:91]\n"
"v_mfma_f32_16x16x32_bf16 v[92:95], v[148:151], a[236:239], v[92:95]\n"
"s_waitcnt lgkmcnt(2)\n"
"v_mfma_f32_16x16x32_bf16 v[64:67], v[128:131], a[240:243], v[64:67]\n"
"ds_read_b128 v[144:147], v23 offset:17408\n"
"v_mfma_f32_16x16x32_bf16 v[68:71], v[128:131], a[244:247], v[68:71]\n"
"ds_read_b128 v[148:151], v23 offset:35840\n"
"v_mfma_f32_16x16x32_bf16 v[72:75], v[128:131], a[248:251], v[72:75]\n"
"v_mfma_f32_16x16x32_bf16 v[76:79], v[128:131], a[252:255], v[76:79]\n"
"s_add_u32 s52, s52, 36864\n"
"s_add_u32 s53, s53, 36864\n"
"s_cmp_ge_u32 s53, 110592\n"
"s_cselect_b32 s40, 110592, 0\n"
"s_sub_u32 s53, s53, s40\n"
"v_mfma_f32_16x16x32_bf16 v[80:83], v[132:135], a[240:243], v[80:83]\n"
"s_add_u32 s44, s44, 36864\n"
"s_cmp_ge_u32 s44, 110592\n"
"s_cselect_b32 s44, 0, s44\n"
"v_mfma_f32_16x16x32_bf16 v[84:87], v[132:135], a[244:247], v[84:87]\n"
"v_mfma_f32_16x16x32_bf16 v[88:91], v[132:135], a[248:251], v[88:91]\n"
"v_add_u32 v23, s44, %[vL16]\n"
"s_waitcnt lgkmcnt(0)\n"
"s_barrier\n"
"v_mfma_f32_16x16x32_bf16 v[92:95], v[132:135], a[252:255], v[92:95]\n"
"s_waitcnt lgkmcnt(2)\n"
"v_mfma_f32_16x16x32_bf16 v[64:67], v[136:139], v[224:227], v[64:67]\n"
"ds_read_b128 v[128:131], v23 offset:0\n"
"v_mfma_f32_16x16x32_bf16 v[68:71], v[136:139], v[228:231], v[68:71]\n"
"v_mfma_f32_16x16x32_bf16 v[72:75], v[136:139], v[232:235], v[72:75]\n"
"v_mfma_f32_16x16x32_bf16 v[76:79], v[136:139], v[236:239], v[76:79]\n"
"v_mfma_f32_16x16x32_bf16 v[80:83], v[140:143], v[224:227], v[80:83]\n"
"ds_read_b128 v[132:135], v23 offset:18432\n"
"v_mfma_f32_16x16x32_bf16 v[84:87], v[140:143], v[228:231], v[84:87]\n"
"v_mfma_f32_16x16x32_bf16 v[88:91], v[140:143], v[232:235], v[88:91]\n"
"v_mfma_f32_16x16x32_bf16 v[92:95], v[140:143], v[236:239], v[92:95]\n"
"s_waitcnt lgkmcnt(2)\n"
"v_mfma_f32_16x16x32_bf16 v[64:67], v[144:147], v[240:243], v[64:67]\n"
"ds_read_b128 v[136:139], v23 offset:1024\n"
"v_mfma_f32_16x16x32_bf16 v[68:71], v[144:147], v[244:247], v[68:71]\n"
"v_mfma_f32_16x16x32_bf16 v[72:75], v[144:147], v[248:251], v[72:75]\n"
"ds_read_b128 v[140:143], v23 offset:19456\n"
"v_mfma_f32_16x16x32_bf16 v[76:79], v[144:147], v[252:255], v[76:79]\n"
"v_mfma_f32_16x16x32_bf16 v[80:83], v[148:151], v[240:243], v[80:83]\n"
"v_mfma_f32_16x16x32_bf16 v[84:87], v[148:151], v[244:247], v[84:87]\n"
"s_add_u32 s43, s43, 1\n"
"s_cmp_lt_u32 s43, 64\n"
"v_mfma_f32_16x16x32_bf16 v[88:91], v[148:151], v[248:251], v[88:91]\n"
"v_mfma_f32_16x16x32_bf16 v[92:95], v[148:151], v[252:255], v[92:95]\n"
"s_waitcnt lgkmcnt(2)\n"
"v_mfma_f32_16x16x32_bf16 v[96:99], v[128:131], a[0:3], 0\n"
"ds_read_b128 v[144:147], v23 offset:2048\n"
"v_mfma_f32_16x16x32_bf16 v[100:103], v[128:131], a[4:7], 0\n"
"v_mfma_f32_16x16x32_bf16 v[104:107], v[128:131], a[8:11], 0\n"
"ds_read_b128 v[148:151], v23 offset:20480\n"
"v_mfma_f32_16x16x32_bf16 v[108:111], v[128:131], a[12:15], 0\n"
"v_mfma_f32_16x16x32_bf16 v[112:115], v[132:135], a[0:3], 0\n"
"s_add_u32 s47, s45, 4096\n"
"v_mfma_f32_16x16x32_bf16 v[116:119], v[132:135], a[4:7], 0\n"
"v_mfma_f32_16x16x32_bf16 v[120:123], v[132:135], a[8:11], 0\n"
"v_mov_b32 v22, s43\n"
"v_mfma_f32_16x16x32_bf16 v[124:127], v[132:135], a[12:15], 0\n"
"s_waitcnt lgkmcnt(2)\n"
"v_mfma_f32_16x16x32_bf16 v[96:99], v[136:139], a[16:19], v[96:99]\n"
"ds_read_b128 v[128:131], v23 offset:3072\n"
"v_mfma_f32_16x16x32_bf16 v[100:103], v[136:139], a[20:23], v[100:103]\n"
"ds_read_b128 v[132:135], v23 offset:21504\n"
"v_mfma_f32_16x16x32_bf16 v[104:107], v[136:139], a[24:27], v[104:107]\n"
"v_cvt_pk_bf16_f32 v160, v64, v65\n"
"v_mfma_f32_16x16x32_bf16 v[108:111], v[136:139], a[28:31], v[108:111]\n"
"v_cvt_pk_bf16_f32 v161, v66, v67\n"
"v_mfma_f32_16x16x32_bf16 v[112:115], v[140:143], a[16:19], v[112:115]\n"
"v_cvt_pk_bf16_f32 v162, v80, v81\n"
"v_mfma_f32_16x16x32_bf16 v[116:119], v[140:143], a[20:23], v[116:119]\n"
"v_cvt_pk_bf16_f32 v163, v82, v83\n"
"v_mfma_f32_16x16x32_bf16 v[120:123], v[140:143], a[24:27], v[120:123]\n"
"buffer_store_dwordx4 v[160:163], %[vL16], %[sWS], s45 offen offset:0 sc1\n"
"v_mfma_f32_16x16x32_bf16 v[124:127], v[140:143], a[28:31], v[124:127]\n"
"s_waitcnt lgkmcnt(2)\n"
"v_mfma_f32_16x16x32_bf16 v[96:99], v[144:147], a[32:35], v[96:99]\n"
"ds_read_b128 v[136:139], v23 offset:4096\n"
"v_mfma_f32_16x16x32_bf16 v[100:103], v[144:147], a[36:39], v[100:103]\n"
"ds_read_b128 v[140:143], v23 offset:22528\n"
"v_mfma_f32_16x16x32_bf16 v[104:107], v[144:147], a[40:43], v[104:107]\n"
"v_cvt_pk_bf16_f32 v164, v68, v69\n"
"v_mfma_f32_16x16x32_bf16 v[108:111], v[144:147], a[44:47], v[108:111]\n"
"v_cvt_pk_bf16_f32 v165, v70, v71\n"
"v_mfma_f32_16x16x32_bf16 v[112:115], v[148:151], a[32:35], v[112:115]\n"
"v_cvt_pk_bf16_f32 v166, v84, v85\n"
"v_mfma_f32_16x16x32_bf16 v[116:119], v[148:151], a[36:39], v[116:119]\n"
"v_cvt_pk_bf16_f32 v167, v86, v87\n"
"v_mfma_f32_16x16x32_bf16 v[120:123], v[148:151], a[40:43], v[120:123]\n"
"buffer_store_dwordx4 v[164:167], %[vL16], %[sWS], s45 offen offset:1024 sc1\n"
"v_mfma_f32_16x16x32_bf16 v[124:127], v[148:151], a[44:47], v[124:127]\n"
"s_waitcnt lgkmcnt(2)\n"
"v_mfma_f32_16x16x32_bf16 v[96:99], v[128:131], a[48:51], v[96:99]\n"
"ds_read_b128 v[144:147], v23 offset:5120\n"
"v_mfma_f32_16x16x32_bf16 v[100:103], v[128:131], a[52:55], v[100:103]\n"
"ds_read_b128 v[148:151], v23 offset:23552\n"
"v_mfma_f32_16x16x32_bf16 v[104:107], v[128:131], a[56:59], v[104:107]\n"
"v_cvt_pk_bf16_f32 v168, v72, v73\n"
"v_mfma_f32_16x16x32_bf16 v[108:111], v[128:131], a[60:63], v[108:111]\n"
"v_cvt_pk_bf16_f32 v169, v74, v75\n"
"v_mfma_f32_16x16x32_bf16 v[112:115], v[132:135], a[48:51], v[112:115]\n"
"v_cvt_pk_bf16_f32 v170, v88, v89\n"
"v_mfma_f32_16x16x32_bf16 v[116:119], v[132:135], a[52:55], v[116:119]\n"
"v_cvt_pk_bf16_f32 v171, v90, v91\n"
"v_mfma_f32_16x16x32_bf16 v[120:123], v[132:135], a[56:59], v[120:123]\n"
"buffer_store_dwordx4 v[168:171], %[vL16], %[sWS], s45 offen offset:2048 sc1\n"
"v_mfma_f32_16x16x32_bf16 v[124:127], v[132:135], a[60:63], v[124:127]\n"
"s_waitcnt lgkmcnt(2)\n"
"v_mfma_f32_16x16x32_bf16 v[96:99], v[136:139], a[64:67], v[96:99]\n"
"ds_read_b128 v[128:131], v23 offset:6144\n"
"v_mfma_f32_16x16x32_bf16 v[100:103], v[136:139], a[68:71], v[100:103]\n"
"ds_read_b128 v[132:135], v23 offset:24576\n"
"v_mfma_f32_16x16x32_bf16 v[104:107], v[136:139], a[72:75], v[104:107]\n"
"v_cvt_pk_bf16_f32 v172, v76, v77\n"
"v_mfma_f32_16x16x32_bf16 v[108:111], v[136:139], a[76:79], v[108:111]\n"
"v_cvt_pk_bf16_f32 v173, v78, v79\n"
"v_mfma_f32_16x16x32_bf16 v[112:115], v[140:143], a[64:67], v[112:115]\n"
"v_cvt_pk_bf16_f32 v174, v92, v93\n"
"v_mfma_f32_16x16x32_bf16 v[116:119], v[140:143], a[68:71], v[116:119]\n"
"v_cvt_pk_bf16_f32 v175, v94, v95\n"
"v_mfma_f32_16x16x32_bf16 v[120:123], v[140:143], a[72:75], v[120:123]\n"
"buffer_store_dwordx4 v[172:175], %[vL16], %[sWS], s45 offen offset:3072 sc1\n"
"v_mfma_f32_16x16x32_bf16 v[124:127], v[140:143], a[76:79], v[124:127]\n"
"s_waitcnt lgkmcnt(2)\n"
"v_mfma_f32_16x16x32_bf16 v[96:99], v[144:147], a[80:83], v[96:99]\n"
"ds_read_b128 v[136:139], v23 offset:7168\n"
"v_mfma_f32_16x16x32_bf16 v[100:103], v[144:147], a[84:87], v[100:103]\n"
"v_mfma_f32_16x16x32_bf16 v[104:107], v[144:147], a[88:91], v[104:107]\n"
"ds_read_b128 v[140:143], v23 offset:25600\n"
"v_mfma_f32_16x16x32_bf16 v[108:111], v[144:147], a[92:95], v[108:111]\n"
"v_mfma_f32_16x16x32_bf16 v[112:115], v[148:151], a[80:83], v[112:115]\n"
"s_cmp_lg_u32 s43, 0\n"
"s_cselect_b32 s40, 786432, 0\n"
"s_add_u32 s45, s45, s40\n"
"v_mfma_f32_16x16x32_bf16 v[116:119], v[148:151], a[84:87], v[116:119]\n"
"v_mfma_f32_16x16x32_bf16 v[120:123], v[148:151], a[88:91], v[120:123]\n"
"s_cmp_eq_u32 s43, 5\n"
"s_cbranch_scc1 LQ_v_all\n"
"LQ_v_allok:\n"
"v_mfma_f32_16x16x32_bf16 v[124:127], v[148:151], a[92:95], v[124:127]\n"
"s_waitcnt lgkmcnt(2)\n"
"v_mfma_f32_16x16x32_bf16 v[96:99], v[128:131], a[96:99], v[96:99]\n"
"ds_read_b128 v[144:147], v23 offset:8192\n"
"v_mfma_f32_16x16x32_bf16 v[100:103], v[128:131], a[100:103], v[100:103]\n"
"v_mfma_f32_16x16x32_bf16 v[104:107], v[128:131], a[104:107], v[104:107]\n"
"ds_read_b128 v[148:151], v23 offset:26624\n"
"v_mfma_f32_16x16x32_bf16 v[108:111], v[128:131], a[108:111], v[108:111]\n"
"v_mfma_f32_16x16x32_bf16 v[112:115], v[132:135], a[96:99], v[112:115]\n"
"v_mfma_f32_16x16x32_bf16 v[116:119], v[132:135], a[100:103], v[116:119]\n"
"s_add_u32 m0, s53, 0\n"
"s_add_u32 s40, s52, 0\n"
"buffer_load_dwordx4 %[vL16], %[sWS], s40 offen offset:0 sc1 lds\n"
"v_mfma_f32_16x16x32_bf16 v[120:123], v[132:135], a[104:107], v[120:123]\n"
"v_mfma_f32_16x16x32_bf16 v[124:127], v[132:135], a[108:111], v[124:127]\n"
"s_waitcnt lgkmcnt(2)\n"
"v_mfma_f32_16x16x32_bf16 v[96:99], v[136:139], a[112:115], v[96:99]\n"
"ds_read_b128 v[128:131], v23 offset:9216\n"
"v_mfma_f32_16x16x32_bf16 v[100:103], v[136:139], a[116:119], v[100:103]\n"
"v_mfma_f32_16x16x32_bf16 v[104:107], v[136:139], a[120:123], v[104:107]\n"
"ds_read_b128 v[132:135], v23 offset:27648\n"
"v_mfma_f32_16x16x32_bf16 v[108:111], v[136:139], a[124:127], v[108:111]\n"
"v_mfma_f32_16x16x32_bf16 v[112:115], v[140:143], a[112:115], v[112:115]\n"
"v_mfma_f32_16x16x32_bf16 v[116:119], v[140:143], a[116:119], v[116:119]\n"
"buffer_load_dwordx4 %[vL16], %[sWS], s40 offen offset:1024 sc1 lds\n"
"v_mfma_f32_16x16x32_bf16 v[120:123], v[140:143], a[120:123], v[120:123]\n"
"v_mfma_f32_16x16x32_bf16 v[124:127], v[140:143], a[124:127], v[124:127]\n"
"s_waitcnt lgkmcnt(2)\n"
"v_mfma_f32_16x16x32_bf16 v[96:99], v[144:147], a[128:131], v[96:99]\n"
"ds_read_b128 v[136:139], v23 offset:10240\n"
"v_mfma_f32_16x16x32_bf16 v[100:103], v[144:147], a[132:135], v[100:103]\n"
"v_mfma_f32_16x16x32_bf16 v[104:107], v[144:147], a[136:139], v[104:107]\n"
"ds_read_b128 v[140:143], v23 offset:28672\n"
"v_mfma_f32_16x16x32_bf16 v[108:111], v[144:147], a[140:143], v[108:111]\n"
"v_mfma_f32_16x16x32_bf16 v[112:115], v[148:151], a[128:131], v[112:115]\n"
"v_mfma_f32_16x16x32_bf16 v[116:119], v[148:151], a[132:135], v[116:119]\n"
"buffer_load_dwordx4 %[vL16], %[sWS], s40 offen offset:2048 sc1 lds\n"
"v_mfma_f32_16x16x32_bf16 v[120:123], v[148:151], a[136:139], v[120:123]\n"
"v_mfma_f32_16x16x32_bf16 v[124:127], v[148:151], a[140:143], v[124:127]\n"
"s_waitcnt lgkmcnt(2)\n"
"v_mfma_f32_16x16x32_bf16 v[96:99], v[128:131], a[144:147], v[96:99]\n"
"ds_read_b128 v[144:147], v23 offset:11264\n"
"v_mfma_f32_16x16x32_bf16 v[100:103], v[128:131], a[148:151], v[100:103]\n"
"v_mfma_f32_16x16x32_bf16 v[104:107], v[128:131], a[152:155], v[104:107]\n"
"ds_read_b128 v[148:151], v23 offset:29696\n"
"v_mfma_f32_16x16x32_bf16 v[108:111], v[128:131], a[156:159], v[108:111]\n"
"v_mfma_f32_16x16x32_bf16 v[112:115], v[132:135], a[144:147], v[112:115]\n"
"v_mfma_f32_16x16x32_bf16 v[116:119], v[132:135], a[148:151], v[116:119]\n"
"buffer_load_dwordx4 %[vL16], %[sWS], s40 offen offset:3072 sc1 lds\n"
"v_mfma_f32_16x16x32_bf16 v[120:123], v[132:135], a[152:155], v[120:123]\n"
"v_mfma_f32_16x16x32_bf16 v[124:127], v[132:135], a[156:159], v[124:127]\n"
"s_waitcnt lgkmcnt(2)\n"
"v_mfma_f32_16x16x32_bf16 v[96:99], v[136:139], a[160:163], v[96:99]\n"
"ds_read_b128 v[128:131], v23 offset:12288\n"
"v_mfma_f32_16x16x32_bf16 v[100:103], v[136:139], a[164:167], v[100:103]\n"
"v_mfma_f32_16x16x32_bf16 v[104:107], v[136:139], a[168:171], v[104:107]\n"
"ds_read_b128 v[132:135], v23 offset:30720\n"
"v_mfma_f32_16x16x32_bf16 v[108:111], v[136:139], a[172:175], v[108:111]\n"
"v_mfma_f32_16x16x32_bf16 v[112:115], v[140:143], a[160:163], v[112:115]\n"
"v_mfma_f32_16x16x32_bf16 v[116:119], v[140:143], a[164:167], v[116:119]\n"
"s_add_u32 m0, s53, 4096\n"
"s_add_u32 s40, s52, 4096\n"
"buffer_load_dwordx4 %[vL16], %[sWS], s40 offen offset:0 sc1 lds\n"
"v_mfma_f32_16x16x32_bf16 v[120:123], v[140:143], a[168:171], v[120:123]\n"
"v_mfma_f32_16x16x32_bf16 v[124:127], v[140:143], a[172:175], v[124:127]\n"
"s_waitcnt lgkmcnt(2)\n"
"v_mfma_f32_16x16x32_bf16 v[96:99], v[144:147], a[176:179], v[96:99]\n"
"s_waitcnt vmcnt(5)\n"
"buffer_store_dword v22, %[vFL], %[sWS], s46 offen sc1\n"
"v_mfma_f32_16x16x32_bf16 v[100:103], v[144:147], a[180:183], v[100:103]\n"
"v_mfma_f32_16x16x32_bf16 v[104:107], v[144:147], a[184:187], v[104:107]\n"
"ds_read_b128 v[136:139], v23 offset:13312\n"
"v_mfma_f32_16x16x32_bf16 v[108:111], v[144:147], a[188:191], v[108:111]\n"
"v_mfma_f32_16x16x32_bf16 v[112:115], v[148:151], a[176:179], v[112:115]\n"
"ds_read_b128 v[140:143], v23 offset:31744\n"
"v_mfma_f32_16x16x32_bf16 v[116:119], v[148:151], a[180:183], v[116:119]\n"
"v_mfma_f32_16x16x32_bf16 v[120:123], v[148:151], a[184:187], v[120:123]\n"
"buffer_load_dwordx4 %[vL16], %[sWS], s40 offen offset:1024 sc1 lds\n"
"v_mfma_f32_16x16x32_bf16 v[124:127], v[148:151], a[188:191], v[124:127]\n"
"s_waitcnt lgkmcnt(2)\n"
"v_mfma_f32_16x16x32_bf16 v[96:99], v[128:131], a[192:195], v[96:99]\n"
"ds_read_b128 v[144:147], v23 offset:14336\n"
"v_mfma_f32_16x16x32_bf16 v[100:103], v[128:131], a[196:199], v[100:103]\n"
"v_mfma_f32_16x16x32_bf16 v[104:107], v[128:131], a[200:203], v[104:107]\n"
"ds_read_b128 v[148:151], v23 offset:32768\n"
"v_mfma_f32_16x16x32_bf16 v[108:111], v[128:131], a[204:207], v[108:111]\n"
"v_mfma_f32_16x16x32_bf16 v[112:115], v[132:135], a[192:195], v[112:115]\n"
"v_mfma_f32_16x16x32_bf16 v[116:119], v[132:135], a[196:199], v[116:119]\n"
"buffer_load_dwordx4 %[vL16], %[sWS], s40 offen offset:2048 sc1 lds\n"
"v_mfma_f32_16x16x32_bf16 v[120:123], v[132:135], a[200:203], v[120:123]\n"
"v_mfma_f32_16x16x32_bf16 v[124:127], v[132:135], a[204:207], v[124:127]\n"
"s_waitcnt lgkmcnt(2)\n"
"v_mfma_f32_16x16x32_bf16 v[96:99], v[136:139], a[208:211], v[96:99]\n"
"ds_read_b128 v[128:131], v23 offset:15360\n"
"v_mfma_f32_16x16x32_bf16 v[100:103], v[136:139], a[212:215], v[100:103]\n"
"v_mfma_f32_16x16x32_bf16 v[104:107], v[136:139], a[216:219], v[104:107]\n"
"ds_read_b128 v[132:135], v23 offset:33792\n"
"v_mfma_f32_16x16x32_bf16 v[108:111], v[136:139], a[220:223], v[108:111]\n"
"v_mfma_f32_16x16x32_bf16 v[112:115], v[140:143], a[208:211], v[112:115]\n"
"v_mfma_f32_16x16x32_bf16 v[116:119], v[140:143], a[212:215], v[116:119]\n"
"buffer_load_dwordx4 %[vL16], %[sWS], s40 offen offset:3072 sc1 lds\n"
"v_mfma_f32_16x16x32_bf16 v[120:123], v[140:143], a[216:219], v[120:123]\n"
"v_mfma_f32_16x16x32_bf16 v[124:127], v[140:143], a[220:223], v[124:127]\n"
"s_waitcnt lgkmcnt(2)\n"
"v_mfma_f32_16x16x32_bf16 v[96:99], v[144:147], a[224:227], v[96:99]\n"
"ds_read_b128 v[136:139], v23 offset:16384\n"
"v_mfma_f32_16x16x32_bf16 v[100:103], v[144:147], a[228:231], v[100:103]\n"
"v_mfma_f32_16x16x32_bf16 v[104:107], v[144:147], a[232:235], v[104:107]\n"
"ds_read_b128 v[140:143], v23 offset:34816\n"
"v_mfma_f32_16x16x32_bf16 v[108:111], v[144:147], a[236:239], v[108:111]\n"
"v_mfma_f32_16x16x32_bf16 v[112:115], v[148:151], a[224:227], v[112:115]\n"
"v_mfma_f32_16x16x32_bf16 v[116:119], v[148:151], a[228:231], v[116:119]\n"
"s_add_u32 m0, s53, 8192\n"
"s_add_u32 s40, s52, 8192\n"
"buffer_load_dwordx4 %[vL16], %[sWS], s40 offen offset:0 sc1 lds\n"
"v_mfma_f32_16x16x32_bf16 v[120:123], v[148:151], a[232:235], v[120:123]\n"
"v_mfma_f32_16x16x32_bf16 v[124:127], v[148:151], a[236:239], v[124:127]\n"
"s_waitcnt lgkmcnt(2)\n"
"v_mfma_f32_16x16x32_bf16 v[96:99], v[128:131], a[240:243], v[96:99]\n"
"ds_read_b128 v[144:147], v23 offset:17408\n"
"v_mfma_f32_16x16x32_bf16 v[100:103], v[128:131], a[244:247], v[100:103]\n"
"ds_read_b128 v[148:151], v23 offset:35840\n"
"v_mfma_f32_16x16x32_bf16 v[104:107], v[128:131], a[248:251], v[104:107]\n"
"v_mfma_f32_16x16x32_bf16 v[108:111], v[128:131], a[252:255], v[108:111]\n"
"s_add_u32 s52, s52, 36864\n"
"s_add_u32 s53, s53, 36864\n"
"s_cmp_ge_u32 s53, 110592\n"
"s_cselect_b32 s40, 110592, 0\n"
"s_sub_u32 s53, s53, s40\n"
"v_mfma_f32_16x16x32_bf16 v[112:115], v[132:135], a[240:243], v[112:115]\n"
"s_add_u32 s44, s44, 36864\n"
"s_cmp_ge_u32 s44, 110592\n"
"s_cselect_b32 s44, 0, s44\n"
"v_mfma_f32_16x16x32_bf16 v[116:119], v[132:135], a[244:247], v[116:119]\n"
"v_mfma_f32_16x16x32_bf16 v[120:123], v[132:135], a[248:251], v[120:123]\n"
"v_add_u32 v23, s44, %[vL16]\n"
"s_waitcnt lgkmcnt(0)\n"
"s_barrier\n"
"v_mfma_f32_16x16x32_bf16 v[124:127], v[132:135], a[252:255], v[124:127]\n"
"s_waitcnt lgkmcnt(2)\n"
"v_mfma_f32_16x16x32_bf16 v[96:99], v[136:139], v[224:227], v[96:99]\n"
"ds_read_b128 v[128:131], v23 offset:0\n"
"v_mfma_f32_16x16x32_bf16 v[100:103], v[136:139], v[228:231], v[100:103]\n"
"v_mfma_f32_16x16x32_bf16 v[104:107], v[136:139], v[232:235], v[104:107]\n"
"v_mfma_f32_16x16x32_bf16 v[108:111], v[136:139], v[236:239], v[108:111]\n"
"v_mfma_f32_16x16x32_bf16 v[112:115], v[140:143], v[224:227], v[112:115]\n"
"ds_read_b128 v[132:135], v23 offset:18432\n"
"v_mfma_f32_16x16x32_bf16 v[116:119], v[140:143], v[228:231], v[116:119]\n"
"v_mfma_f32_16x16x32_bf16 v[120:123], v[140:143], v[232:235], v[120:123]\n"
"v_mfma_f32_16x16x32_bf16 v[124:127], v[140:143], v[236:239], v[124:127]\n"
"s_waitcnt lgkmcnt(2)\n"
"v_mfma_f32_16x16x32_bf16 v[96:99], v[144:147], v[240:243], v[96:99]\n"
"ds_read_b128 v[136:139], v23 offset:1024\n"
"v_mfma_f32_16x16x32_bf16 v[100:103], v[144:147], v[244:247], v[100:103]\n"
"v_mfma_f32_16x16x32_bf16 v[104:107], v[144:147], v[248:251], v[104:107]\n"
"ds_read_b128 v[140:143], v23 offset:19456\n"
"v_mfma_f32_16x16x32_bf16 v[108:111], v[144:147], v[252:255], v[108:111]\n"
"v_mfma_f32_16x16x32_bf16 v[112:115], v[148:151], v[240:243], v[112:115]\n"
"v_mfma_f32_16x16x32_bf16 v[116:119], v[148:151], v[244:247], v[116:119]\n"
"s_add_u32 s43, s43, 1\n"
"s_cmp_lt_u32 s43, 64\n"
"v_mfma_f32_16x16x32_bf16 v[120:123], v[148:151], v[248:251], v[120:123]\n"
"v_mfma_f32_16x16x32_bf16 v[124:127], v[148:151], v[252:255], v[124:127]\n"
"s_cbranch_scc1 LQ_v_loop\n"
"s_add_u32 s47, s45, 4096\n"
"v_cvt_pk_bf16_f32 v160, v96, v97\n"
"v_cvt_pk_bf16_f32 v161, v98, v99\n"
"v_cvt_pk_bf16_f32 v162, v112, v113\n"
"v_cvt_pk_bf16_f32 v163, v114, v115\n"
"buffer_store_dwordx4 v[160:163], %[vL16], %[sWS], s45 offen offset:0 sc1\n"
"v_cvt_pk_bf16_f32 v164, v100, v101\n"
"v_cvt_pk_bf16_f32 v165, v102, v103\n"
"v_cvt_pk_bf16_f32 v166, v116, v117\n"
"v_cvt_pk_bf16_f32 v167, v118, v119\n"
"buffer_store_dwordx4 v[164:167], %[vL16], %[sWS], s45 offen offset:1024 sc1\n"
"v_cvt_pk_bf16_f32 v168, v104, v105\n"
"v_cvt_pk_bf16_f32 v169, v106, v107\n"
"v_cvt_pk_bf16_f32 v170, v120, v121\n"
"v_cvt_pk_bf16_f32 v171, v122, v123\n"
"buffer_store_dwordx4 v[168:171], %[vL16], %[sWS], s45 offen offset:2048 sc1\n"
"v_cvt_pk_bf16_f32 v172, v108, v109\n"
"v_cvt_pk_bf16_f32 v173, v110, v111\n"
"v_cvt_pk_bf16_f32 v174, v124, v125\n"
"v_cvt_pk_bf16_f32 v175, v126, v127\n"
"buffer_store_dwordx4 v[172:175], %[vL16], %[sWS], s45 offen offset:3072 sc1\n"
"s_branch LQ_done\n"
"LQ_v_all:\n"
"v_and_b32 v21, 0x1f0, %[vL16]\n"
"v_lshlrev_b32 v21, 3, v21\n"
"s_lshr_b32 s40, %[sW9K], 13\n"
"s_lshl_b32 s40, s40, 12\n"
"v_add_u32 v21, s40, v21\n"
"v_min_u32 v21, 13184, v21\n"
"s_mov_b32 s40, 1111040\n"
"s_mov_b64 exec, -1\n"
"s_mov_b32 s41, 0\n"
"s_branch LQ_v_ln_go\n"
"LQ_v_ln:\n"
"s_sleep 2\n"
"LQ_v_ln_go:\n"
"buffer_load_dword v20, v21, %[sWS], s40 offen sc1\n"
"s_add_u32 s41, s41, 1\n"
"s_cmp_gt_u32 s41, 60000\n"
"s_cbranch_scc1 LQ_tmo\n"
"s_waitcnt vmcnt(0)\n"
"v_cmp_gt_i32 vcc, 3, v20\n"
"s_cbranch_vccnz LQ_v_ln\n"
"LQ_v_ln_ok:\n"
"s_mov_b64 exec, -1\n"
"s_branch LQ_v_allok\n"
"LQ_tmo:\n"
"s_mov_b64 exec, 1\n"
"v_mov_b32 v20, 3\n"
"s_mov_b32 s40, 1065088\n"
"buffer_store_dword v20, off, %[sWS], s40 sc1\n"
"s_endpgm\n"
"LQ_done:\n"
"s_waitcnt vmcnt(0)\n"
"v_mov_b32 v22, 64\n"
"buffer_store_dword v22, %[vFL], %[sWS], s46 offen sc1\n"
        :
        : [sWS] "s"(sWS), [sW] "s"(sW), [sWST] "s"(sWST), [sW9K] "s"(sW9K), [sISV] "s"(sISV), [sHB] "s"(hb_off()), [sOUT] "s"(sOUT),
          [vG0] "v"(vG0), [vG1] "v"(vG1), [vT] "v"(vT), [vL16] "v"(vL16), [vFL] "v"(vFL)
        : "v16", "v17", "v18", "v19", "v20", "v21", "v22", "v23", "v24", "v25", "v26", "v27", "v28", "v29", "v30", "v31", "v32", "v33", "v34", "v35", "v36", "v37", "v38", "v39",
"v40", "v41", "v42", "v43", "v44", "v45", "v46", "v47", "v48", "v49", "v50", "v51", "v52", "v53", "v54", "v55", "v56", "v57", "v58", "v59", "v60", "v61", "v62", "v63",
"v64", "v65", "v66", "v67", "v68", "v69", "v70", "v71", "v72", "v73", "v74", "v75", "v76", "v77", "v78", "v79", "v80", "v81", "v82", "v83", "v84", "v85", "v86", "v87",
"v88", "v89", "v90", "v91", "v92", "v93", "v94", "v95", "v96", "v97", "v98", "v99", "v100", "v101", "v102", "v103", "v104", "v105", "v106", "v107", "v108", "v109", "v110", "v111",
"v112", "v113", "v114", "v115", "v116", "v117", "v118", "v119", "v120", "v121", "v122", "v123", "v124", "v125", "v126", "v127", "v128", "v129", "v130", "v131", "v132", "v133", "v134", "v135",
"v136", "v137", "v138", "v139", "v140", "v141", "v142", "v143", "v144", "v145", "v146", "v147", "v148", "v149", "v150", "v151", "v152", "v153", "v154", "v155", "v156", "v157", "v158", "v159",
"v160", "v161", "v162", "v163", "v164", "v165", "v166", "v167", "v168", "v169", "v170", "v171", "v172", "v173", "v174", "v175", "v176", "v177", "v178", "v179", "v180", "v181", "v182", "v183",
"v184", "v185", "v186", "v187", "v188", "v189", "v190", "v191", "v192", "v193", "v194", "v195", "v196", "v197", "v198", "v199", "v200", "v201", "v202", "v203", "v204", "v205", "v206", "v207",
"v208", "v209", "v210", "v211", "v212", "v213", "v214", "v215", "v216", "v217", "v218", "v219", "v220", "v221", "v222", "v223", "v224", "v225", "v226", "v227", "v228", "v229", "v230", "v231",
"v232", "v233", "v234", "v235", "v236", "v237", "v238", "v239", "v240", "v241", "v242", "v243", "v244", "v245", "v246", "v247", "v248", "v249", "v250", "v251", "v252", "v253", "v254", "v255",
"a0", "a1", "a2", "a3", "a4", "a5", "a6", "a7", "a8", "a9", "a10", "a11", "a12", "a13", "a14", "a15", "a16", "a17", "a18", "a19", "a20", "a21", "a22", "a23",
"a24", "a25", "a26", "a27", "a28", "a29", "a30", "a31", "a32", "a33", "a34", "a35", "a36", "a37", "a38", "a39", "a40", "a41", "a42", "a43", "a44", "a45", "a46", "a47",
"a48", "a49", "a50", "a51", "a52", "a53", "a54", "a55", "a56", "a57", "a58", "a59", "a60", "a61", "a62", "a63", "a64", "a65", "a66", "a67", "a68", "a69", "a70", "a71",
"a72", "a73", "a74", "a75", "a76", "a77", "a78", "a79", "a80", "a81", "a82", "a83", "a84", "a85", "a86", "a87", "a88", "a89", "a90", "a91", "a92", "a93", "a94", "a95",
"a96", "a97", "a98", "a99", "a100", "a101", "a102", "a103", "a104", "a105", "a106", "a107", "a108", "a109", "a110", "a111", "a112", "a113", "a114", "a115", "a116", "a117", "a118", "a119",
"a120", "a121", "a122", "a123", "a124", "a125", "a126", "a127", "a128", "a129", "a130", "a131", "a132", "a133", "a134", "a135", "a136", "a137", "a138", "a139", "a140", "a141", "a142", "a143",
"a144", "a145", "a146", "a147", "a148", "a149", "a150", "a151", "a152", "a153", "a154", "a155", "a156", "a157", "a158", "a159", "a160", "a161", "a162", "a163", "a164", "a165", "a166", "a167",
"a168", "a169", "a170", "a171", "a172", "a173", "a174", "a175", "a176", "a177", "a178", "a179", "a180", "a181", "a182", "a183", "a184", "a185", "a186", "a187", "a188", "a189", "a190", "a191",
"a192", "a193", "a194", "a195", "a196", "a197", "a198", "a199", "a200", "a201", "a202", "a203", "a204", "a205", "a206", "a207", "a208", "a209", "a210", "a211", "a212", "a213", "a214", "a215",
"a216", "a217", "a218", "a219", "a220", "a221", "a222", "a223", "a224", "a225", "a226", "a227", "a228", "a229", "a230", "a231", "a232", "a233", "a234", "a235", "a236", "a237", "a238", "a239",
"a240", "a241", "a242", "a243", "a244", "a245", "a246", "a247", "a248", "a249", "a250", "a251", "a252", "a253", "a254", "a255", "s40", "s41", "s42", "s43", "s44", "s45", "s46", "s47",
"s48", "s49", "s50", "s51", "s52", "s53", "s54", "s55", "s56", "s57", "s58", "s59", "s60", "s61", "s62", "s63", "s64", "s65", "s66", "s67", "vcc", "scc", "memory");
}

__device__ __forceinline__ void role_b1(char* ws, const void* w1, const void* b1, int xg, int r) {
    int tid = threadIdx.x; asm volatile("" : "+v"(tid));
    const int w = rfl(tid >> 6), l = tid & 63, g = l >> 4, c = l & 15, x = c >> 2;
    const int ft = 6 * xg + r / 3, dt = r % 3, wf = w >> 1, wd = w & 1;
    const v4i sWS = make_srd(ws, WS_B), sW1 = make_srd(w1, 2048 * 9216), sB1 = make_srd(b1, 9216);
    unsigned vG[3], vT[6];
#pragma unroll
    for (int j = 0; j < 3; ++j) {
        const int b = (3 * w + j) * 64 + l, row = b / 24, pos = b % 24;
        vG[j] = row * 9216 + (FSPLIT ? ft * 128 + (pos >> 3) * 3072 : ft * 384 + (pos >> 3) * 128) + ((((pos >> 1) & 3) ^ (((row >> 1) & 1) | (((row >> 3) & 1) << 1))) * 32) + (pos & 1) * 16;
    }
#pragma unroll
    for (int i = 0; i < 6; ++i) vT[i] = (8 * g + x) * 384 + (((6 * wf + i) ^ ((x >> 1) | ((g & 1) << 1))) * 32) + (c & 3) * 8;
    const unsigned vL16 = l * 16, vFL = tid == 0 ? (unsigned)((xg * 18 + r) * 128) : OOB;
    const unsigned vBI = ((FSPLIT ? 64 : 192) * ft + 4 * g) * 2, vR = 12288 + 6144 * wd + l * 16;
    const int sW3K = w * 3072, sHOFF = rfl(WS_BW + BW_HF + (12 * dt + 3 * w) * 1024), sUOFF = rfl(WS_UF + (FSPLIT ? 2 : 6) * ft * 98304 + (2 * dt + wd) * 16384);
    asm volatile(
"LB_top:\n"
"s_nop 4\n"
"v_mov_b32 v44, %[vG0]\n"
"v_mov_b32 v45, %[vG1]\n"
"v_mov_b32 v46, %[vG2]\n"
"v_mov_b32 v50, %[vT0]\n"
"v_mov_b32 v51, %[vT1]\n"
"v_mov_b32 v52, %[vT2]\n"
"v_mov_b32 v53, %[vT3]\n"
"v_mov_b32 v54, %[vT4]\n"
"v_mov_b32 v55, %[vT5]\n"
"v_mov_b32 v47, %[vBI]\n"
"v_mov_b32 v56, %[vR]\n"
"s_add_u32 s40, %[sT0], 0\n"
"s_lshr_b32 s42, s40, 2\n"
"s_mul_i32 s42, s42, 128\n"
"s_and_b32 s40, s40, 3\n"
"s_lshl_b32 s40, s40, 5\n"
"s_add_u32 s40, s40, s42\n"
"buffer_load_dwordx2 v[24:25], v47, %[sB1], s40 offen\n"
"s_add_u32 s40, %[sT0], 1\n"
"s_lshr_b32 s42, s40, 2\n"
"s_mul_i32 s42, s42, 128\n"
"s_and_b32 s40, s40, 3\n"
"s_lshl_b32 s40, s40, 5\n"
"s_add_u32 s40, s40, s42\n"
"buffer_load_dwordx2 v[26:27], v47, %[sB1], s40 offen\n"
"s_add_u32 s40, %[sT0], 2\n"
"s_lshr_b32 s42, s40, 2\n"
"s_mul_i32 s42, s42, 128\n"
"s_and_b32 s40, s40, 3\n"
"s_lshl_b32 s40, s40, 5\n"
"s_add_u32 s40, s40, s42\n"
"buffer_load_dwordx2 v[28:29], v47, %[sB1], s40 offen\n"
"s_add_u32 s40, %[sT0], 3\n"
"s_lshr_b32 s42, s40, 2\n"
"s_mul_i32 s42, s42, 128\n"
"s_and_b32 s40, s40, 3\n"
"s_lshl_b32 s40, s40, 5\n"
"s_add_u32 s40, s40, s42\n"
"buffer_load_dwordx2 v[30:31], v47, %[sB1], s40 offen\n"
"s_add_u32 s40, %[sT0], 4\n"
"s_lshr_b32 s42, s40, 2\n"
"s_mul_i32 s42, s42, 128\n"
"s_and_b32 s40, s40, 3\n"
"s_lshl_b32 s40, s40, 5\n"
"s_add_u32 s40, s40, s42\n"
"buffer_load_dwordx2 v[32:33], v47, %[sB1], s40 offen\n"
"s_add_u32 s40, %[sT0], 5\n"
"s_lshr_b32 s42, s40, 2\n"
"s_mul_i32 s42, s42, 128\n"
"s_and_b32 s40, s40, 3\n"
"s_lshl_b32 s40, s40, 5\n"
"s_add_u32 s40, s40, s42\n"
"buffer_load_dwordx2 v[34:35], v47, %[sB1], s40 offen\n"
"v_mbcnt_lo_u32_b32 v21, -1, 0\n"
"v_mbcnt_hi_u32_b32 v21, -1, v21\n"
"v_lshlrev_b32 v21, 7, v21\n"
"v_min_u32 v21, 8064, v21\n"
"s_mov_b32 s40, 1111040\n"
"s_mov_b64 exec, -1\n"
"s_mov_b32 s41, 0\n"
"s_branch LB_ln0_go\n"
"LB_ln0:\n"
"s_sleep 4\n"
"LB_ln0_go:\n"
"buffer_load_dword v20, v21, %[sWS], s40 offen sc1\n"
"s_add_u32 s41, s41, 1\n"
"s_cmp_gt_u32 s41, 60000\n"
"s_cbranch_scc1 LB_tmo\n"
"s_waitcnt vmcnt(0)\n"
"v_cmp_gt_i32 vcc, 3, v20\n"
"s_cbranch_vccnz LB_ln0\n"
"LB_ln0_ok:\n"
"s_mov_b64 exec, -1\n"
"v_mbcnt_lo_u32_b32 v21, -1, 0\n"
"v_mbcnt_hi_u32_b32 v21, -1, v21\n"
"v_lshlrev_b32 v21, 7, v21\n"
"v_min_u32 v21, 4992, v21\n"
"s_mov_b32 s40, 1119232\n"
"s_mov_b64 exec, -1\n"
"s_mov_b32 s41, 0\n"
"s_branch LB_ln64_go\n"
"LB_ln64:\n"
"s_sleep 4\n"
"LB_ln64_go:\n"
"buffer_load_dword v20, v21, %[sWS], s40 offen sc1\n"
"s_add_u32 s41, s41, 1\n"
"s_cmp_gt_u32 s41, 60000\n"
"s_cbranch_scc1 LB_tmo\n"
"s_waitcnt vmcnt(0)\n"
"v_cmp_gt_i32 vcc, 3, v20\n"
"s_cbranch_vccnz LB_ln64\n"
"LB_ln64_ok:\n"
"s_mov_b64 exec, -1\n"
"v_lshlrev_b32 v64, 16, v24\n"
"v_and_b32 v65, 0xffff0000, v24\n"
"v_lshlrev_b32 v66, 16, v25\n"
"v_and_b32 v67, 0xffff0000, v25\n"
"v_lshlrev_b32 v68, 16, v26\n"
"v_and_b32 v69, 0xffff0000, v26\n"
"v_lshlrev_b32 v70, 16, v27\n"
"v_and_b32 v71, 0xffff0000, v27\n"
"v_lshlrev_b32 v72, 16, v28\n"
"v_and_b32 v73, 0xffff0000, v28\n"
"v_lshlrev_b32 v74, 16, v29\n"
"v_and_b32 v75, 0xffff0000, v29\n"
"v_lshlrev_b32 v76, 16, v30\n"
"v_and_b32 v77, 0xffff0000, v30\n"
"v_lshlrev_b32 v78, 16, v31\n"
"v_and_b32 v79, 0xffff0000, v31\n"
"v_lshlrev_b32 v80, 16, v32\n"
"v_and_b32 v81, 0xffff0000, v32\n"
"v_lshlrev_b32 v82, 16, v33\n"
"v_and_b32 v83, 0xffff0000, v33\n"
"v_lshlrev_b32 v84, 16, v34\n"
"v_and_b32 v85, 0xffff0000, v34\n"
"v_lshlrev_b32 v86, 16, v35\n"
"v_and_b32 v87, 0xffff0000, v35\n"
"s_mov_b32 s49, %[sK0]\n"
"s_mov_b32 s50, 3\n"
"s_mov_b32 s42, 0\n"
"s_min_u32 s40, s49, 63\n"
"s_mul_i32 s54, s40, 294912\n"
"s_mul_i32 s52, s40, 36864\n"
"s_add_u32 s52, s52, %[sHOFF]\n"
"s_mov_b32 s53, 0\n"
"s_mov_b32 s44, 0\n"
"s_mov_b32 s55, 24576\n"
"s_add_u32 s40, s53, %[sW3K]\n"
"s_add_u32 m0, s40, 0\n"
"s_nop 0\n"
"buffer_load_dwordx4 v44, %[sW1], s54 offen nt lds\n"
"s_add_u32 s40, s53, %[sW3K]\n"
"s_add_u32 m0, s40, 1024\n"
"s_nop 0\n"
"buffer_load_dwordx4 v45, %[sW1], s54 offen nt lds\n"
"s_add_u32 s40, s53, %[sW3K]\n"
"s_add_u32 m0, s40, 2048\n"
"s_nop 0\n"
"buffer_load_dwordx4 v46, %[sW1], s54 offen nt lds\n"
"s_add_u32 s40, s53, %[sW3K]\n"
"s_add_u32 m0, s40, 12288\n"
"s_nop 0\n"
"buffer_load_dwordx4 %[vL16], %[sWS], s52 offen offset:0 sc1 lds\n"
"buffer_load_dwordx4 %[vL16], %[sWS], s52 offen offset:1024 sc1 lds\n"
"buffer_load_dwordx4 %[vL16], %[sWS], s52 offen offset:2048 sc1 lds\n"
"s_add_u32 s53, s53, 24576\n"
"s_cmp_ge_u32 s53, 98304\n"
"s_cselect_b32 s53, 0, s53\n"
"s_add_u32 s49, s49, 1\n"
"s_cmp_eq_u32 s49, s50\n"
"s_cselect_b32 s40, 3, 0\n"
"s_sub_u32 s49, s49, s40\n"
"s_add_u32 s42, s42, 1\n"
"s_cmp_eq_u32 s42, 3\n"
"s_cselect_b32 s40, 3, 0\n"
"s_sub_u32 s42, s42, s40\n"
"s_add_u32 s49, s49, s40\n"
"s_add_u32 s50, s50, s40\n"
"s_min_u32 s40, s49, 63\n"
"s_mul_i32 s54, s40, 294912\n"
"s_mul_i32 s52, s40, 36864\n"
"s_add_u32 s52, s52, %[sHOFF]\n"
"s_add_u32 s40, s53, %[sW3K]\n"
"s_add_u32 m0, s40, 0\n"
"s_nop 0\n"
"buffer_load_dwordx4 v44, %[sW1], s54 offen nt lds\n"
"s_add_u32 s40, s53, %[sW3K]\n"
"s_add_u32 m0, s40, 1024\n"
"s_nop 0\n"
"buffer_load_dwordx4 v45, %[sW1], s54 offen nt lds\n"
"s_add_u32 s40, s53, %[sW3K]\n"
"s_add_u32 m0, s40, 2048\n"
"s_nop 0\n"
"buffer_load_dwordx4 v46, %[sW1], s54 offen nt lds\n"
"s_add_u32 s40, s53, %[sW3K]\n"
"s_add_u32 m0, s40, 12288\n"
"s_nop 0\n"
"buffer_load_dwordx4 %[vL16], %[sWS], s52 offen offset:0 sc1 lds\n"
"buffer_load_dwordx4 %[vL16], %[sWS], s52 offen offset:1024 sc1 lds\n"
"buffer_load_dwordx4 %[vL16], %[sWS], s52 offen offset:2048 sc1 lds\n"
"s_add_u32 s53, s53, 24576\n"
"s_cmp_ge_u32 s53, 98304\n"
"s_cselect_b32 s53, 0, s53\n"
"s_add_u32 s49, s49, 1\n"
"s_cmp_eq_u32 s49, s50\n"
"s_cselect_b32 s40, 3, 0\n"
"s_sub_u32 s49, s49, s40\n"
"s_add_u32 s42, s42, 1\n"
"s_cmp_eq_u32 s42, 3\n"
"s_cselect_b32 s40, 3, 0\n"
"s_sub_u32 s42, s42, s40\n"
"s_add_u32 s49, s49, s40\n"
"s_add_u32 s50, s50, s40\n"
"s_min_u32 s40, s49, 63\n"
"s_mul_i32 s54, s40, 294912\n"
"s_mul_i32 s52, s40, 36864\n"
"s_add_u32 s52, s52, %[sHOFF]\n"
"s_add_u32 s40, s53, %[sW3K]\n"
"s_add_u32 m0, s40, 0\n"
"s_nop 0\n"
"buffer_load_dwordx4 v44, %[sW1], s54 offen nt lds\n"
"s_add_u32 s40, s53, %[sW3K]\n"
"s_add_u32 m0, s40, 1024\n"
"s_nop 0\n"
"buffer_load_dwordx4 v45, %[sW1], s54 offen nt lds\n"
"s_add_u32 s40, s53, %[sW3K]\n"
"s_add_u32 m0, s40, 2048\n"
"s_nop 0\n"
"buffer_load_dwordx4 v46, %[sW1], s54 offen nt lds\n"
"s_add_u32 s40, s53, %[sW3K]\n"
"s_add_u32 m0, s40, 12288\n"
"s_nop 0\n"
"buffer_load_dwordx4 %[vL16], %[sWS], s52 offen offset:0 sc1 lds\n"
"buffer_load_dwordx4 %[vL16], %[sWS], s52 offen offset:1024 sc1 lds\n"
"buffer_load_dwordx4 %[vL16], %[sWS], s52 offen offset:2048 sc1 lds\n"
"s_add_u32 s53, s53, 24576\n"
"s_cmp_ge_u32 s53, 98304\n"
"s_cselect_b32 s53, 0, s53\n"
"s_add_u32 s49, s49, 1\n"
"s_cmp_eq_u32 s49, s50\n"
"s_cselect_b32 s40, 3, 0\n"
"s_sub_u32 s49, s49, s40\n"
"s_add_u32 s42, s42, 1\n"
"s_cmp_eq_u32 s42, 3\n"
"s_cselect_b32 s40, 3, 0\n"
"s_sub_u32 s42, s42, s40\n"
"s_add_u32 s49, s49, s40\n"
"s_add_u32 s50, s50, s40\n"
"s_min_u32 s40, s49, 63\n"
"s_mul_i32 s54, s40, 294912\n"
"s_mul_i32 s52, s40, 36864\n"
"s_add_u32 s52, s52, %[sHOFF]\n"
"s_add_u32 s40, s53, %[sW3K]\n"
"s_add_u32 m0, s40, 0\n"
"s_nop 0\n"
"buffer_load_dwordx4 v44, %[sW1], s54 offen nt lds\n"
"s_add_u32 s40, s53, %[sW3K]\n"
"s_add_u32 m0, s40, 1024\n"
"s_nop 0\n"
"buffer_load_dwordx4 v45, %[sW1], s54 offen nt lds\n"
"s_add_u32 s40, s53, %[sW3K]\n"
"s_add_u32 m0, s40, 2048\n"
"s_nop 0\n"
"buffer_load_dwordx4 v46, %[sW1], s54 offen nt lds\n"
"s_add_u32 s40, s53, %[sW3K]\n"
"s_add_u32 m0, s40, 12288\n"
"s_nop 0\n"
"buffer_load_dwordx4 %[vL16], %[sWS], s52 offen offset:0 sc1 lds\n"
"buffer_load_dwordx4 %[vL16], %[sWS], s52 offen offset:1024 sc1 lds\n"
"buffer_load_dwordx4 %[vL16], %[sWS], s52 offen offset:2048 sc1 lds\n"
"s_add_u32 s53, s53, 24576\n"
"s_cmp_ge_u32 s53, 98304\n"
"s_cselect_b32 s53, 0, s53\n"
"s_add_u32 s49, s49, 1\n"
"s_cmp_eq_u32 s49, s50\n"
"s_cselect_b32 s40, 3, 0\n"
"s_sub_u32 s49, s49, s40\n"
"s_add_u32 s42, s42, 1\n"
"s_cmp_eq_u32 s42, 3\n"
"s_cselect_b32 s40, 3, 0\n"
"s_sub_u32 s42, s42, s40\n"
"s_add_u32 s49, s49, s40\n"
"s_add_u32 s50, s50, s40\n"
"s_min_u32 s40, s49, 63\n"
"s_mul_i32 s54, s40, 294912\n"
"s_mul_i32 s52, s40, 36864\n"
"s_add_u32 s52, s52, %[sHOFF]\n"
"v_accvgpr_write_b32 a0, v64\n"
"v_accvgpr_write_b32 a1, v65\n"
"v_accvgpr_write_b32 a2, v66\n"
"v_accvgpr_write_b32 a3, v67\n"
"v_accvgpr_write_b32 a4, v64\n"
"v_accvgpr_write_b32 a5, v65\n"
"v_accvgpr_write_b32 a6, v66\n"
"v_accvgpr_write_b32 a7, v67\n"
"v_accvgpr_write_b32 a8, v64\n"
"v_accvgpr_write_b32 a9, v65\n"
"v_accvgpr_write_b32 a10, v66\n"
"v_accvgpr_write_b32 a11, v67\n"
"v_accvgpr_write_b32 a12, v64\n"
"v_accvgpr_write_b32 a13, v65\n"
"v_accvgpr_write_b32 a14, v66\n"
"v_accvgpr_write_b32 a15, v67\n"
"v_accvgpr_write_b32 a16, v64\n"
"v_accvgpr_write_b32 a17, v65\n"
"v_accvgpr_write_b32 a18, v66\n"
"v_accvgpr_write_b32 a19, v67\n"
"v_accvgpr_write_b32 a20, v64\n"
"v_accvgpr_write_b32 a21, v65\n"
"v_accvgpr_write_b32 a22, v66\n"
"v_accvgpr_write_b32 a23, v67\n"
"v_accvgpr_write_b32 a24, v68\n"
"v_accvgpr_write_b32 a25, v69\n"
"v_accvgpr_write_b32 a26, v70\n"
"v_accvgpr_write_b32 a27, v71\n"
"v_accvgpr_write_b32 a28, v68\n"
"v_accvgpr_write_b32 a29, v69\n"
"v_accvgpr_write_b32 a30, v70\n"
"v_accvgpr_write_b32 a31, v71\n"
"v_accvgpr_write_b32 a32, v68\n"
"v_accvgpr_write_b32 a33, v69\n"
"v_accvgpr_write_b32 a34, v70\n"
"v_accvgpr_write_b32 a35, v71\n"
"v_accvgpr_write_b32 a36, v68\n"
"v_accvgpr_write_b32 a37, v69\n"
"v_accvgpr_write_b32 a38, v70\n"
"v_accvgpr_write_b32 a39, v71\n"
"v_accvgpr_write_b32 a40, v68\n"
"v_accvgpr_write_b32 a41, v69\n"
"v_accvgpr_write_b32 a42, v70\n"
"v_accvgpr_write_b32 a43, v71\n"
"v_accvgpr_write_b32 a44, v68\n"
"v_accvgpr_write_b32 a45, v69\n"
"v_accvgpr_write_b32 a46, v70\n"
"v_accvgpr_write_b32 a47, v71\n"
"v_accvgpr_write_b32 a48, v72\n"
"v_accvgpr_write_b32 a49, v73\n"
"v_accvgpr_write_b32 a50, v74\n"
"v_accvgpr_write_b32 a51, v75\n"
"v_accvgpr_write_b32 a52, v72\n"
"v_accvgpr_write_b32 a53, v73\n"
"v_accvgpr_write_b32 a54, v74\n"
"v_accvgpr_write_b32 a55, v75\n"
"v_accvgpr_write_b32 a56, v72\n"
"v_accvgpr_write_b32 a57, v73\n"
"v_accvgpr_write_b32 a58, v74\n"
"v_accvgpr_write_b32 a59, v75\n"
"v_accvgpr_write_b32 a60, v72\n"
"v_accvgpr_write_b32 a61, v73\n"
"v_accvgpr_write_b32 a62, v74\n"
"v_accvgpr_write_b32 a63, v75\n"
"v_accvgpr_write_b32 a64, v72\n"
"v_accvgpr_write_b32 a65, v73\n"
"v_accvgpr_write_b32 a66, v74\n"
"v_accvgpr_write_b32 a67, v75\n"
"v_accvgpr_write_b32 a68, v72\n"
"v_accvgpr_write_b32 a69, v73\n"
"v_accvgpr_write_b32 a70, v74\n"
"v_accvgpr_write_b32 a71, v75\n"
"v_accvgpr_write_b32 a72, v76\n"
"v_accvgpr_write_b32 a73, v77\n"
"v_accvgpr_write_b32 a74, v78\n"
"v_accvgpr_write_b32 a75, v79\n"
"v_accvgpr_write_b32 a76, v76\n"
"v_accvgpr_write_b32 a77, v77\n"
"v_accvgpr_write_b32 a78, v78\n"
"v_accvgpr_write_b32 a79, v79\n"
"v_accvgpr_write_b32 a80, v76\n"
"v_accvgpr_write_b32 a81, v77\n"
"v_accvgpr_write_b32 a82, v78\n"
"v_accvgpr_write_b32 a83, v79\n"
"v_accvgpr_write_b32 a84, v76\n"
"v_accvgpr_write_b32 a85, v77\n"
"v_accvgpr_write_b32 a86, v78\n"
"v_accvgpr_write_b32 a87, v79\n"
"v_accvgpr_write_b32 a88, v76\n"
"v_accvgpr_write_b32 a89, v77\n"
"v_accvgpr_write_b32 a90, v78\n"
"v_accvgpr_write_b32 a91, v79\n"
"v_accvgpr_write_b32 a92, v76\n"
"v_accvgpr_write_b32 a93, v77\n"
"v_accvgpr_write_b32 a94, v78\n"
"v_accvgpr_write_b32 a95, v79\n"
"v_accvgpr_write_b32 a96, v80\n"
"v_accvgpr_write_b32 a97, v81\n"
"v_accvgpr_write_b32 a98, v82\n"
"v_accvgpr_write_b32 a99, v83\n"
"v_accvgpr_write_b32 a100, v80\n"
"v_accvgpr_write_b32 a101, v81\n"
"v_accvgpr_write_b32 a102, v82\n"
"v_accvgpr_write_b32 a103, v83\n"
"v_accvgpr_write_b32 a104, v80\n"
"v_accvgpr_write_b32 a105, v81\n"
"v_accvgpr_write_b32 a106, v82\n"
"v_accvgpr_write_b32 a107, v83\n"
"v_accvgpr_write_b32 a108, v80\n"
"v_accvgpr_write_b32 a109, v81\n"
"v_accvgpr_write_b32 a110, v82\n"
"v_accvgpr_write_b32 a111, v83\n"
"v_accvgpr_write_b32 a112, v80\n"
"v_accvgpr_write_b32 a113, v81\n"
"v_accvgpr_write_b32 a114, v82\n"
"v_accvgpr_write_b32 a115, v83\n"
"v_accvgpr_write_b32 a116, v80\n"
"v_accvgpr_write_b32 a117, v81\n"
"v_accvgpr_write_b32 a118, v82\n"
"v_accvgpr_write_b32 a119, v83\n"
"v_accvgpr_write_b32 a120, v84\n"
"v_accvgpr_write_b32 a121, v85\n"
"v_accvgpr_write_b32 a122, v86\n"
"v_accvgpr_write_b32 a123, v87\n"
"v_accvgpr_write_b32 a124, v84\n"
"v_accvgpr_write_b32 a125, v85\n"
"v_accvgpr_write_b32 a126, v86\n"
"v_accvgpr_write_b32 a127, v87\n"
"v_accvgpr_write_b32 a128, v84\n"
"v_accvgpr_write_b32 a129, v85\n"
"v_accvgpr_write_b32 a130, v86\n"
"v_accvgpr_write_b32 a131, v87\n"
"v_accvgpr_write_b32 a132, v84\n"
"v_accvgpr_write_b32 a133, v85\n"
"v_accvgpr_write_b32 a134, v86\n"
"v_accvgpr_write_b32 a135, v87\n"
"v_accvgpr_write_b32 a136, v84\n"
"v_accvgpr_write_b32 a137, v85\n"
"v_accvgpr_write_b32 a138, v86\n"
"v_accvgpr_write_b32 a139, v87\n"
"v_accvgpr_write_b32 a140, v84\n"
"v_accvgpr_write_b32 a141, v85\n"
"v_accvgpr_write_b32 a142, v86\n"
"v_accvgpr_write_b32 a143, v87\n"
"s_waitcnt vmcnt(18)\n"
"s_barrier\n"
"ds_read_b64_tr_b16 v[100:101], v50 offset:0\n"
"ds_read_b64_tr_b16 v[102:103], v50 offset:1536\n"
"ds_read_b64_tr_b16 v[104:105], v51 offset:0\n"
"ds_read_b64_tr_b16 v[106:107], v51 offset:1536\n"
"ds_read_b64_tr_b16 v[108:109], v52 offset:0\n"
"ds_read_b64_tr_b16 v[110:111], v52 offset:1536\n"
"ds_read_b64_tr_b16 v[112:113], v53 offset:0\n"
"ds_read_b64_tr_b16 v[114:115], v53 offset:1536\n"
"ds_read_b64_tr_b16 v[116:117], v54 offset:0\n"
"ds_read_b64_tr_b16 v[118:119], v54 offset:1536\n"
"ds_read_b64_tr_b16 v[120:121], v55 offset:0\n"
"ds_read_b64_tr_b16 v[122:123], v55 offset:1536\n"
"ds_read_b128 v[124:127], v56 offset:0\n"
"ds_read_b128 v[128:131], v56 offset:1024\n"
"ds_read_b128 v[132:135], v56 offset:2048\n"
"ds_read_b128 v[136:139], v56 offset:3072\n"
"ds_read_b128 v[140:143], v56 offset:4096\n"
"ds_read_b128 v[144:147], v56 offset:5120\n"
"s_add_u32 s44, s44, 24576\n"
"s_cmp_ge_u32 s44, 98304\n"
"s_cselect_b32 s45, 4294893568, s55\n"
"s_cselect_b32 s44, 0, s44\n"
"v_add_u32 v50, s45, v50\n"
"v_add_u32 v51, s45, v51\n"
"v_add_u32 v52, s45, v52\n"
"v_add_u32 v53, s45, v53\n"
"v_add_u32 v54, s45, v54\n"
"v_add_u32 v55, s45, v55\n"
"v_add_u32 v56, s45, v56\n"
"s_mov_b32 s43, 32\n"
"LB_g1:\n"
"s_waitcnt vmcnt(12) lgkmcnt(0)\n"
"s_barrier\n"
"v_mfma_f32_16x16x32_bf16 a[0:3], v[100:103], v[124:127], a[0:3]\n"
"ds_read_b128 v[172:175], v56 offset:0\n"
"v_mfma_f32_16x16x32_bf16 a[4:7], v[100:103], v[128:131], a[4:7]\n"
"ds_read_b128 v[176:179], v56 offset:1024\n"
"v_mfma_f32_16x16x32_bf16 a[8:11], v[100:103], v[132:135], a[8:11]\n"
"s_add_u32 s40, s53, %[sW3K]\n"
"s_add_u32 m0, s40, 0\n"
"s_nop 0\n"
"buffer_load_dwordx4 v44, %[sW1], s54 offen nt lds\n"
"v_mfma_f32_16x16x32_bf16 a[12:15], v[100:103], v[136:139], a[12:15]\n"
"ds_read_b128 v[180:183], v56 offset:2048\n"
"v_mfma_f32_16x16x32_bf16 a[16:19], v[100:103], v[140:143], a[16:19]\n"
"ds_read_b128 v[184:187], v56 offset:3072\n"
"v_mfma_f32_16x16x32_bf16 a[20:23], v[100:103], v[144:147], a[20:23]\n"
"s_add_u32 s40, s53, %[sW3K]\n"
"s_add_u32 m0, s40, 1024\n"
"s_nop 0\n"
"buffer_load_dwordx4 v45, %[sW1], s54 offen nt lds\n"
"v_mfma_f32_16x16x32_bf16 a[24:27], v[104:107], v[124:127], a[24:27]\n"
"ds_read_b128 v[188:191], v56 offset:4096\n"
"v_mfma_f32_16x16x32_bf16 a[28:31], v[104:107], v[128:131], a[28:31]\n"
"ds_read_b128 v[192:195], v56 offset:5120\n"
"v_mfma_f32_16x16x32_bf16 a[32:35], v[104:107], v[132:135], a[32:35]\n"
"s_add_u32 s40, s53, %[sW3K]\n"
"s_add_u32 m0, s40, 2048\n"
"s_nop 0\n"
"buffer_load_dwordx4 v46, %[sW1], s54 offen nt lds\n"
"v_mfma_f32_16x16x32_bf16 a[36:39], v[104:107], v[136:139], a[36:39]\n"
"ds_read_b64_tr_b16 v[148:149], v50 offset:0\n"
"v_mfma_f32_16x16x32_bf16 a[40:43], v[104:107], v[140:143], a[40:43]\n"
"ds_read_b64_tr_b16 v[150:151], v50 offset:1536\n"
"v_mfma_f32_16x16x32_bf16 a[44:47], v[104:107], v[144:147], a[44:47]\n"
"s_add_u32 s40, s53, %[sW3K]\n"
"s_add_u32 m0, s40, 12288\n"
"s_nop 0\n"
"buffer_load_dwordx4 %[vL16], %[sWS], s52 offen offset:0 sc1 lds\n"
"v_mfma_f32_16x16x32_bf16 a[48:51], v[108:111], v[124:127], a[48:51]\n"
"ds_read_b64_tr_b16 v[152:153], v51 offset:0\n"
"v_mfma_f32_16x16x32_bf16 a[52:55], v[108:111], v[128:131], a[52:55]\n"
"ds_read_b64_tr_b16 v[154:155], v51 offset:1536\n"
"v_mfma_f32_16x16x32_bf16 a[56:59], v[108:111], v[132:135], a[56:59]\n"
"buffer_load_dwordx4 %[vL16], %[sWS], s52 offen offset:1024 sc1 lds\n"
"v_mfma_f32_16x16x32_bf16 a[60:63], v[108:111], v[136:139], a[60:63]\n"
"ds_read_b64_tr_b16 v[156:157], v52 offset:0\n"
"v_mfma_f32_16x16x32_bf16 a[64:67], v[108:111], v[140:143], a[64:67]\n"
"ds_read_b64_tr_b16 v[158:159], v52 offset:1536\n"
"v_mfma_f32_16x16x32_bf16 a[68:71], v[108:111], v[144:147], a[68:71]\n"
"buffer_load_dwordx4 %[vL16], %[sWS], s52 offen offset:2048 sc1 lds\n"
"v_mfma_f32_16x16x32_bf16 a[72:75], v[112:115], v[124:127], a[72:75]\n"
"ds_read_b64_tr_b16 v[160:161], v53 offset:0\n"
"v_mfma_f32_16x16x32_bf16 a[76:79], v[112:115], v[128:131], a[76:79]\n"
"ds_read_b64_tr_b16 v[162:163], v53 offset:1536\n"
"v_mfma_f32_16x16x32_bf16 a[80:83], v[112:115], v[132:135], a[80:83]\n"
"s_add_u32 s53, s53, 24576\n"
"s_cmp_ge_u32 s53, 98304\n"
"s_cselect_b32 s53, 0, s53\n"
"v_mfma_f32_16x16x32_bf16 a[84:87], v[112:115], v[136:139], a[84:87]\n"
"ds_read_b64_tr_b16 v[164:165], v54 offset:0\n"
"v_mfma_f32_16x16x32_bf16 a[88:91], v[112:115], v[140:143], a[88:91]\n"
"ds_read_b64_tr_b16 v[166:167], v54 offset:1536\n"
"v_mfma_f32_16x16x32_bf16 a[92:95], v[112:115], v[144:147], a[92:95]\n"
"ds_read_b64_tr_b16 v[168:169], v55 offset:0\n"
"v_mfma_f32_16x16x32_bf16 a[96:99], v[116:119], v[124:127], a[96:99]\n"
"ds_read_b64_tr_b16 v[170:171], v55 offset:1536\n"
"v_mfma_f32_16x16x32_bf16 a[100:103], v[116:119], v[128:131], a[100:103]\n"
"s_add_u32 s44, s44, 24576\n"
"s_cmp_ge_u32 s44, 98304\n"
"s_cselect_b32 s45, 4294893568, s55\n"
"s_cselect_b32 s44, 0, s44\n"
"v_mfma_f32_16x16x32_bf16 a[104:107], v[116:119], v[132:135], a[104:107]\n"
"v_add_u32 v50, s45, v50\n"
"s_add_u32 s49, s49, 1\n"
"s_cmp_eq_u32 s49, s50\n"
"s_cselect_b32 s40, 3, 0\n"
"v_mfma_f32_16x16x32_bf16 a[108:111], v[116:119], v[136:139], a[108:111]\n"
"v_add_u32 v51, s45, v51\n"
"s_sub_u32 s49, s49, s40\n"
"s_add_u32 s42, s42, 1\n"
"v_mfma_f32_16x16x32_bf16 a[112:115], v[116:119], v[140:143], a[112:115]\n"
"v_add_u32 v52, s45, v52\n"
"s_cmp_eq_u32 s42, 3\n"
"s_cselect_b32 s40, 3, 0\n"
"s_sub_u32 s42, s42, s40\n"
"v_mfma_f32_16x16x32_bf16 a[116:119], v[116:119], v[144:147], a[116:119]\n"
"v_add_u32 v53, s45, v53\n"
"s_add_u32 s49, s49, s40\n"
"s_add_u32 s50, s50, s40\n"
"v_mfma_f32_16x16x32_bf16 a[120:123], v[120:123], v[124:127], a[120:123]\n"
"v_add_u32 v54, s45, v54\n"
"s_min_u32 s40, s49, 63\n"
"s_mul_i32 s54, s40, 294912\n"
"s_mul_i32 s52, s40, 36864\n"
"s_add_u32 s52, s52, %[sHOFF]\n"
"v_mfma_f32_16x16x32_bf16 a[124:127], v[120:123], v[128:131], a[124:127]\n"
"v_add_u32 v55, s45, v55\n"
"v_mfma_f32_16x16x32_bf16 a[128:131], v[120:123], v[132:135], a[128:131]\n"
"v_add_u32 v56, s45, v56\n"
"v_mfma_f32_16x16x32_bf16 a[132:135], v[120:123], v[136:139], a[132:135]\n"
"v_mfma_f32_16x16x32_bf16 a[136:139], v[120:123], v[140:143], a[136:139]\n"
"v_mfma_f32_16x16x32_bf16 a[140:143], v[120:123], v[144:147], a[140:143]\n"
"s_waitcnt vmcnt(12) lgkmcnt(0)\n"
"s_barrier\n"
"v_mfma_f32_16x16x32_bf16 a[0:3], v[148:151], v[172:175], a[0:3]\n"
"ds_read_b128 v[124:127], v56 offset:0\n"
"v_mfma_f32_16x16x32_bf16 a[4:7], v[148:151], v[176:179], a[4:7]\n"
"ds_read_b128 v[128:131], v56 offset:1024\n"
"v_mfma_f32_16x16x32_bf16 a[8:11], v[148:151], v[180:183], a[8:11]\n"
"s_add_u32 s40, s53, %[sW3K]\n"
"s_add_u32 m0, s40, 0\n"
"s_nop 0\n"
"buffer_load_dwordx4 v44, %[sW1], s54 offen nt lds\n"
"v_mfma_f32_16x16x32_bf16 a[12:15], v[148:151], v[184:187], a[12:15]\n"
"ds_read_b128 v[132:135], v56 offset:2048\n"
"v_mfma_f32_16x16x32_bf16 a[16:19], v[148:151], v[188:191], a[16:19]\n"
"ds_read_b128 v[136:139], v56 offset:3072\n"
"v_mfma_f32_16x16x32_bf16 a[20:23], v[148:151], v[192:195], a[20:23]\n"
"s_add_u32 s40, s53, %[sW3K]\n"
"s_add_u32 m0, s40, 1024\n"
"s_nop 0\n"
"buffer_load_dwordx4 v45, %[sW1], s54 offen nt lds\n"
"v_mfma_f32_16x16x32_bf16 a[24:27], v[152:155], v[172:175], a[24:27]\n"
"ds_read_b128 v[140:143], v56 offset:4096\n"
"v_mfma_f32_16x16x32_bf16 a[28:31], v[152:155], v[176:179], a[28:31]\n"
"ds_read_b128 v[144:147], v56 offset:5120\n"
"v_mfma_f32_16x16x32_bf16 a[32:35], v[152:155], v[180:183], a[32:35]\n"
"s_add_u32 s40, s53, %[sW3K]\n"
"s_add_u32 m0, s40, 2048\n"
"s_nop 0\n"
"buffer_load_dwordx4 v46, %[sW1], s54 offen nt lds\n"
"v_mfma_f32_16x16x32_bf16 a[36:39], v[152:155], v[184:187], a[36:39]\n"
"ds_read_b64_tr_b16 v[100:101], v50 offset:0\n"
"v_mfma_f32_16x16x32_bf16 a[40:43], v[152:155], v[188:191], a[40:43]\n"
"ds_read_b64_tr_b16 v[102:103], v50 offset:1536\n"
"v_mfma_f32_16x16x32_bf16 a[44:47], v[152:155], v[192:195], a[44:47]\n"
"s_add_u32 s40, s53, %[sW3K]\n"
"s_add_u32 m0, s40, 12288\n"
"s_nop 0\n"
"buffer_load_dwordx4 %[vL16], %[sWS], s52 offen offset:0 sc1 lds\n"
"v_mfma_f32_16x16x32_bf16 a[48:51], v[156:159], v[172:175], a[48:51]\n"
"ds_read_b64_tr_b16 v[104:105], v51 offset:0\n"
"v_mfma_f32_16x16x32_bf16 a[52:55], v[156:159], v[176:179], a[52:55]\n"
"ds_read_b64_tr_b16 v[106:107], v51 offset:1536\n"
"v_mfma_f32_16x16x32_bf16 a[56:59], v[156:159], v[180:183], a[56:59]\n"
"buffer_load_dwordx4 %[vL16], %[sWS], s52 offen offset:1024 sc1 lds\n"
"v_mfma_f32_16x16x32_bf16 a[60:63], v[156:159], v[184:187], a[60:63]\n"
"ds_read_b64_tr_b16 v[108:109], v52 offset:0\n"
"v_mfma_f32_16x16x32_bf16 a[64:67], v[156:159], v[188:191], a[64:67]\n"
"ds_read_b64_tr_b16 v[110:111], v52 offset:1536\n"
"v_mfma_f32_16x16x32_bf16 a[68:71], v[156:159], v[192:195], a[68:71]\n"
"buffer_load_dwordx4 %[vL16], %[sWS], s52 offen offset:2048 sc1 lds\n"
"v_mfma_f32_16x16x32_bf16 a[72:75], v[160:163], v[172:175], a[72:75]\n"
"ds_read_b64_tr_b16 v[112:113], v53 offset:0\n"
"v_mfma_f32_16x16x32_bf16 a[76:79], v[160:163], v[176:179], a[76:79]\n"
"ds_read_b64_tr_b16 v[114:115], v53 offset:1536\n"
"v_mfma_f32_16x16x32_bf16 a[80:83], v[160:163], v[180:183], a[80:83]\n"
"s_add_u32 s53, s53, 24576\n"
"s_cmp_ge_u32 s53, 98304\n"
"s_cselect_b32 s53, 0, s53\n"
"v_mfma_f32_16x16x32_bf16 a[84:87], v[160:163], v[184:187], a[84:87]\n"
"ds_read_b64_tr_b16 v[116:117], v54 offset:0\n"
"v_mfma_f32_16x16x32_bf16 a[88:91], v[160:163], v[188:191], a[88:91]\n"
"ds_read_b64_tr_b16 v[118:119], v54 offset:1536\n"
"v_mfma_f32_16x16x32_bf16 a[92:95], v[160:163], v[192:195], a[92:95]\n"
"ds_read_b64_tr_b16 v[120:121], v55 offset:0\n"
"v_mfma_f32_16x16x32_bf16 a[96:99], v[164:167], v[172:175], a[96:99]\n"
"ds_read_b64_tr_b16 v[122:123], v55 offset:1536\n"
"v_mfma_f32_16x16x32_bf16 a[100:103], v[164:167], v[176:179], a[100:103]\n"
"s_add_u32 s44, s44, 24576\n"
"s_cmp_ge_u32 s44, 98304\n"
"s_cselect_b32 s45, 4294893568, s55\n"
"s_cselect_b32 s44, 0, s44\n"
"v_mfma_f32_16x16x32_bf16 a[104:107], v[164:167], v[180:183], a[104:107]\n"
"v_add_u32 v50, s45, v50\n"
"s_add_u32 s49, s49, 1\n"
"s_cmp_eq_u32 s49, s50\n"
"s_cselect_b32 s40, 3, 0\n"
"v_mfma_f32_16x16x32_bf16 a[108:111], v[164:167], v[184:187], a[108:111]\n"
"v_add_u32 v51, s45, v51\n"
"s_sub_u32 s49, s49, s40\n"
"s_add_u32 s42, s42, 1\n"
"v_mfma_f32_16x16x32_bf16 a[112:115], v[164:167], v[188:191], a[112:115]\n"
"v_add_u32 v52, s45, v52\n"
"s_cmp_eq_u32 s42, 3\n"
"s_cselect_b32 s40, 3, 0\n"
"s_sub_u32 s42, s42, s40\n"
"v_mfma_f32_16x16x32_bf16 a[116:119], v[164:167], v[192:195], a[116:119]\n"
"v_add_u32 v53, s45, v53\n"
"s_add_u32 s49, s49, s40\n"
"s_add_u32 s50, s50, s40\n"
"v_mfma_f32_16x16x32_bf16 a[120:123], v[168:171], v[172:175], a[120:123]\n"
"v_add_u32 v54, s45, v54\n"
"s_min_u32 s40, s49, 63\n"
"s_mul_i32 s54, s40, 294912\n"
"s_mul_i32 s52, s40, 36864\n"
"s_add_u32 s52, s52, %[sHOFF]\n"
"v_mfma_f32_16x16x32_bf16 a[124:127], v[168:171], v[176:179], a[124:127]\n"
"v_add_u32 v55, s45, v55\n"
"v_mfma_f32_16x16x32_bf16 a[128:131], v[168:171], v[180:183], a[128:131]\n"
"v_add_u32 v56, s45, v56\n"
"v_mfma_f32_16x16x32_bf16 a[132:135], v[168:171], v[184:187], a[132:135]\n"
"s_sub_u32 s43, s43, 1\n"
"s_cmp_lg_u32 s43, 0\n"
"v_mfma_f32_16x16x32_bf16 a[136:139], v[168:171], v[188:191], a[136:139]\n"
"v_mfma_f32_16x16x32_bf16 a[140:143], v[168:171], v[192:195], a[140:143]\n"
"s_cbranch_scc1 LB_g1\n"
"s_waitcnt vmcnt(0) lgkmcnt(0)\n"
"s_barrier\n"
"s_mov_b32 s46, 0xbdd2d3e8\n"
"s_mov_b32 s47, s46\n"
"v_mov_b32 v40, 0xc0135761\n"
"v_mov_b32 v41, v40\n"
"v_mov_b32 v42, 1.0\n"
"v_mov_b32 v43, 1.0\n"
"v_mov_b32 v21, 0\n"
"s_mov_b32 s40, 1065984\n"
"s_mov_b64 exec, 1\n"
"s_mov_b32 s41, 0\n"
"LB_mask:\n"
"buffer_load_dword v20, v21, %[sWS], s40 offen sc1\n"
"s_add_u32 s41, s41, 1\n"
"s_cmp_gt_u32 s41, 60000\n"
"s_cbranch_scc1 LB_tmo\n"
"s_waitcnt vmcnt(0)\n"
"v_cmp_gt_i32 vcc, 0, v20\n"
"s_cbranch_vccnz LB_mask\n"
"LB_mask_ok:\n"
"s_mov_b64 exec, -1\n"
"v_readfirstlane_b32 s51, v20\n"
"s_mov_b32 s50, %[sUOFF]\n"
"s_mov_b32 s49, 0\n"
"s_nop 2\n"
"s_lshr_b32 s40, %[sT0], 1\n"
"s_add_u32 s40, s40, 0\n"
"s_lshr_b32 s42, s40, 1\n"
"s_mul_i32 s42, s42, 2\n"
"s_and_b32 s40, s40, 1\n"
"s_add_u32 s40, s40, s42\n"
"s_mul_i32 s40, s40, 98304\n"
"s_add_u32 s50, s40, %[sUOFF]\n"
"s_lshr_b32 s48, s50, 21\n"
"s_lshr_b32 s48, s51, s48\n"
"s_and_b32 s48, s48, 1\n"
"s_lshl_b32 s48, s48, 13\n"
"s_add_u32 s48, s48, s50\n"
"v_accvgpr_read_b32 v128, a0\n"
"v_accvgpr_read_b32 v129, a1\n"
"v_accvgpr_read_b32 v130, a2\n"
"v_accvgpr_read_b32 v131, a3\n"
"v_accvgpr_read_b32 v132, a24\n"
"v_accvgpr_read_b32 v133, a25\n"
"v_accvgpr_read_b32 v134, a26\n"
"v_accvgpr_read_b32 v135, a27\n"
"v_pk_mul_f32 v[136:137], v[128:129], v[128:129]\n"
"v_pk_mul_f32 v[138:139], v[130:131], v[130:131]\n"
"v_pk_mul_f32 v[140:141], v[132:133], v[132:133]\n"
"v_pk_mul_f32 v[142:143], v[134:135], v[134:135]\n"
"v_pk_fma_f32 v[136:137], v[136:137], s[46:47], v[40:41]\n"
"v_pk_fma_f32 v[138:139], v[138:139], s[46:47], v[40:41]\n"
"v_pk_fma_f32 v[140:141], v[140:141], s[46:47], v[40:41]\n"
"v_pk_fma_f32 v[142:143], v[142:143], s[46:47], v[40:41]\n"
"v_pk_mul_f32 v[136:137], v[136:137], v[128:129]\n"
"v_pk_mul_f32 v[138:139], v[138:139], v[130:131]\n"
"v_pk_mul_f32 v[140:141], v[140:141], v[132:133]\n"
"v_pk_mul_f32 v[142:143], v[142:143], v[134:135]\n"
"v_exp_f32 v136, v136\n"
"v_exp_f32 v137, v137\n"
"v_exp_f32 v138, v138\n"
"v_exp_f32 v139, v139\n"
"v_exp_f32 v140, v140\n"
"v_exp_f32 v141, v141\n"
"v_exp_f32 v142, v142\n"
"v_exp_f32 v143, v143\n"
"v_pk_add_f32 v[136:137], v[136:137], v[42:43]\n"
"v_pk_add_f32 v[138:139], v[138:139], v[42:43]\n"
"v_pk_add_f32 v[140:141], v[140:141], v[42:43]\n"
"v_pk_add_f32 v[142:143], v[142:143], v[42:43]\n"
"v_rcp_f32 v136, v136\n"
"v_rcp_f32 v137, v137\n"
"v_rcp_f32 v138, v138\n"
"v_rcp_f32 v139, v139\n"
"v_rcp_f32 v140, v140\n"
"v_rcp_f32 v141, v141\n"
"v_rcp_f32 v142, v142\n"
"v_rcp_f32 v143, v143\n"
"v_pk_mul_f32 v[128:129], v[128:129], v[136:137]\n"
"v_pk_mul_f32 v[130:131], v[130:131], v[138:139]\n"
"v_pk_mul_f32 v[132:133], v[132:133], v[140:141]\n"
"v_pk_mul_f32 v[134:135], v[134:135], v[142:143]\n"
"v_cvt_pk_bf16_f32 v144, v128, v129\n"
"v_cvt_pk_bf16_f32 v145, v130, v131\n"
"v_cvt_pk_bf16_f32 v146, v132, v133\n"
"v_cvt_pk_bf16_f32 v147, v134, v135\n"
"buffer_store_dwordx4 v[144:147], %[vL16], %[sWS], s48 offen offset:0 sc1\n"
"v_accvgpr_read_b32 v160, a4\n"
"v_accvgpr_read_b32 v161, a5\n"
"v_accvgpr_read_b32 v162, a6\n"
"v_accvgpr_read_b32 v163, a7\n"
"v_accvgpr_read_b32 v164, a28\n"
"v_accvgpr_read_b32 v165, a29\n"
"v_accvgpr_read_b32 v166, a30\n"
"v_accvgpr_read_b32 v167, a31\n"
"v_pk_mul_f32 v[168:169], v[160:161], v[160:161]\n"
"v_pk_mul_f32 v[170:171], v[162:163], v[162:163]\n"
"v_pk_mul_f32 v[172:173], v[164:165], v[164:165]\n"
"v_pk_mul_f32 v[174:175], v[166:167], v[166:167]\n"
"v_pk_fma_f32 v[168:169], v[168:169], s[46:47], v[40:41]\n"
"v_pk_fma_f32 v[170:171], v[170:171], s[46:47], v[40:41]\n"
"v_pk_fma_f32 v[172:173], v[172:173], s[46:47], v[40:41]\n"
"v_pk_fma_f32 v[174:175], v[174:175], s[46:47], v[40:41]\n"
"v_pk_mul_f32 v[168:169], v[168:169], v[160:161]\n"
"v_pk_mul_f32 v[170:171], v[170:171], v[162:163]\n"
"v_pk_mul_f32 v[172:173], v[172:173], v[164:165]\n"
"v_pk_mul_f32 v[174:175], v[174:175], v[166:167]\n"
"v_exp_f32 v168, v168\n"
"v_exp_f32 v169, v169\n"
"v_exp_f32 v170, v170\n"
"v_exp_f32 v171, v171\n"
"v_exp_f32 v172, v172\n"
"v_exp_f32 v173, v173\n"
"v_exp_f32 v174, v174\n"
"v_exp_f32 v175, v175\n"
"v_pk_add_f32 v[168:169], v[168:169], v[42:43]\n"
"v_pk_add_f32 v[170:171], v[170:171], v[42:43]\n"
"v_pk_add_f32 v[172:173], v[172:173], v[42:43]\n"
"v_pk_add_f32 v[174:175], v[174:175], v[42:43]\n"
"v_rcp_f32 v168, v168\n"
"v_rcp_f32 v169, v169\n"
"v_rcp_f32 v170, v170\n"
"v_rcp_f32 v171, v171\n"
"v_rcp_f32 v172, v172\n"
"v_rcp_f32 v173, v173\n"
"v_rcp_f32 v174, v174\n"
"v_rcp_f32 v175, v175\n"
"v_pk_mul_f32 v[160:161], v[160:161], v[168:169]\n"
"v_pk_mul_f32 v[162:163], v[162:163], v[170:171]\n"
"v_pk_mul_f32 v[164:165], v[164:165], v[172:173]\n"
"v_pk_mul_f32 v[166:167], v[166:167], v[174:175]\n"
"v_cvt_pk_bf16_f32 v176, v160, v161\n"
"v_cvt_pk_bf16_f32 v177, v162, v163\n"
"v_cvt_pk_bf16_f32 v178, v164, v165\n"
"v_cvt_pk_bf16_f32 v179, v166, v167\n"
"buffer_store_dwordx4 v[176:179], %[vL16], %[sWS], s48 offen offset:1024 sc1\n"
"v_accvgpr_read_b32 v128, a8\n"
"v_accvgpr_read_b32 v129, a9\n"
"v_accvgpr_read_b32 v130, a10\n"
"v_accvgpr_read_b32 v131, a11\n"
"v_accvgpr_read_b32 v132, a32\n"
"v_accvgpr_read_b32 v133, a33\n"
"v_accvgpr_read_b32 v134, a34\n"
"v_accvgpr_read_b32 v135, a35\n"
"v_pk_mul_f32 v[136:137], v[128:129], v[128:129]\n"
"v_pk_mul_f32 v[138:139], v[130:131], v[130:131]\n"
"v_pk_mul_f32 v[140:141], v[132:133], v[132:133]\n"
"v_pk_mul_f32 v[142:143], v[134:135], v[134:135]\n"
"v_pk_fma_f32 v[136:137], v[136:137], s[46:47], v[40:41]\n"
"v_pk_fma_f32 v[138:139], v[138:139], s[46:47], v[40:41]\n"
"v_pk_fma_f32 v[140:141], v[140:141], s[46:47], v[40:41]\n"
"v_pk_fma_f32 v[142:143], v[142:143], s[46:47], v[40:41]\n"
"v_pk_mul_f32 v[136:137], v[136:137], v[128:129]\n"
"v_pk_mul_f32 v[138:139], v[138:139], v[130:131]\n"
"v_pk_mul_f32 v[140:141], v[140:141], v[132:133]\n"
"v_pk_mul_f32 v[142:143], v[142:143], v[134:135]\n"
"v_exp_f32 v136, v136\n"
"v_exp_f32 v137, v137\n"
"v_exp_f32 v138, v138\n"
"v_exp_f32 v139, v139\n"
"v_exp_f32 v140, v140\n"
"v_exp_f32 v141, v141\n"
"v_exp_f32 v142, v142\n"
"v_exp_f32 v143, v143\n"
"v_pk_add_f32 v[136:137], v[136:137], v[42:43]\n"
"v_pk_add_f32 v[138:139], v[138:139], v[42:43]\n"
"v_pk_add_f32 v[140:141], v[140:141], v[42:43]\n"
"v_pk_add_f32 v[142:143], v[142:143], v[42:43]\n"
"v_rcp_f32 v136, v136\n"
"v_rcp_f32 v137, v137\n"
"v_rcp_f32 v138, v138\n"
"v_rcp_f32 v139, v139\n"
"v_rcp_f32 v140, v140\n"
"v_rcp_f32 v141, v141\n"
"v_rcp_f32 v142, v142\n"
"v_rcp_f32 v143, v143\n"
"v_pk_mul_f32 v[128:129], v[128:129], v[136:137]\n"
"v_pk_mul_f32 v[130:131], v[130:131], v[138:139]\n"
"v_pk_mul_f32 v[132:133], v[132:133], v[140:141]\n"
"v_pk_mul_f32 v[134:135], v[134:135], v[142:143]\n"
"v_cvt_pk_bf16_f32 v144, v128, v129\n"
"v_cvt_pk_bf16_f32 v145, v130, v131\n"
"v_cvt_pk_bf16_f32 v146, v132, v133\n"
"v_cvt_pk_bf16_f32 v147, v134, v135\n"
"buffer_store_dwordx4 v[144:147], %[vL16], %[sWS], s48 offen offset:2048 sc1\n"
"v_accvgpr_read_b32 v160, a12\n"
"v_accvgpr_read_b32 v161, a13\n"
"v_accvgpr_read_b32 v162, a14\n"
"v_accvgpr_read_b32 v163, a15\n"
"v_accvgpr_read_b32 v164, a36\n"
"v_accvgpr_read_b32 v165, a37\n"
"v_accvgpr_read_b32 v166, a38\n"
"v_accvgpr_read_b32 v167, a39\n"
"v_pk_mul_f32 v[168:169], v[160:161], v[160:161]\n"
"v_pk_mul_f32 v[170:171], v[162:163], v[162:163]\n"
"v_pk_mul_f32 v[172:173], v[164:165], v[164:165]\n"
"v_pk_mul_f32 v[174:175], v[166:167], v[166:167]\n"
"v_pk_fma_f32 v[168:169], v[168:169], s[46:47], v[40:41]\n"
"v_pk_fma_f32 v[170:171], v[170:171], s[46:47], v[40:41]\n"
"v_pk_fma_f32 v[172:173], v[172:173], s[46:47], v[40:41]\n"
"v_pk_fma_f32 v[174:175], v[174:175], s[46:47], v[40:41]\n"
"v_pk_mul_f32 v[168:169], v[168:169], v[160:161]\n"
"v_pk_mul_f32 v[170:171], v[170:171], v[162:163]\n"
"v_pk_mul_f32 v[172:173], v[172:173], v[164:165]\n"
"v_pk_mul_f32 v[174:175], v[174:175], v[166:167]\n"
"v_exp_f32 v168, v168\n"
"v_exp_f32 v169, v169\n"
"v_exp_f32 v170, v170\n"
"v_exp_f32 v171, v171\n"
"v_exp_f32 v172, v172\n"
"v_exp_f32 v173, v173\n"
"v_exp_f32 v174, v174\n"
"v_exp_f32 v175, v175\n"
"v_pk_add_f32 v[168:169], v[168:169], v[42:43]\n"
"v_pk_add_f32 v[170:171], v[170:171], v[42:43]\n"
"v_pk_add_f32 v[172:173], v[172:173], v[42:43]\n"
"v_pk_add_f32 v[174:175], v[174:175], v[42:43]\n"
"v_rcp_f32 v168, v168\n"
"v_rcp_f32 v169, v169\n"
"v_rcp_f32 v170, v170\n"
"v_rcp_f32 v171, v171\n"
"v_rcp_f32 v172, v172\n"
"v_rcp_f32 v173, v173\n"
"v_rcp_f32 v174, v174\n"
"v_rcp_f32 v175, v175\n"
"v_pk_mul_f32 v[160:161], v[160:161], v[168:169]\n"
"v_pk_mul_f32 v[162:163], v[162:163], v[170:171]\n"
"v_pk_mul_f32 v[164:165], v[164:165], v[172:173]\n"
"v_pk_mul_f32 v[166:167], v[166:167], v[174:175]\n"
"v_cvt_pk_bf16_f32 v176, v160, v161\n"
"v_cvt_pk_bf16_f32 v177, v162, v163\n"
"v_cvt_pk_bf16_f32 v178, v164, v165\n"
"v_cvt_pk_bf16_f32 v179, v166, v167\n"
"buffer_store_dwordx4 v[176:179], %[vL16], %[sWS], s48 offen offset:3072 sc1\n"
"v_accvgpr_read_b32 v128, a16\n"
"v_accvgpr_read_b32 v129, a17\n"
"v_accvgpr_read_b32 v130, a18\n"
"v_accvgpr_read_b32 v131, a19\n"
"v_accvgpr_read_b32 v132, a40\n"
"v_accvgpr_read_b32 v133, a41\n"
"v_accvgpr_read_b32 v134, a42\n"
"v_accvgpr_read_b32 v135, a43\n"
"v_pk_mul_f32 v[136:137], v[128:129], v[128:129]\n"
"v_pk_mul_f32 v[138:139], v[130:131], v[130:131]\n"
"v_pk_mul_f32 v[140:141], v[132:133], v[132:133]\n"
"v_pk_mul_f32 v[142:143], v[134:135], v[134:135]\n"
"v_pk_fma_f32 v[136:137], v[136:137], s[46:47], v[40:41]\n"
"v_pk_fma_f32 v[138:139], v[138:139], s[46:47], v[40:41]\n"
"v_pk_fma_f32 v[140:141], v[140:141], s[46:47], v[40:41]\n"
"v_pk_fma_f32 v[142:143], v[142:143], s[46:47], v[40:41]\n"
"v_pk_mul_f32 v[136:137], v[136:137], v[128:129]\n"
"v_pk_mul_f32 v[138:139], v[138:139], v[130:131]\n"
"v_pk_mul_f32 v[140:141], v[140:141], v[132:133]\n"
"v_pk_mul_f32 v[142:143], v[142:143], v[134:135]\n"
"v_exp_f32 v136, v136\n"
"v_exp_f32 v137, v137\n"
"v_exp_f32 v138, v138\n"
"v_exp_f32 v139, v139\n"
"v_exp_f32 v140, v140\n"
"v_exp_f32 v141, v141\n"
"v_exp_f32 v142, v142\n"
"v_exp_f32 v143, v143\n"
"v_pk_add_f32 v[136:137], v[136:137], v[42:43]\n"
"v_pk_add_f32 v[138:139], v[138:139], v[42:43]\n"
"v_pk_add_f32 v[140:141], v[140:141], v[42:43]\n"
"v_pk_add_f32 v[142:143], v[142:143], v[42:43]\n"
"v_rcp_f32 v136, v136\n"
"v_rcp_f32 v137, v137\n"
"v_rcp_f32 v138, v138\n"
"v_rcp_f32 v139, v139\n"
"v_rcp_f32 v140, v140\n"
"v_rcp_f32 v141, v141\n"
"v_rcp_f32 v142, v142\n"
"v_rcp_f32 v143, v143\n"
"v_pk_mul_f32 v[128:129], v[128:129], v[136:137]\n"
"v_pk_mul_f32 v[130:131], v[130:131], v[138:139]\n"
"v_pk_mul_f32 v[132:133], v[132:133], v[140:141]\n"
"v_pk_mul_f32 v[134:135], v[134:135], v[142:143]\n"
"v_cvt_pk_bf16_f32 v144, v128, v129\n"
"v_cvt_pk_bf16_f32 v145, v130, v131\n"
"v_cvt_pk_bf16_f32 v146, v132, v133\n"
"v_cvt_pk_bf16_f32 v147, v134, v135\n"
"s_add_u32 s48, s48, 4096\n"
"buffer_store_dwordx4 v[144:147], %[vL16], %[sWS], s48 offen offset:0 sc1\n"
"v_accvgpr_read_b32 v160, a20\n"
"v_accvgpr_read_b32 v161, a21\n"
"v_accvgpr_read_b32 v162, a22\n"
"v_accvgpr_read_b32 v163, a23\n"
"v_accvgpr_read_b32 v164, a44\n"
"v_accvgpr_read_b32 v165, a45\n"
"v_accvgpr_read_b32 v166, a46\n"
"v_accvgpr_read_b32 v167, a47\n"
"v_pk_mul_f32 v[168:169], v[160:161], v[160:161]\n"
"v_pk_mul_f32 v[170:171], v[162:163], v[162:163]\n"
"v_pk_mul_f32 v[172:173], v[164:165], v[164:165]\n"
"v_pk_mul_f32 v[174:175], v[166:167], v[166:167]\n"
"v_pk_fma_f32 v[168:169], v[168:169], s[46:47], v[40:41]\n"
"v_pk_fma_f32 v[170:171], v[170:171], s[46:47], v[40:41]\n"
"v_pk_fma_f32 v[172:173], v[172:173], s[46:47], v[40:41]\n"
"v_pk_fma_f32 v[174:175], v[174:175], s[46:47], v[40:41]\n"
"v_pk_mul_f32 v[168:169], v[168:169], v[160:161]\n"
"v_pk_mul_f32 v[170:171], v[170:171], v[162:163]\n"
"v_pk_mul_f32 v[172:173], v[172:173], v[164:165]\n"
"v_pk_mul_f32 v[174:175], v[174:175], v[166:167]\n"
"v_exp_f32 v168, v168\n"
"v_exp_f32 v169, v169\n"
"v_exp_f32 v170, v170\n"
"v_exp_f32 v171, v171\n"
"v_exp_f32 v172, v172\n"
"v_exp_f32 v173, v173\n"
"v_exp_f32 v174, v174\n"
"v_exp_f32 v175, v175\n"
"v_pk_add_f32 v[168:169], v[168:169], v[42:43]\n"
"v_pk_add_f32 v[170:171], v[170:171], v[42:43]\n"
"v_pk_add_f32 v[172:173], v[172:173], v[42:43]\n"
"v_pk_add_f32 v[174:175], v[174:175], v[42:43]\n"
"v_rcp_f32 v168, v168\n"
"v_rcp_f32 v169, v169\n"
"v_rcp_f32 v170, v170\n"
"v_rcp_f32 v171, v171\n"
"v_rcp_f32 v172, v172\n"
"v_rcp_f32 v173, v173\n"
"v_rcp_f32 v174, v174\n"
"v_rcp_f32 v175, v175\n"
"v_pk_mul_f32 v[160:161], v[160:161], v[168:169]\n"
"v_pk_mul_f32 v[162:163], v[162:163], v[170:171]\n"
"v_pk_mul_f32 v[164:165], v[164:165], v[172:173]\n"
"v_pk_mul_f32 v[166:167], v[166:167], v[174:175]\n"
"v_cvt_pk_bf16_f32 v176, v160, v161\n"
"v_cvt_pk_bf16_f32 v177, v162, v163\n"
"v_cvt_pk_bf16_f32 v178, v164, v165\n"
"v_cvt_pk_bf16_f32 v179, v166, v167\n"
"buffer_store_dwordx4 v[176:179], %[vL16], %[sWS], s48 offen offset:1024 sc1\n"
"s_lshr_b32 s40, %[sT0], 1\n"
"s_add_u32 s40, s40, 1\n"
"s_lshr_b32 s42, s40, 1\n"
"s_mul_i32 s42, s42, 2\n"
"s_and_b32 s40, s40, 1\n"
"s_add_u32 s40, s40, s42\n"
"s_mul_i32 s40, s40, 98304\n"
"s_add_u32 s50, s40, %[sUOFF]\n"
"s_lshr_b32 s48, s50, 21\n"
"s_lshr_b32 s48, s51, s48\n"
"s_and_b32 s48, s48, 1\n"
"s_lshl_b32 s48, s48, 13\n"
"s_add_u32 s48, s48, s50\n"
"v_accvgpr_read_b32 v128, a48\n"
"v_accvgpr_read_b32 v129, a49\n"
"v_accvgpr_read_b32 v130, a50\n"
"v_accvgpr_read_b32 v131, a51\n"
"v_accvgpr_read_b32 v132, a72\n"
"v_accvgpr_read_b32 v133, a73\n"
"v_accvgpr_read_b32 v134, a74\n"
"v_accvgpr_read_b32 v135, a75\n"
"v_pk_mul_f32 v[136:137], v[128:129], v[128:129]\n"
"v_pk_mul_f32 v[138:139], v[130:131], v[130:131]\n"
"v_pk_mul_f32 v[140:141], v[132:133], v[132:133]\n"
"v_pk_mul_f32 v[142:143], v[134:135], v[134:135]\n"
"v_pk_fma_f32 v[136:137], v[136:137], s[46:47], v[40:41]\n"
"v_pk_fma_f32 v[138:139], v[138:139], s[46:47], v[40:41]\n"
"v_pk_fma_f32 v[140:141], v[140:141], s[46:47], v[40:41]\n"
"v_pk_fma_f32 v[142:143], v[142:143], s[46:47], v[40:41]\n"
"v_pk_mul_f32 v[136:137], v[136:137], v[128:129]\n"
"v_pk_mul_f32 v[138:139], v[138:139], v[130:131]\n"
"v_pk_mul_f32 v[140:141], v[140:141], v[132:133]\n"
"v_pk_mul_f32 v[142:143], v[142:143], v[134:135]\n"
"v_exp_f32 v136, v136\n"
"v_exp_f32 v137, v137\n"
"v_exp_f32 v138, v138\n"
"v_exp_f32 v139, v139\n"
"v_exp_f32 v140, v140\n"
"v_exp_f32 v141, v141\n"
"v_exp_f32 v142, v142\n"
"v_exp_f32 v143, v143\n"
"v_pk_add_f32 v[136:137], v[136:137], v[42:43]\n"
"v_pk_add_f32 v[138:139], v[138:139], v[42:43]\n"
"v_pk_add_f32 v[140:141], v[140:141], v[42:43]\n"
"v_pk_add_f32 v[142:143], v[142:143], v[42:43]\n"
"v_rcp_f32 v136, v136\n"
"v_rcp_f32 v137, v137\n"
"v_rcp_f32 v138, v138\n"
"v_rcp_f32 v139, v139\n"
"v_rcp_f32 v140, v140\n"
"v_rcp_f32 v141, v141\n"
"v_rcp_f32 v142, v142\n"
"v_rcp_f32 v143, v143\n"
"v_pk_mul_f32 v[128:129], v[128:129], v[136:137]\n"
"v_pk_mul_f32 v[130:131], v[130:131], v[138:139]\n"
"v_pk_mul_f32 v[132:133], v[132:133], v[140:141]\n"
"v_pk_mul_f32 v[134:135], v[134:135], v[142:143]\n"
"v_cvt_pk_bf16_f32 v144, v128, v129\n"
"v_cvt_pk_bf16_f32 v145, v130, v131\n"
"v_cvt_pk_bf16_f32 v146, v132, v133\n"
"v_cvt_pk_bf16_f32 v147, v134, v135\n"
"buffer_store_dwordx4 v[144:147], %[vL16], %[sWS], s48 offen offset:0 sc1\n"
"v_accvgpr_read_b32 v160, a52\n"
"v_accvgpr_read_b32 v161, a53\n"
"v_accvgpr_read_b32 v162, a54\n"
"v_accvgpr_read_b32 v163, a55\n"
"v_accvgpr_read_b32 v164, a76\n"
"v_accvgpr_read_b32 v165, a77\n"
"v_accvgpr_read_b32 v166, a78\n"
"v_accvgpr_read_b32 v167, a79\n"
"v_pk_mul_f32 v[168:169], v[160:161], v[160:161]\n"
"v_pk_mul_f32 v[170:171], v[162:163], v[162:163]\n"
"v_pk_mul_f32 v[172:173], v[164:165], v[164:165]\n"
"v_pk_mul_f32 v[174:175], v[166:167], v[166:167]\n"
"v_pk_fma_f32 v[168:169], v[168:169], s[46:47], v[40:41]\n"
"v_pk_fma_f32 v[170:171], v[170:171], s[46:47], v[40:41]\n"
"v_pk_fma_f32 v[172:173], v[172:173], s[46:47], v[40:41]\n"
"v_pk_fma_f32 v[174:175], v[174:175], s[46:47], v[40:41]\n"
"v_pk_mul_f32 v[168:169], v[168:169], v[160:161]\n"
"v_pk_mul_f32 v[170:171], v[170:171], v[162:163]\n"
"v_pk_mul_f32 v[172:173], v[172:173], v[164:165]\n"
"v_pk_mul_f32 v[174:175], v[174:175], v[166:167]\n"
"v_exp_f32 v168, v168\n"
"v_exp_f32 v169, v169\n"
"v_exp_f32 v170, v170\n"
"v_exp_f32 v171, v171\n"
"v_exp_f32 v172, v172\n"
"v_exp_f32 v173, v173\n"
"v_exp_f32 v174, v174\n"
"v_exp_f32 v175, v175\n"
"v_pk_add_f32 v[168:169], v[168:169], v[42:43]\n"
"v_pk_add_f32 v[170:171], v[170:171], v[42:43]\n"
"v_pk_add_f32 v[172:173], v[172:173], v[42:43]\n"
"v_pk_add_f32 v[174:175], v[174:175], v[42:43]\n"
"v_rcp_f32 v168, v168\n"
"v_rcp_f32 v169, v169\n"
"v_rcp_f32 v170, v170\n"
"v_rcp_f32 v171, v171\n"
"v_rcp_f32 v172, v172\n"
"v_rcp_f32 v173, v173\n"
"v_rcp_f32 v174, v174\n"
"v_rcp_f32 v175, v175\n"
"v_pk_mul_f32 v[160:161], v[160:161], v[168:169]\n"
"v_pk_mul_f32 v[162:163], v[162:163], v[170:171]\n"
"v_pk_mul_f32 v[164:165], v[164:165], v[172:173]\n"
"v_pk_mul_f32 v[166:167], v[166:167], v[174:175]\n"
"v_cvt_pk_bf16_f32 v176, v160, v161\n"
"v_cvt_pk_bf16_f32 v177, v162, v163\n"
"v_cvt_pk_bf16_f32 v178, v164, v165\n"
"v_cvt_pk_bf16_f32 v179, v166, v167\n"
"buffer_store_dwordx4 v[176:179], %[vL16], %[sWS], s48 offen offset:1024 sc1\n"
"v_accvgpr_read_b32 v128, a56\n"
"v_accvgpr_read_b32 v129, a57\n"
"v_accvgpr_read_b32 v130, a58\n"
"v_accvgpr_read_b32 v131, a59\n"
"v_accvgpr_read_b32 v132, a80\n"
"v_accvgpr_read_b32 v133, a81\n"
"v_accvgpr_read_b32 v134, a82\n"
"v_accvgpr_read_b32 v135, a83\n"
"v_pk_mul_f32 v[136:137], v[128:129], v[128:129]\n"
"v_pk_mul_f32 v[138:139], v[130:131], v[130:131]\n"
"v_pk_mul_f32 v[140:141], v[132:133], v[132:133]\n"
"v_pk_mul_f32 v[142:143], v[134:135], v[134:135]\n"
"v_pk_fma_f32 v[136:137], v[136:137], s[46:47], v[40:41]\n"
"v_pk_fma_f32 v[138:139], v[138:139], s[46:47], v[40:41]\n"
"v_pk_fma_f32 v[140:141], v[140:141], s[46:47], v[40:41]\n"
"v_pk_fma_f32 v[142:143], v[142:143], s[46:47], v[40:41]\n"
"v_pk_mul_f32 v[136:137], v[136:137], v[128:129]\n"
"v_pk_mul_f32 v[138:139], v[138:139], v[130:131]\n"
"v_pk_mul_f32 v[140:141], v[140:141], v[132:133]\n"
"v_pk_mul_f32 v[142:143], v[142:143], v[134:135]\n"
"v_exp_f32 v136, v136\n"
"v_exp_f32 v137, v137\n"
"v_exp_f32 v138, v138\n"
"v_exp_f32 v139, v139\n"
"v_exp_f32 v140, v140\n"
"v_exp_f32 v141, v141\n"
"v_exp_f32 v142, v142\n"
"v_exp_f32 v143, v143\n"
"v_pk_add_f32 v[136:137], v[136:137], v[42:43]\n"
"v_pk_add_f32 v[138:139], v[138:139], v[42:43]\n"
"v_pk_add_f32 v[140:141], v[140:141], v[42:43]\n"
"v_pk_add_f32 v[142:143], v[142:143], v[42:43]\n"
"v_rcp_f32 v136, v136\n"
"v_rcp_f32 v137, v137\n"
"v_rcp_f32 v138, v138\n"
"v_rcp_f32 v139, v139\n"
"v_rcp_f32 v140, v140\n"
"v_rcp_f32 v141, v141\n"
"v_rcp_f32 v142, v142\n"
"v_rcp_f32 v143, v143\n"
"v_pk_mul_f32 v[128:129], v[128:129], v[136:137]\n"
"v_pk_mul_f32 v[130:131], v[130:131], v[138:139]\n"
"v_pk_mul_f32 v[132:133], v[132:133], v[140:141]\n"
"v_pk_mul_f32 v[134:135], v[134:135], v[142:143]\n"
"v_cvt_pk_bf16_f32 v144, v128, v129\n"
"v_cvt_pk_bf16_f32 v145, v130, v131\n"
"v_cvt_pk_bf16_f32 v146, v132, v133\n"
"v_cvt_pk_bf16_f32 v147, v134, v135\n"
"buffer_store_dwordx4 v[144:147], %[vL16], %[sWS], s48 offen offset:2048 sc1\n"
"v_accvgpr_read_b32 v160, a60\n"
"v_accvgpr_read_b32 v161, a61\n"
"v_accvgpr_read_b32 v162, a62\n"
"v_accvgpr_read_b32 v163, a63\n"
"v_accvgpr_read_b32 v164, a84\n"
"v_accvgpr_read_b32 v165, a85\n"
"v_accvgpr_read_b32 v166, a86\n"
"v_accvgpr_read_b32 v167, a87\n"
"v_pk_mul_f32 v[168:169], v[160:161], v[160:161]\n"
"v_pk_mul_f32 v[170:171], v[162:163], v[162:163]\n"
"v_pk_mul_f32 v[172:173], v[164:165], v[164:165]\n"
"v_pk_mul_f32 v[174:175], v[166:167], v[166:167]\n"
"v_pk_fma_f32 v[168:169], v[168:169], s[46:47], v[40:41]\n"
"v_pk_fma_f32 v[170:171], v[170:171], s[46:47], v[40:41]\n"
"v_pk_fma_f32 v[172:173], v[172:173], s[46:47], v[40:41]\n"
"v_pk_fma_f32 v[174:175], v[174:175], s[46:47], v[40:41]\n"
"v_pk_mul_f32 v[168:169], v[168:169], v[160:161]\n"
"v_pk_mul_f32 v[170:171], v[170:171], v[162:163]\n"
"v_pk_mul_f32 v[172:173], v[172:173], v[164:165]\n"
"v_pk_mul_f32 v[174:175], v[174:175], v[166:167]\n"
"v_exp_f32 v168, v168\n"
"v_exp_f32 v169, v169\n"
"v_exp_f32 v170, v170\n"
"v_exp_f32 v171, v171\n"
"v_exp_f32 v172, v172\n"
"v_exp_f32 v173, v173\n"
"v_exp_f32 v174, v174\n"
"v_exp_f32 v175, v175\n"
"v_pk_add_f32 v[168:169], v[168:169], v[42:43]\n"
"v_pk_add_f32 v[170:171], v[170:171], v[42:43]\n"
"v_pk_add_f32 v[172:173], v[172:173], v[42:43]\n"
"v_pk_add_f32 v[174:175], v[174:175], v[42:43]\n"
"v_rcp_f32 v168, v168\n"
"v_rcp_f32 v169, v169\n"
"v_rcp_f32 v170, v170\n"
"v_rcp_f32 v171, v171\n"
"v_rcp_f32 v172, v172\n"
"v_rcp_f32 v173, v173\n"
"v_rcp_f32 v174, v174\n"
"v_rcp_f32 v175, v175\n"
"v_pk_mul_f32 v[160:161], v[160:161], v[168:169]\n"
"v_pk_mul_f32 v[162:163], v[162:163], v[170:171]\n"
"v_pk_mul_f32 v[164:165], v[164:165], v[172:173]\n"
"v_pk_mul_f32 v[166:167], v[166:167], v[174:175]\n"
"v_cvt_pk_bf16_f32 v176, v160, v161\n"
"v_cvt_pk_bf16_f32 v177, v162, v163\n"
"v_cvt_pk_bf16_f32 v178, v164, v165\n"
"v_cvt_pk_bf16_f32 v179, v166, v167\n"
"buffer_store_dwordx4 v[176:179], %[vL16], %[sWS], s48 offen offset:3072 sc1\n"
"v_accvgpr_read_b32 v128, a64\n"
"v_accvgpr_read_b32 v129, a65\n"
"v_accvgpr_read_b32 v130, a66\n"
"v_accvgpr_read_b32 v131, a67\n"
"v_accvgpr_read_b32 v132, a88\n"
"v_accvgpr_read_b32 v133, a89\n"
"v_accvgpr_read_b32 v134, a90\n"
"v_accvgpr_read_b32 v135, a91\n"
"v_pk_mul_f32 v[136:137], v[128:129], v[128:129]\n"
"v_pk_mul_f32 v[138:139], v[130:131], v[130:131]\n"
"v_pk_mul_f32 v[140:141], v[132:133], v[132:133]\n"
"v_pk_mul_f32 v[142:143], v[134:135], v[134:135]\n"
"v_pk_fma_f32 v[136:137], v[136:137], s[46:47], v[40:41]\n"
"v_pk_fma_f32 v[138:139], v[138:139], s[46:47], v[40:41]\n"
"v_pk_fma_f32 v[140:141], v[140:141], s[46:47], v[40:41]\n"
"v_pk_fma_f32 v[142:143], v[142:143], s[46:47], v[40:41]\n"
"v_pk_mul_f32 v[136:137], v[136:137], v[128:129]\n"
"v_pk_mul_f32 v[138:139], v[138:139], v[130:131]\n"
"v_pk_mul_f32 v[140:141], v[140:141], v[132:133]\n"
"v_pk_mul_f32 v[142:143], v[142:143], v[134:135]\n"
"v_exp_f32 v136, v136\n"
"v_exp_f32 v137, v137\n"
"v_exp_f32 v138, v138\n"
"v_exp_f32 v139, v139\n"
"v_exp_f32 v140, v140\n"
"v_exp_f32 v141, v141\n"
"v_exp_f32 v142, v142\n"
"v_exp_f32 v143, v143\n"
"v_pk_add_f32 v[136:137], v[136:137], v[42:43]\n"
"v_pk_add_f32 v[138:139], v[138:139], v[42:43]\n"
"v_pk_add_f32 v[140:141], v[140:141], v[42:43]\n"
"v_pk_add_f32 v[142:143], v[142:143], v[42:43]\n"
"v_rcp_f32 v136, v136\n"
"v_rcp_f32 v137, v137\n"
"v_rcp_f32 v138, v138\n"
"v_rcp_f32 v139, v139\n"
"v_rcp_f32 v140, v140\n"
"v_rcp_f32 v141, v141\n"
"v_rcp_f32 v142, v142\n"
"v_rcp_f32 v143, v143\n"
"v_pk_mul_f32 v[128:129], v[128:129], v[136:137]\n"
"v_pk_mul_f32 v[130:131], v[130:131], v[138:139]\n"
"v_pk_mul_f32 v[132:133], v[132:133], v[140:141]\n"
"v_pk_mul_f32 v[134:135], v[134:135], v[142:143]\n"
"v_cvt_pk_bf16_f32 v144, v128, v129\n"
"v_cvt_pk_bf16_f32 v145, v130, v131\n"
"v_cvt_pk_bf16_f32 v146, v132, v133\n"
"v_cvt_pk_bf16_f32 v147, v134, v135\n"
"s_add_u32 s48, s48, 4096\n"
"buffer_store_dwordx4 v[144:147], %[vL16], %[sWS], s48 offen offset:0 sc1\n"
"v_accvgpr_read_b32 v160, a68\n"
"v_accvgpr_read_b32 v161, a69\n"
"v_accvgpr_read_b32 v162, a70\n"
"v_accvgpr_read_b32 v163, a71\n"
"v_accvgpr_read_b32 v164, a92\n"
"v_accvgpr_read_b32 v165, a93\n"
"v_accvgpr_read_b32 v166, a94\n"
"v_accvgpr_read_b32 v167, a95\n"
"v_pk_mul_f32 v[168:169], v[160:161], v[160:161]\n"
"v_pk_mul_f32 v[170:171], v[162:163], v[162:163]\n"
"v_pk_mul_f32 v[172:173], v[164:165], v[164:165]\n"
"v_pk_mul_f32 v[174:175], v[166:167], v[166:167]\n"
"v_pk_fma_f32 v[168:169], v[168:169], s[46:47], v[40:41]\n"
"v_pk_fma_f32 v[170:171], v[170:171], s[46:47], v[40:41]\n"
"v_pk_fma_f32 v[172:173], v[172:173], s[46:47], v[40:41]\n"
"v_pk_fma_f32 v[174:175], v[174:175], s[46:47], v[40:41]\n"
"v_pk_mul_f32 v[168:169], v[168:169], v[160:161]\n"
"v_pk_mul_f32 v[170:171], v[170:171], v[162:163]\n"
"v_pk_mul_f32 v[172:173], v[172:173], v[164:165]\n"
"v_pk_mul_f32 v[174:175], v[174:175], v[166:167]\n"
"v_exp_f32 v168, v168\n"
"v_exp_f32 v169, v169\n"
"v_exp_f32 v170, v170\n"
"v_exp_f32 v171, v171\n"
"v_exp_f32 v172, v172\n"
"v_exp_f32 v173, v173\n"
"v_exp_f32 v174, v174\n"
"v_exp_f32 v175, v175\n"
"v_pk_add_f32 v[168:169], v[168:169], v[42:43]\n"
"v_pk_add_f32 v[170:171], v[170:171], v[42:43]\n"
"v_pk_add_f32 v[172:173], v[172:173], v[42:43]\n"
"v_pk_add_f32 v[174:175], v[174:175], v[42:43]\n"
"v_rcp_f32 v168, v168\n"
"v_rcp_f32 v169, v169\n"
"v_rcp_f32 v170, v170\n"
"v_rcp_f32 v171, v171\n"
"v_rcp_f32 v172, v172\n"
"v_rcp_f32 v173, v173\n"
"v_rcp_f32 v174, v174\n"
"v_rcp_f32 v175, v175\n"
"v_pk_mul_f32 v[160:161], v[160:161], v[168:169]\n"
"v_pk_mul_f32 v[162:163], v[162:163], v[170:171]\n"
"v_pk_mul_f32 v[164:165], v[164:165], v[172:173]\n"
"v_pk_mul_f32 v[166:167], v[166:167], v[174:175]\n"
"v_cvt_pk_bf16_f32 v176, v160, v161\n"
"v_cvt_pk_bf16_f32 v177, v162, v163\n"
"v_cvt_pk_bf16_f32 v178, v164, v165\n"
"v_cvt_pk_bf16_f32 v179, v166, v167\n"
"buffer_store_dwordx4 v[176:179], %[vL16], %[sWS], s48 offen offset:1024 sc1\n"
"s_lshr_b32 s40, %[sT0], 1\n"
"s_add_u32 s40, s40, 2\n"
"s_lshr_b32 s42, s40, 1\n"
"s_mul_i32 s42, s42, 2\n"
"s_and_b32 s40, s40, 1\n"
"s_add_u32 s40, s40, s42\n"
"s_mul_i32 s40, s40, 98304\n"
"s_add_u32 s50, s40, %[sUOFF]\n"
"s_lshr_b32 s48, s50, 21\n"
"s_lshr_b32 s48, s51, s48\n"
"s_and_b32 s48, s48, 1\n"
"s_lshl_b32 s48, s48, 13\n"
"s_add_u32 s48, s48, s50\n"
"v_accvgpr_read_b32 v128, a96\n"
"v_accvgpr_read_b32 v129, a97\n"
"v_accvgpr_read_b32 v130, a98\n"
"v_accvgpr_read_b32 v131, a99\n"
"v_accvgpr_read_b32 v132, a120\n"
"v_accvgpr_read_b32 v133, a121\n"
"v_accvgpr_read_b32 v134, a122\n"
"v_accvgpr_read_b32 v135, a123\n"
"v_pk_mul_f32 v[136:137], v[128:129], v[128:129]\n"
"v_pk_mul_f32 v[138:139], v[130:131], v[130:131]\n"
"v_pk_mul_f32 v[140:141], v[132:133], v[132:133]\n"
"v_pk_mul_f32 v[142:143], v[134:135], v[134:135]\n"
"v_pk_fma_f32 v[136:137], v[136:137], s[46:47], v[40:41]\n"
"v_pk_fma_f32 v[138:139], v[138:139], s[46:47], v[40:41]\n"
"v_pk_fma_f32 v[140:141], v[140:141], s[46:47], v[40:41]\n"
"v_pk_fma_f32 v[142:143], v[142:143], s[46:47], v[40:41]\n"
"v_pk_mul_f32 v[136:137], v[136:137], v[128:129]\n"
"v_pk_mul_f32 v[138:139], v[138:139], v[130:131]\n"
"v_pk_mul_f32 v[140:141], v[140:141], v[132:133]\n"
"v_pk_mul_f32 v[142:143], v[142:143], v[134:135]\n"
"v_exp_f32 v136, v136\n"
"v_exp_f32 v137, v137\n"
"v_exp_f32 v138, v138\n"
"v_exp_f32 v139, v139\n"
"v_exp_f32 v140, v140\n"
"v_exp_f32 v141, v141\n"
"v_exp_f32 v142, v142\n"
"v_exp_f32 v143, v143\n"
"v_pk_add_f32 v[136:137], v[136:137], v[42:43]\n"
"v_pk_add_f32 v[138:139], v[138:139], v[42:43]\n"
"v_pk_add_f32 v[140:141], v[140:141], v[42:43]\n"
"v_pk_add_f32 v[142:143], v[142:143], v[42:43]\n"
"v_rcp_f32 v136, v136\n"
"v_rcp_f32 v137, v137\n"
"v_rcp_f32 v138, v138\n"
"v_rcp_f32 v139, v139\n"
"v_rcp_f32 v140, v140\n"
"v_rcp_f32 v141, v141\n"
"v_rcp_f32 v142, v142\n"
"v_rcp_f32 v143, v143\n"
"v_pk_mul_f32 v[128:129], v[128:129], v[136:137]\n"
"v_pk_mul_f32 v[130:131], v[130:131], v[138:139]\n"
"v_pk_mul_f32 v[132:133], v[132:133], v[140:141]\n"
"v_pk_mul_f32 v[134:135], v[134:135], v[142:143]\n"
"v_cvt_pk_bf16_f32 v144, v128, v129\n"
"v_cvt_pk_bf16_f32 v145, v130, v131\n"
"v_cvt_pk_bf16_f32 v146, v132, v133\n"
"v_cvt_pk_bf16_f32 v147, v134, v135\n"
"buffer_store_dwordx4 v[144:147], %[vL16], %[sWS], s48 offen offset:0 sc1\n"
"v_accvgpr_read_b32 v160, a100\n"
"v_accvgpr_read_b32 v161, a101\n"
"v_accvgpr_read_b32 v162, a102\n"
"v_accvgpr_read_b32 v163, a103\n"
"v_accvgpr_read_b32 v164, a124\n"
"v_accvgpr_read_b32 v165, a125\n"
"v_accvgpr_read_b32 v166, a126\n"
"v_accvgpr_read_b32 v167, a127\n"
"v_pk_mul_f32 v[168:169], v[160:161], v[160:161]\n"
"v_pk_mul_f32 v[170:171], v[162:163], v[162:163]\n"
"v_pk_mul_f32 v[172:173], v[164:165], v[164:165]\n"
"v_pk_mul_f32 v[174:175], v[166:167], v[166:167]\n"
"v_pk_fma_f32 v[168:169], v[168:169], s[46:47], v[40:41]\n"
"v_pk_fma_f32 v[170:171], v[170:171], s[46:47], v[40:41]\n"
"v_pk_fma_f32 v[172:173], v[172:173], s[46:47], v[40:41]\n"
"v_pk_fma_f32 v[174:175], v[174:175], s[46:47], v[40:41]\n"
"v_pk_mul_f32 v[168:169], v[168:169], v[160:161]\n"
"v_pk_mul_f32 v[170:171], v[170:171], v[162:163]\n"
"v_pk_mul_f32 v[172:173], v[172:173], v[164:165]\n"
"v_pk_mul_f32 v[174:175], v[174:175], v[166:167]\n"
"v_exp_f32 v168, v168\n"
"v_exp_f32 v169, v169\n"
"v_exp_f32 v170, v170\n"
"v_exp_f32 v171, v171\n"
"v_exp_f32 v172, v172\n"
"v_exp_f32 v173, v173\n"
"v_exp_f32 v174, v174\n"
"v_exp_f32 v175, v175\n"
"v_pk_add_f32 v[168:169], v[168:169], v[42:43]\n"
"v_pk_add_f32 v[170:171], v[170:171], v[42:43]\n"
"v_pk_add_f32 v[172:173], v[172:173], v[42:43]\n"
"v_pk_add_f32 v[174:175], v[174:175], v[42:43]\n"
"v_rcp_f32 v168, v168\n"
"v_rcp_f32 v169, v169\n"
"v_rcp_f32 v170, v170\n"
"v_rcp_f32 v171, v171\n"
"v_rcp_f32 v172, v172\n"
"v_rcp_f32 v173, v173\n"
"v_rcp_f32 v174, v174\n"
"v_rcp_f32 v175, v175\n"
"v_pk_mul_f32 v[160:161], v[160:161], v[168:169]\n"
"v_pk_mul_f32 v[162:163], v[162:163], v[170:171]\n"
"v_pk_mul_f32 v[164:165], v[164:165], v[172:173]\n"
"v_pk_mul_f32 v[166:167], v[166:167], v[174:175]\n"
"v_cvt_pk_bf16_f32 v176, v160, v161\n"
"v_cvt_pk_bf16_f32 v177, v162, v163\n"
"v_cvt_pk_bf16_f32 v178, v164, v165\n"
"v_cvt_pk_bf16_f32 v179, v166, v167\n"
"buffer_store_dwordx4 v[176:179], %[vL16], %[sWS], s48 offen offset:1024 sc1\n"
"v_accvgpr_read_b32 v128, a104\n"
"v_accvgpr_read_b32 v129, a105\n"
"v_accvgpr_read_b32 v130, a106\n"
"v_accvgpr_read_b32 v131, a107\n"
"v_accvgpr_read_b32 v132, a128\n"
"v_accvgpr_read_b32 v133, a129\n"
"v_accvgpr_read_b32 v134, a130\n"
"v_accvgpr_read_b32 v135, a131\n"
"v_pk_mul_f32 v[136:137], v[128:129], v[128:129]\n"
"v_pk_mul_f32 v[138:139], v[130:131], v[130:131]\n"
"v_pk_mul_f32 v[140:141], v[132:133], v[132:133]\n"
"v_pk_mul_f32 v[142:143], v[134:135], v[134:135]\n"
"v_pk_fma_f32 v[136:137], v[136:137], s[46:47], v[40:41]\n"
"v_pk_fma_f32 v[138:139], v[138:139], s[46:47], v[40:41]\n"
"v_pk_fma_f32 v[140:141], v[140:141], s[46:47], v[40:41]\n"
"v_pk_fma_f32 v[142:143], v[142:143], s[46:47], v[40:41]\n"
"v_pk_mul_f32 v[136:137], v[136:137], v[128:129]\n"
"v_pk_mul_f32 v[138:139], v[138:139], v[130:131]\n"
"v_pk_mul_f32 v[140:141], v[140:141], v[132:133]\n"
"v_pk_mul_f32 v[142:143], v[142:143], v[134:135]\n"
"v_exp_f32 v136, v136\n"
"v_exp_f32 v137, v137\n"
"v_exp_f32 v138, v138\n"
"v_exp_f32 v139, v139\n"
"v_exp_f32 v140, v140\n"
"v_exp_f32 v141, v141\n"
"v_exp_f32 v142, v142\n"
"v_exp_f32 v143, v143\n"
"v_pk_add_f32 v[136:137], v[136:137], v[42:43]\n"
"v_pk_add_f32 v[138:139], v[138:139], v[42:43]\n"
"v_pk_add_f32 v[140:141], v[140:141], v[42:43]\n"
"v_pk_add_f32 v[142:143], v[142:143], v[42:43]\n"
"v_rcp_f32 v136, v136\n"
"v_rcp_f32 v137, v137\n"
"v_rcp_f32 v138, v138\n"
"v_rcp_f32 v139, v139\n"
"v_rcp_f32 v140, v140\n"
"v_rcp_f32 v141, v141\n"
"v_rcp_f32 v142, v142\n"
"v_rcp_f32 v143, v143\n"
"v_pk_mul_f32 v[128:129], v[128:129], v[136:137]\n"
"v_pk_mul_f32 v[130:131], v[130:131], v[138:139]\n"
"v_pk_mul_f32 v[132:133], v[132:133], v[140:141]\n"
"v_pk_mul_f32 v[134:135], v[134:135], v[142:143]\n"
"v_cvt_pk_bf16_f32 v144, v128, v129\n"
"v_cvt_pk_bf16_f32 v145, v130, v131\n"
"v_cvt_pk_bf16_f32 v146, v132, v133\n"
"v_cvt_pk_bf16_f32 v147, v134, v135\n"
"buffer_store_dwordx4 v[144:147], %[vL16], %[sWS], s48 offen offset:2048 sc1\n"
"v_accvgpr_read_b32 v160, a108\n"
"v_accvgpr_read_b32 v161, a109\n"
"v_accvgpr_read_b32 v162, a110\n"
"v_accvgpr_read_b32 v163, a111\n"
"v_accvgpr_read_b32 v164, a132\n"
"v_accvgpr_read_b32 v165, a133\n"
"v_accvgpr_read_b32 v166, a134\n"
"v_accvgpr_read_b32 v167, a135\n"
"v_pk_mul_f32 v[168:169], v[160:161], v[160:161]\n"
"v_pk_mul_f32 v[170:171], v[162:163], v[162:163]\n"
"v_pk_mul_f32 v[172:173], v[164:165], v[164:165]\n"
"v_pk_mul_f32 v[174:175], v[166:167], v[166:167]\n"
"v_pk_fma_f32 v[168:169], v[168:169], s[46:47], v[40:41]\n"
"v_pk_fma_f32 v[170:171], v[170:171], s[46:47], v[40:41]\n"
"v_pk_fma_f32 v[172:173], v[172:173], s[46:47], v[40:41]\n"
"v_pk_fma_f32 v[174:175], v[174:175], s[46:47], v[40:41]\n"
"v_pk_mul_f32 v[168:169], v[168:169], v[160:161]\n"
"v_pk_mul_f32 v[170:171], v[170:171], v[162:163]\n"
"v_pk_mul_f32 v[172:173], v[172:173], v[164:165]\n"
"v_pk_mul_f32 v[174:175], v[174:175], v[166:167]\n"
"v_exp_f32 v168, v168\n"
"v_exp_f32 v169, v169\n"
"v_exp_f32 v170, v170\n"
"v_exp_f32 v171, v171\n"
"v_exp_f32 v172, v172\n"
"v_exp_f32 v173, v173\n"
"v_exp_f32 v174, v174\n"
"v_exp_f32 v175, v175\n"
"v_pk_add_f32 v[168:169], v[168:169], v[42:43]\n"
"v_pk_add_f32 v[170:171], v[170:171], v[42:43]\n"
"v_pk_add_f32 v[172:173], v[172:173], v[42:43]\n"
"v_pk_add_f32 v[174:175], v[174:175], v[42:43]\n"
"v_rcp_f32 v168, v168\n"
"v_rcp_f32 v169, v169\n"
"v_rcp_f32 v170, v170\n"
"v_rcp_f32 v171, v171\n"
"v_rcp_f32 v172, v172\n"
"v_rcp_f32 v173, v173\n"
"v_rcp_f32 v174, v174\n"
"v_rcp_f32 v175, v175\n"
"v_pk_mul_f32 v[160:161], v[160:161], v[168:169]\n"
"v_pk_mul_f32 v[162:163], v[162:163], v[170:171]\n"
"v_pk_mul_f32 v[164:165], v[164:165], v[172:173]\n"
"v_pk_mul_f32 v[166:167], v[166:167], v[174:175]\n"
"v_cvt_pk_bf16_f32 v176, v160, v161\n"
"v_cvt_pk_bf16_f32 v177, v162, v163\n"
"v_cvt_pk_bf16_f32 v178, v164, v165\n"
"v_cvt_pk_bf16_f32 v179, v166, v167\n"
"buffer_store_dwordx4 v[176:179], %[vL16], %[sWS], s48 offen offset:3072 sc1\n"
"v_accvgpr_read_b32 v128, a112\n"
"v_accvgpr_read_b32 v129, a113\n"
"v_accvgpr_read_b32 v130, a114\n"
"v_accvgpr_read_b32 v131, a115\n"
"v_accvgpr_read_b32 v132, a136\n"
"v_accvgpr_read_b32 v133, a137\n"
"v_accvgpr_read_b32 v134, a138\n"
"v_accvgpr_read_b32 v135, a139\n"
"v_pk_mul_f32 v[136:137], v[128:129], v[128:129]\n"
"v_pk_mul_f32 v[138:139], v[130:131], v[130:131]\n"
"v_pk_mul_f32 v[140:141], v[132:133], v[132:133]\n"
"v_pk_mul_f32 v[142:143], v[134:135], v[134:135]\n"
"v_pk_fma_f32 v[136:137], v[136:137], s[46:47], v[40:41]\n"
"v_pk_fma_f32 v[138:139], v[138:139], s[46:47], v[40:41]\n"
"v_pk_fma_f32 v[140:141], v[140:141], s[46:47], v[40:41]\n"
"v_pk_fma_f32 v[142:143], v[142:143], s[46:47], v[40:41]\n"
"v_pk_mul_f32 v[136:137], v[136:137], v[128:129]\n"
"v_pk_mul_f32 v[138:139], v[138:139], v[130:131]\n"
"v_pk_mul_f32 v[140:141], v[140:141], v[132:133]\n"
"v_pk_mul_f32 v[142:143], v[142:143], v[134:135]\n"
"v_exp_f32 v136, v136\n"
"v_exp_f32 v137, v137\n"
"v_exp_f32 v138, v138\n"
"v_exp_f32 v139, v139\n"
"v_exp_f32 v140, v140\n"
"v_exp_f32 v141, v141\n"
"v_exp_f32 v142, v142\n"
"v_exp_f32 v143, v143\n"
"v_pk_add_f32 v[136:137], v[136:137], v[42:43]\n"
"v_pk_add_f32 v[138:139], v[138:139], v[42:43]\n"
"v_pk_add_f32 v[140:141], v[140:141], v[42:43]\n"
"v_pk_add_f32 v[142:143], v[142:143], v[42:43]\n"
"v_rcp_f32 v136, v136\n"
"v_rcp_f32 v137, v137\n"
"v_rcp_f32 v138, v138\n"
"v_rcp_f32 v139, v139\n"
"v_rcp_f32 v140, v140\n"
"v_rcp_f32 v141, v141\n"
"v_rcp_f32 v142, v142\n"
"v_rcp_f32 v143, v143\n"
"v_pk_mul_f32 v[128:129], v[128:129], v[136:137]\n"
"v_pk_mul_f32 v[130:131], v[130:131], v[138:139]\n"
"v_pk_mul_f32 v[132:133], v[132:133], v[140:141]\n"
"v_pk_mul_f32 v[134:135], v[134:135], v[142:143]\n"
"v_cvt_pk_bf16_f32 v144, v128, v129\n"
"v_cvt_pk_bf16_f32 v145, v130, v131\n"
"v_cvt_pk_bf16_f32 v146, v132, v133\n"
"v_cvt_pk_bf16_f32 v147, v134, v135\n"
"s_add_u32 s48, s48, 4096\n"
"buffer_store_dwordx4 v[144:147], %[vL16], %[sWS], s48 offen offset:0 sc1\n"
"v_accvgpr_read_b32 v160, a116\n"
"v_accvgpr_read_b32 v161, a117\n"
"v_accvgpr_read_b32 v162, a118\n"
"v_accvgpr_read_b32 v163, a119\n"
"v_accvgpr_read_b32 v164, a140\n"
"v_accvgpr_read_b32 v165, a141\n"
"v_accvgpr_read_b32 v166, a142\n"
"v_accvgpr_read_b32 v167, a143\n"
"v_pk_mul_f32 v[168:169], v[160:161], v[160:161]\n"
"v_pk_mul_f32 v[170:171], v[162:163], v[162:163]\n"
"v_pk_mul_f32 v[172:173], v[164:165], v[164:165]\n"
"v_pk_mul_f32 v[174:175], v[166:167], v[166:167]\n"
"v_pk_fma_f32 v[168:169], v[168:169], s[46:47], v[40:41]\n"
"v_pk_fma_f32 v[170:171], v[170:171], s[46:47], v[40:41]\n"
"v_pk_fma_f32 v[172:173], v[172:173], s[46:47], v[40:41]\n"
"v_pk_fma_f32 v[174:175], v[174:175], s[46:47], v[40:41]\n"
"v_pk_mul_f32 v[168:169], v[168:169], v[160:161]\n"
"v_pk_mul_f32 v[170:171], v[170:171], v[162:163]\n"
"v_pk_mul_f32 v[172:173], v[172:173], v[164:165]\n"
"v_pk_mul_f32 v[174:175], v[174:175], v[166:167]\n"
"v_exp_f32 v168, v168\n"
"v_exp_f32 v169, v169\n"
"v_exp_f32 v170, v170\n"
"v_exp_f32 v171, v171\n"
"v_exp_f32 v172, v172\n"
"v_exp_f32 v173, v173\n"
"v_exp_f32 v174, v174\n"
"v_exp_f32 v175, v175\n"
"v_pk_add_f32 v[168:169], v[168:169], v[42:43]\n"
"v_pk_add_f32 v[170:171], v[170:171], v[42:43]\n"
"v_pk_add_f32 v[172:173], v[172:173], v[42:43]\n"
"v_pk_add_f32 v[174:175], v[174:175], v[42:43]\n"
"v_rcp_f32 v168, v168\n"
"v_rcp_f32 v169, v169\n"
"v_rcp_f32 v170, v170\n"
"v_rcp_f32 v171, v171\n"
"v_rcp_f32 v172, v172\n"
"v_rcp_f32 v173, v173\n"
"v_rcp_f32 v174, v174\n"
"v_rcp_f32 v175, v175\n"
"v_pk_mul_f32 v[160:161], v[160:161], v[168:169]\n"
"v_pk_mul_f32 v[162:163], v[162:163], v[170:171]\n"
"v_pk_mul_f32 v[164:165], v[164:165], v[172:173]\n"
"v_pk_mul_f32 v[166:167], v[166:167], v[174:175]\n"
"v_cvt_pk_bf16_f32 v176, v160, v161\n"
"v_cvt_pk_bf16_f32 v177, v162, v163\n"
"v_cvt_pk_bf16_f32 v178, v164, v165\n"
"v_cvt_pk_bf16_f32 v179, v166, v167\n"
"buffer_store_dwordx4 v[176:179], %[vL16], %[sWS], s48 offen offset:1024 sc1\n"
"s_waitcnt vmcnt(0)\n"
"s_barrier\n"
"v_mov_b32 v20, 1\n"
"s_mov_b32 s40, 1125376\n"
"buffer_store_dword v20, %[vFL], %[sWS], s40 offen sc1\n"
"s_branch LB_done\n"
"LB_tmo:\n"
"s_mov_b64 exec, 1\n"
"v_mov_b32 v20, 4\n"
"s_mov_b32 s40, 1065088\n"
"buffer_store_dword v20, off, %[sWS], s40 sc1\n"
"s_endpgm\n"
"LB_done:\n"
        :
        : [sWS] "s"(sWS), [sW1] "s"(sW1), [sB1] "s"(sB1), [sW3K] "s"(sW3K), [sHOFF] "s"(sHOFF), [sK0] "s"(rfl(dt * K1J + (tid & 0))), [sUOFF] "s"(sUOFF), [sT0] "s"(6 * wf),
          [vG0] "v"(vG[0]), [vG1] "v"(vG[1]), [vG2] "v"(vG[2]), [vT0] "v"(vT[0]), [vT1] "v"(vT[1]), [vT2] "v"(vT[2]), [vT3] "v"(vT[3]), [vT4] "v"(vT[4]), [vT5] "v"(vT[5]),
          [vBI] "v"(vBI), [vR] "v"(vR), [vL16] "v"(vL16), [vFL] "v"(vFL)
        : "v20", "v21", "v22", "v23", "v24", "v25", "v26", "v27", "v28", "v29", "v30", "v31", "v32", "v33", "v34", "v35", "v36", "v37", "v38", "v39", "v40", "v41", "v42", "v43",
"v44", "v45", "v46", "v47", "v48", "v49", "v50", "v51", "v52", "v53", "v54", "v55", "v56", "v57", "v58", "v59", "v60", "v61", "v62", "v63", "v64", "v65", "v66", "v67",
"v68", "v69", "v70", "v71", "v72", "v73", "v74", "v75", "v76", "v77", "v78", "v79", "v80", "v81", "v82", "v83", "v84", "v85", "v86", "v87", "v88", "v89", "v90", "v91",
"v92", "v93", "v94", "v95", "v96", "v97", "v98", "v99", "v100", "v101", "v102", "v103", "v104", "v105", "v106", "v107", "v108", "v109", "v110", "v111", "v112", "v113", "v114", "v115",
"v116", "v117", "v118", "v119", "v120", "v121", "v122", "v123", "v124", "v125", "v126", "v127", "v128", "v129", "v130", "v131", "v132", "v133", "v134", "v135", "v136", "v137", "v138", "v139",
"v140", "v141", "v142", "v143", "v144", "v145", "v146", "v147", "v148", "v149", "v150", "v151", "v152", "v153", "v154", "v155", "v156", "v157", "v158", "v159", "v160", "v161", "v162", "v163",
"v164", "v165", "v166", "v167", "v168", "v169", "v170", "v171", "v172", "v173", "v174", "v175", "v176", "v177", "v178", "v179", "v180", "v181", "v182", "v183", "v184", "v185", "v186", "v187",
"v188", "v189", "v190", "v191", "v192", "v193", "v194", "v195", "v196", "v197", "v198", "v199", "v200", "v201", "v202", "v203", "v204", "v205", "v206", "v207", "v208", "v209", "v210", "v211",
"v212", "v213", "v214", "v215", "v216", "v217", "v218", "v219", "v220", "v221", "v222", "v223", "v224", "v225", "v226", "v227", "v228", "v229", "v230", "v231", "v232", "v233", "v234", "v235",
"v236", "v237", "v238", "v239", "v240", "v241", "v242", "v243", "v244", "v245", "v246", "v247", "v248", "v249", "v250", "v251", "v252", "v253", "v254", "v255", "a0", "a1", "a2", "a3",
"a4", "a5", "a6", "a7", "a8", "a9", "a10", "a11", "a12", "a13", "a14", "a15", "a16", "a17", "a18", "a19", "a20", "a21", "a22", "a23", "a24", "a25", "a26", "a27",
"a28", "a29", "a30", "a31", "a32", "a33", "a34", "a35", "a36", "a37", "a38", "a39", "a40", "a41", "a42", "a43", "a44", "a45", "a46", "a47", "a48", "a49", "a50", "a51",
"a52", "a53", "a54", "a55", "a56", "a57", "a58", "a59", "a60", "a61", "a62", "a63", "a64", "a65", "a66", "a67", "a68", "a69", "a70", "a71", "a72", "a73", "a74", "a75",
"a76", "a77", "a78", "a79", "a80", "a81", "a82", "a83", "a84", "a85", "a86", "a87", "a88", "a89", "a90", "a91", "a92", "a93", "a94", "a95", "a96", "a97", "a98", "a99",
"a100", "a101", "a102", "a103", "a104", "a105", "a106", "a107", "a108", "a109", "a110", "a111", "a112", "a113", "a114", "a115", "a116", "a117", "a118", "a119", "a120", "a121", "a122", "a123",
"a124", "a125", "a126", "a127", "a128", "a129", "a130", "a131", "a132", "a133", "a134", "a135", "a136", "a137", "a138", "a139", "a140", "a141", "a142", "a143", "a144", "a145", "a146", "a147",
"a148", "a149", "a150", "a151", "a152", "a153", "a154", "a155", "a156", "a157", "a158", "a159", "a160", "a161", "a162", "a163", "a164", "a165", "a166", "a167", "a168", "a169", "a170", "a171",
"a172", "a173", "a174", "a175", "a176", "a177", "a178", "a179", "a180", "a181", "a182", "a183", "a184", "a185", "a186", "a187", "a188", "a189", "a190", "a191", "a192", "a193", "a194", "a195",
"a196", "a197", "a198", "a199", "a200", "a201", "a202", "a203", "a204", "a205", "a206", "a207", "a208", "a209", "a210", "a211", "a212", "a213", "a214", "a215", "a216", "a217", "a218", "a219",
"a220", "a221", "a222", "a223", "a224", "a225", "a226", "a227", "a228", "a229", "a230", "a231", "a232", "a233", "a234", "a235", "a236", "a237", "a238", "a239", "a240", "a241", "a242", "a243",
"a244", "a245", "a246", "a247", "a248", "a249", "a250", "a251", "a252", "a253", "a254", "a255", "s40", "s41", "s42", "s43", "s44", "s45", "s46", "s47", "s48", "s49", "s50", "s51",
"s52", "s53", "s54", "s55", "s56", "s57", "vcc", "scc", "memory");
}

__device__ __forceinline__ void role_b2(char* ws, const void* w2, int xg, int r) {
    int tid = threadIdx.x; asm volatile("" : "+v"(tid));
    const int w = rfl(tid >> 6), l = tid & 63, g = l >> 4, c = l & 15, x = c >> 2;
    const int st = 2 * xg + r / 9, dt = (r / 3) % 3, sk = r % 3, wd = w & 1, wsx = w >> 1;
    const v4i sWS = make_srd(ws, WS_B), sW2 = make_srd(w2, 4608 * 4096);
    const int row = 2 * w + (l >> 5), pos = l & 31, ft = FSPLIT ? min(l, 23) : 8 * sk + (l & 7);
    const unsigned vG = (1536 * sk + row) * 4096 + st * 512 + ((pos >> 1) ^ row) * 32 + (pos & 1) * 16;
    const unsigned vT = (4 * g + x) * 512 + ((8 * wsx) ^ ((4 * g + x) & 7)) * 32 + (c & 3) * 8;
    const unsigned vR = 16384 + 6144 * wd + l * 16, vPF = ((ft / 6) * 18 + (ft % 6) * 3 + dt) * 128;
    const unsigned vL16 = l * 16, vFL = tid == 0 ? (unsigned)((xg * 18 + r) * 128) : OOB;
    const int sW3K = w * 3072, sW1K = w * 1024, sU2OFF = rfl(WS_UF + 48 * sk * 98304 + (2 * dt + (w >> 1)) * 16384 + (w & 1) * 3072);
    const int sYOFF = rfl(WS_YB + sk * 6291456 + (16 * st + 8 * wsx) * 49152 + dt * 16384 + wd * 3072);
    asm volatile(
"s_nop 4\n"
"v_mov_b32 v48, %[vPF]\n"
"v_mov_b32 v58, %[vR]\n"
"v_mov_b32 v59, %[vG]\n"
"v_mov_b32 v60, %[vT]\n"
"s_mov_b32 s40, 1125376\n"
"s_mov_b64 exec, -1\n"
"s_mov_b32 s41, 0\n"
"LC_b1:\n"
"buffer_load_dword v20, v48, %[sWS], s40 offen sc1\n"
"s_add_u32 s41, s41, 1\n"
"s_cmp_gt_u32 s41, 60000\n"
"s_cbranch_scc1 LC_tmo\n"
"s_waitcnt vmcnt(0)\n"
"v_cmp_gt_i32 vcc, 0, v20\n"
"s_cbranch_vccnz LC_b1\n"
"LC_b1_ok:\n"
"s_mov_b64 exec, -1\n"
"s_barrier\n"
"v_xor_b32 v61, 32, v60\n"
"v_xor_b32 v62, 64, v60\n"
"v_xor_b32 v63, 96, v60\n"
"v_xor_b32 v64, 128, v60\n"
"v_xor_b32 v65, 160, v60\n"
"v_xor_b32 v66, 192, v60\n"
"v_xor_b32 v67, 224, v60\n"
"s_mov_b32 s49, %[sK0]\n"
"s_mov_b32 s50, 3\n"
"s_mov_b32 s42, 0\n"
"s_min_u32 s40, s49, 47\n"
"s_mul_i32 s54, s40, 131072\n"
"s_mul_i32 s52, s40, 98304\n"
"s_add_u32 s52, s52, %[sU2OFF]\n"
"s_mov_b32 s53, 0\n"
"s_mov_b32 s44, 0\n"
"s_mov_b32 s55, 28672\n"
"s_add_u32 s40, s53, %[sW1K]\n"
"s_add_u32 m0, s40, 0\n"
"s_add_u32 s40, s54, 0\n"
"buffer_load_dwordx4 v59, %[sW2], s40 offen nt lds\n"
"s_add_u32 s40, s53, %[sW1K]\n"
"s_add_u32 m0, s40, 4096\n"
"s_add_u32 s40, s54, 32768\n"
"buffer_load_dwordx4 v59, %[sW2], s40 offen nt lds\n"
"s_add_u32 s40, s53, %[sW1K]\n"
"s_add_u32 m0, s40, 8192\n"
"s_add_u32 s40, s54, 65536\n"
"buffer_load_dwordx4 v59, %[sW2], s40 offen nt lds\n"
"s_add_u32 s40, s53, %[sW1K]\n"
"s_add_u32 m0, s40, 12288\n"
"s_add_u32 s40, s54, 98304\n"
"buffer_load_dwordx4 v59, %[sW2], s40 offen nt lds\n"
"s_add_u32 s40, s53, %[sW3K]\n"
"s_add_u32 m0, s40, 16384\n"
"s_lshr_b32 s51, s52, 21\n"
"s_lshr_b32 s51, %[sFAR], s51\n"
"s_and_b32 s51, s51, 1\n"
"s_lshl_b32 s51, s51, 13\n"
"s_add_u32 s51, s51, s52\n"
"buffer_load_dwordx4 %[vL16], %[sWS], s51 offen offset:0 sc1 lds\n"
"buffer_load_dwordx4 %[vL16], %[sWS], s51 offen offset:1024 sc1 lds\n"
"buffer_load_dwordx4 %[vL16], %[sWS], s51 offen offset:2048 sc1 lds\n"
"s_add_u32 s53, s53, 28672\n"
"s_cmp_ge_u32 s53, 114688\n"
"s_cselect_b32 s53, 0, s53\n"
"s_add_u32 s49, s49, 1\n"
"s_cmp_eq_u32 s49, s50\n"
"s_cselect_b32 s40, 3, 0\n"
"s_sub_u32 s49, s49, s40\n"
"s_add_u32 s42, s42, 1\n"
"s_cmp_eq_u32 s42, 3\n"
"s_cselect_b32 s40, 3, 0\n"
"s_sub_u32 s42, s42, s40\n"
"s_add_u32 s49, s49, s40\n"
"s_add_u32 s50, s50, s40\n"
"s_min_u32 s40, s49, 47\n"
"s_mul_i32 s54, s40, 131072\n"
"s_mul_i32 s52, s40, 98304\n"
"s_add_u32 s52, s52, %[sU2OFF]\n"
"s_add_u32 s40, s53, %[sW1K]\n"
"s_add_u32 m0, s40, 0\n"
"s_add_u32 s40, s54, 0\n"
"buffer_load_dwordx4 v59, %[sW2], s40 offen nt lds\n"
"s_add_u32 s40, s53, %[sW1K]\n"
"s_add_u32 m0, s40, 4096\n"
"s_add_u32 s40, s54, 32768\n"
"buffer_load_dwordx4 v59, %[sW2], s40 offen nt lds\n"
"s_add_u32 s40, s53, %[sW1K]\n"
"s_add_u32 m0, s40, 8192\n"
"s_add_u32 s40, s54, 65536\n"
"buffer_load_dwordx4 v59, %[sW2], s40 offen nt lds\n"
"s_add_u32 s40, s53, %[sW1K]\n"
"s_add_u32 m0, s40, 12288\n"
"s_add_u32 s40, s54, 98304\n"
"buffer_load_dwordx4 v59, %[sW2], s40 offen nt lds\n"
"s_add_u32 s40, s53, %[sW3K]\n"
"s_add_u32 m0, s40, 16384\n"
"s_lshr_b32 s51, s52, 21\n"
"s_lshr_b32 s51, %[sFAR], s51\n"
"s_and_b32 s51, s51, 1\n"
"s_lshl_b32 s51, s51, 13\n"
"s_add_u32 s51, s51, s52\n"
"buffer_load_dwordx4 %[vL16], %[sWS], s51 offen offset:0 sc1 lds\n"
"buffer_load_dwordx4 %[vL16], %[sWS], s51 offen offset:1024 sc1 lds\n"
"buffer_load_dwordx4 %[vL16], %[sWS], s51 offen offset:2048 sc1 lds\n"
"s_add_u32 s53, s53, 28672\n"
"s_cmp_ge_u32 s53, 114688\n"
"s_cselect_b32 s53, 0, s53\n"
"s_add_u32 s49, s49, 1\n"
"s_cmp_eq_u32 s49, s50\n"
"s_cselect_b32 s40, 3, 0\n"
"s_sub_u32 s49, s49, s40\n"
"s_add_u32 s42, s42, 1\n"
"s_cmp_eq_u32 s42, 3\n"
"s_cselect_b32 s40, 3, 0\n"
"s_sub_u32 s42, s42, s40\n"
"s_add_u32 s49, s49, s40\n"
"s_add_u32 s50, s50, s40\n"
"s_min_u32 s40, s49, 47\n"
"s_mul_i32 s54, s40, 131072\n"
"s_mul_i32 s52, s40, 98304\n"
"s_add_u32 s52, s52, %[sU2OFF]\n"
"s_add_u32 s40, s53, %[sW1K]\n"
"s_add_u32 m0, s40, 0\n"
"s_add_u32 s40, s54, 0\n"
"buffer_load_dwordx4 v59, %[sW2], s40 offen nt lds\n"
"s_add_u32 s40, s53, %[sW1K]\n"
"s_add_u32 m0, s40, 4096\n"
"s_add_u32 s40, s54, 32768\n"
"buffer_load_dwordx4 v59, %[sW2], s40 offen nt lds\n"
"s_add_u32 s40, s53, %[sW1K]\n"
"s_add_u32 m0, s40, 8192\n"
"s_add_u32 s40, s54, 65536\n"
"buffer_load_dwordx4 v59, %[sW2], s40 offen nt lds\n"
"s_add_u32 s40, s53, %[sW1K]\n"
"s_add_u32 m0, s40, 12288\n"
"s_add_u32 s40, s54, 98304\n"
"buffer_load_dwordx4 v59, %[sW2], s40 offen nt lds\n"
"s_add_u32 s40, s53, %[sW3K]\n"
"s_add_u32 m0, s40, 16384\n"
"s_lshr_b32 s51, s52, 21\n"
"s_lshr_b32 s51, %[sFAR], s51\n"
"s_and_b32 s51, s51, 1\n"
"s_lshl_b32 s51, s51, 13\n"
"s_add_u32 s51, s51, s52\n"
"buffer_load_dwordx4 %[vL16], %[sWS], s51 offen offset:0 sc1 lds\n"
"buffer_load_dwordx4 %[vL16], %[sWS], s51 offen offset:1024 sc1 lds\n"
"buffer_load_dwordx4 %[vL16], %[sWS], s51 offen offset:2048 sc1 lds\n"
"s_add_u32 s53, s53, 28672\n"
"s_cmp_ge_u32 s53, 114688\n"
"s_cselect_b32 s53, 0, s53\n"
"s_add_u32 s49, s49, 1\n"
"s_cmp_eq_u32 s49, s50\n"
"s_cselect_b32 s40, 3, 0\n"
"s_sub_u32 s49, s49, s40\n"
"s_add_u32 s42, s42, 1\n"
"s_cmp_eq_u32 s42, 3\n"
"s_cselect_b32 s40, 3, 0\n"
"s_sub_u32 s42, s42, s40\n"
"s_add_u32 s49, s49, s40\n"
"s_add_u32 s50, s50, s40\n"
"s_min_u32 s40, s49, 47\n"
"s_mul_i32 s54, s40, 131072\n"
"s_mul_i32 s52, s40, 98304\n"
"s_add_u32 s52, s52, %[sU2OFF]\n"
"s_add_u32 s40, s53, %[sW1K]\n"
"s_add_u32 m0, s40, 0\n"
"s_add_u32 s40, s54, 0\n"
"buffer_load_dwordx4 v59, %[sW2], s40 offen nt lds\n"
"s_add_u32 s40, s53, %[sW1K]\n"
"s_add_u32 m0, s40, 4096\n"
"s_add_u32 s40, s54, 32768\n"
"buffer_load_dwordx4 v59, %[sW2], s40 offen nt lds\n"
"s_add_u32 s40, s53, %[sW1K]\n"
"s_add_u32 m0, s40, 8192\n"
"s_add_u32 s40, s54, 65536\n"
"buffer_load_dwordx4 v59, %[sW2], s40 offen nt lds\n"
"s_add_u32 s40, s53, %[sW1K]\n"
"s_add_u32 m0, s40, 12288\n"
"s_add_u32 s40, s54, 98304\n"
"buffer_load_dwordx4 v59, %[sW2], s40 offen nt lds\n"
"s_add_u32 s40, s53, %[sW3K]\n"
"s_add_u32 m0, s40, 16384\n"
"s_lshr_b32 s51, s52, 21\n"
"s_lshr_b32 s51, %[sFAR], s51\n"
"s_and_b32 s51, s51, 1\n"
"s_lshl_b32 s51, s51, 13\n"
"s_add_u32 s51, s51, s52\n"
"buffer_load_dwordx4 %[vL16], %[sWS], s51 offen offset:0 sc1 lds\n"
"buffer_load_dwordx4 %[vL16], %[sWS], s51 offen offset:1024 sc1 lds\n"
"buffer_load_dwordx4 %[vL16], %[sWS], s51 offen offset:2048 sc1 lds\n"
"s_add_u32 s53, s53, 28672\n"
"s_cmp_ge_u32 s53, 114688\n"
"s_cselect_b32 s53, 0, s53\n"
"s_add_u32 s49, s49, 1\n"
"s_cmp_eq_u32 s49, s50\n"
"s_cselect_b32 s40, 3, 0\n"
"s_sub_u32 s49, s49, s40\n"
"s_add_u32 s42, s42, 1\n"
"s_cmp_eq_u32 s42, 3\n"
"s_cselect_b32 s40, 3, 0\n"
"s_sub_u32 s42, s42, s40\n"
"s_add_u32 s49, s49, s40\n"
"s_add_u32 s50, s50, s40\n"
"s_min_u32 s40, s49, 47\n"
"s_mul_i32 s54, s40, 131072\n"
"s_mul_i32 s52, s40, 98304\n"
"s_add_u32 s52, s52, %[sU2OFF]\n"
"v_accvgpr_write_b32 a0, 0\n"
"v_accvgpr_write_b32 a1, 0\n"
"v_accvgpr_write_b32 a2, 0\n"
"v_accvgpr_write_b32 a3, 0\n"
"v_accvgpr_write_b32 a4, 0\n"
"v_accvgpr_write_b32 a5, 0\n"
"v_accvgpr_write_b32 a6, 0\n"
"v_accvgpr_write_b32 a7, 0\n"
"v_accvgpr_write_b32 a8, 0\n"
"v_accvgpr_write_b32 a9, 0\n"
"v_accvgpr_write_b32 a10, 0\n"
"v_accvgpr_write_b32 a11, 0\n"
"v_accvgpr_write_b32 a12, 0\n"
"v_accvgpr_write_b32 a13, 0\n"
"v_accvgpr_write_b32 a14, 0\n"
"v_accvgpr_write_b32 a15, 0\n"
"v_accvgpr_write_b32 a16, 0\n"
"v_accvgpr_write_b32 a17, 0\n"
"v_accvgpr_write_b32 a18, 0\n"
"v_accvgpr_write_b32 a19, 0\n"
"v_accvgpr_write_b32 a20, 0\n"
"v_accvgpr_write_b32 a21, 0\n"
"v_accvgpr_write_b32 a22, 0\n"
"v_accvgpr_write_b32 a23, 0\n"
"v_accvgpr_write_b32 a24, 0\n"
"v_accvgpr_write_b32 a25, 0\n"
"v_accvgpr_write_b32 a26, 0\n"
"v_accvgpr_write_b32 a27, 0\n"
"v_accvgpr_write_b32 a28, 0\n"
"v_accvgpr_write_b32 a29, 0\n"
"v_accvgpr_write_b32 a30, 0\n"
"v_accvgpr_write_b32 a31, 0\n"
"v_accvgpr_write_b32 a32, 0\n"
"v_accvgpr_write_b32 a33, 0\n"
"v_accvgpr_write_b32 a34, 0\n"
"v_accvgpr_write_b32 a35, 0\n"
"v_accvgpr_write_b32 a36, 0\n"
"v_accvgpr_write_b32 a37, 0\n"
"v_accvgpr_write_b32 a38, 0\n"
"v_accvgpr_write_b32 a39, 0\n"
"v_accvgpr_write_b32 a40, 0\n"
"v_accvgpr_write_b32 a41, 0\n"
"v_accvgpr_write_b32 a42, 0\n"
"v_accvgpr_write_b32 a43, 0\n"
"v_accvgpr_write_b32 a44, 0\n"
"v_accvgpr_write_b32 a45, 0\n"
"v_accvgpr_write_b32 a46, 0\n"
"v_accvgpr_write_b32 a47, 0\n"
"v_accvgpr_write_b32 a48, 0\n"
"v_accvgpr_write_b32 a49, 0\n"
"v_accvgpr_write_b32 a50, 0\n"
"v_accvgpr_write_b32 a51, 0\n"
"v_accvgpr_write_b32 a52, 0\n"
"v_accvgpr_write_b32 a53, 0\n"
"v_accvgpr_write_b32 a54, 0\n"
"v_accvgpr_write_b32 a55, 0\n"
"v_accvgpr_write_b32 a56, 0\n"
"v_accvgpr_write_b32 a57, 0\n"
"v_accvgpr_write_b32 a58, 0\n"
"v_accvgpr_write_b32 a59, 0\n"
"v_accvgpr_write_b32 a60, 0\n"
"v_accvgpr_write_b32 a61, 0\n"
"v_accvgpr_write_b32 a62, 0\n"
"v_accvgpr_write_b32 a63, 0\n"
"v_accvgpr_write_b32 a64, 0\n"
"v_accvgpr_write_b32 a65, 0\n"
"v_accvgpr_write_b32 a66, 0\n"
"v_accvgpr_write_b32 a67, 0\n"
"v_accvgpr_write_b32 a68, 0\n"
"v_accvgpr_write_b32 a69, 0\n"
"v_accvgpr_write_b32 a70, 0\n"
"v_accvgpr_write_b32 a71, 0\n"
"v_accvgpr_write_b32 a72, 0\n"
"v_accvgpr_write_b32 a73, 0\n"
"v_accvgpr_write_b32 a74, 0\n"
"v_accvgpr_write_b32 a75, 0\n"
"v_accvgpr_write_b32 a76, 0\n"
"v_accvgpr_write_b32 a77, 0\n"
"v_accvgpr_write_b32 a78, 0\n"
"v_accvgpr_write_b32 a79, 0\n"
"v_accvgpr_write_b32 a80, 0\n"
"v_accvgpr_write_b32 a81, 0\n"
"v_accvgpr_write_b32 a82, 0\n"
"v_accvgpr_write_b32 a83, 0\n"
"v_accvgpr_write_b32 a84, 0\n"
"v_accvgpr_write_b32 a85, 0\n"
"v_accvgpr_write_b32 a86, 0\n"
"v_accvgpr_write_b32 a87, 0\n"
"v_accvgpr_write_b32 a88, 0\n"
"v_accvgpr_write_b32 a89, 0\n"
"v_accvgpr_write_b32 a90, 0\n"
"v_accvgpr_write_b32 a91, 0\n"
"v_accvgpr_write_b32 a92, 0\n"
"v_accvgpr_write_b32 a93, 0\n"
"v_accvgpr_write_b32 a94, 0\n"
"v_accvgpr_write_b32 a95, 0\n"
"v_accvgpr_write_b32 a96, 0\n"
"v_accvgpr_write_b32 a97, 0\n"
"v_accvgpr_write_b32 a98, 0\n"
"v_accvgpr_write_b32 a99, 0\n"
"v_accvgpr_write_b32 a100, 0\n"
"v_accvgpr_write_b32 a101, 0\n"
"v_accvgpr_write_b32 a102, 0\n"
"v_accvgpr_write_b32 a103, 0\n"
"v_accvgpr_write_b32 a104, 0\n"
"v_accvgpr_write_b32 a105, 0\n"
"v_accvgpr_write_b32 a106, 0\n"
"v_accvgpr_write_b32 a107, 0\n"
"v_accvgpr_write_b32 a108, 0\n"
"v_accvgpr_write_b32 a109, 0\n"
"v_accvgpr_write_b32 a110, 0\n"
"v_accvgpr_write_b32 a111, 0\n"
"v_accvgpr_write_b32 a112, 0\n"
"v_accvgpr_write_b32 a113, 0\n"
"v_accvgpr_write_b32 a114, 0\n"
"v_accvgpr_write_b32 a115, 0\n"
"v_accvgpr_write_b32 a116, 0\n"
"v_accvgpr_write_b32 a117, 0\n"
"v_accvgpr_write_b32 a118, 0\n"
"v_accvgpr_write_b32 a119, 0\n"
"v_accvgpr_write_b32 a120, 0\n"
"v_accvgpr_write_b32 a121, 0\n"
"v_accvgpr_write_b32 a122, 0\n"
"v_accvgpr_write_b32 a123, 0\n"
"v_accvgpr_write_b32 a124, 0\n"
"v_accvgpr_write_b32 a125, 0\n"
"v_accvgpr_write_b32 a126, 0\n"
"v_accvgpr_write_b32 a127, 0\n"
"v_accvgpr_write_b32 a128, 0\n"
"v_accvgpr_write_b32 a129, 0\n"
"v_accvgpr_write_b32 a130, 0\n"
"v_accvgpr_write_b32 a131, 0\n"
"v_accvgpr_write_b32 a132, 0\n"
"v_accvgpr_write_b32 a133, 0\n"
"v_accvgpr_write_b32 a134, 0\n"
"v_accvgpr_write_b32 a135, 0\n"
"v_accvgpr_write_b32 a136, 0\n"
"v_accvgpr_write_b32 a137, 0\n"
"v_accvgpr_write_b32 a138, 0\n"
"v_accvgpr_write_b32 a139, 0\n"
"v_accvgpr_write_b32 a140, 0\n"
"v_accvgpr_write_b32 a141, 0\n"
"v_accvgpr_write_b32 a142, 0\n"
"v_accvgpr_write_b32 a143, 0\n"
"v_accvgpr_write_b32 a144, 0\n"
"v_accvgpr_write_b32 a145, 0\n"
"v_accvgpr_write_b32 a146, 0\n"
"v_accvgpr_write_b32 a147, 0\n"
"v_accvgpr_write_b32 a148, 0\n"
"v_accvgpr_write_b32 a149, 0\n"
"v_accvgpr_write_b32 a150, 0\n"
"v_accvgpr_write_b32 a151, 0\n"
"v_accvgpr_write_b32 a152, 0\n"
"v_accvgpr_write_b32 a153, 0\n"
"v_accvgpr_write_b32 a154, 0\n"
"v_accvgpr_write_b32 a155, 0\n"
"v_accvgpr_write_b32 a156, 0\n"
"v_accvgpr_write_b32 a157, 0\n"
"v_accvgpr_write_b32 a158, 0\n"
"v_accvgpr_write_b32 a159, 0\n"
"v_accvgpr_write_b32 a160, 0\n"
"v_accvgpr_write_b32 a161, 0\n"
"v_accvgpr_write_b32 a162, 0\n"
"v_accvgpr_write_b32 a163, 0\n"
"v_accvgpr_write_b32 a164, 0\n"
"v_accvgpr_write_b32 a165, 0\n"
"v_accvgpr_write_b32 a166, 0\n"
"v_accvgpr_write_b32 a167, 0\n"
"v_accvgpr_write_b32 a168, 0\n"
"v_accvgpr_write_b32 a169, 0\n"
"v_accvgpr_write_b32 a170, 0\n"
"v_accvgpr_write_b32 a171, 0\n"
"v_accvgpr_write_b32 a172, 0\n"
"v_accvgpr_write_b32 a173, 0\n"
"v_accvgpr_write_b32 a174, 0\n"
"v_accvgpr_write_b32 a175, 0\n"
"v_accvgpr_write_b32 a176, 0\n"
"v_accvgpr_write_b32 a177, 0\n"
"v_accvgpr_write_b32 a178, 0\n"
"v_accvgpr_write_b32 a179, 0\n"
"v_accvgpr_write_b32 a180, 0\n"
"v_accvgpr_write_b32 a181, 0\n"
"v_accvgpr_write_b32 a182, 0\n"
"v_accvgpr_write_b32 a183, 0\n"
"v_accvgpr_write_b32 a184, 0\n"
"v_accvgpr_write_b32 a185, 0\n"
"v_accvgpr_write_b32 a186, 0\n"
"v_accvgpr_write_b32 a187, 0\n"
"v_accvgpr_write_b32 a188, 0\n"
"v_accvgpr_write_b32 a189, 0\n"
"v_accvgpr_write_b32 a190, 0\n"
"v_accvgpr_write_b32 a191, 0\n"
"s_waitcnt vmcnt(21)\n"
"s_barrier\n"
"ds_read_b128 v[100:103], v58 offset:0\n"
"ds_read_b128 v[104:107], v58 offset:1024\n"
"ds_read_b128 v[108:111], v58 offset:2048\n"
"ds_read_b128 v[112:115], v58 offset:3072\n"
"ds_read_b128 v[116:119], v58 offset:4096\n"
"ds_read_b128 v[120:123], v58 offset:5120\n"
"ds_read_b64_tr_b16 v[124:125], v60 offset:0\n"
"ds_read_b64_tr_b16 v[126:127], v60 offset:8192\n"
"ds_read_b64_tr_b16 v[128:129], v61 offset:0\n"
"ds_read_b64_tr_b16 v[130:131], v61 offset:8192\n"
"ds_read_b64_tr_b16 v[132:133], v62 offset:0\n"
"ds_read_b64_tr_b16 v[134:135], v62 offset:8192\n"
"ds_read_b64_tr_b16 v[136:137], v63 offset:0\n"
"ds_read_b64_tr_b16 v[138:139], v63 offset:8192\n"
"ds_read_b64_tr_b16 v[140:141], v64 offset:0\n"
"ds_read_b64_tr_b16 v[142:143], v64 offset:8192\n"
"ds_read_b64_tr_b16 v[144:145], v65 offset:0\n"
"ds_read_b64_tr_b16 v[146:147], v65 offset:8192\n"
"ds_read_b64_tr_b16 v[148:149], v66 offset:0\n"
"ds_read_b64_tr_b16 v[150:151], v66 offset:8192\n"
"ds_read_b64_tr_b16 v[152:153], v67 offset:0\n"
"ds_read_b64_tr_b16 v[154:155], v67 offset:8192\n"
"s_add_u32 s44, s44, 28672\n"
"s_cmp_ge_u32 s44, 114688\n"
"s_cselect_b32 s45, 4294881280, s55\n"
"s_cselect_b32 s44, 0, s44\n"
"v_add_u32 v58, s45, v58\n"
"v_add_u32 v60, s45, v60\n"
"v_add_u32 v61, s45, v61\n"
"v_add_u32 v62, s45, v62\n"
"v_add_u32 v63, s45, v63\n"
"v_add_u32 v64, s45, v64\n"
"v_add_u32 v65, s45, v65\n"
"v_add_u32 v66, s45, v66\n"
"v_add_u32 v67, s45, v67\n"
"s_mov_b32 s43, 24\n"
"LC_g2:\n"
"s_waitcnt vmcnt(14) lgkmcnt(0)\n"
"s_barrier\n"
"v_mfma_f32_16x16x32_bf16 a[0:3], v[100:103], v[124:127], a[0:3]\n"
"ds_read_b64_tr_b16 v[180:181], v60 offset:0\n"
"v_mfma_f32_16x16x32_bf16 a[4:7], v[100:103], v[128:131], a[4:7]\n"
"ds_read_b64_tr_b16 v[182:183], v60 offset:8192\n"
"v_mfma_f32_16x16x32_bf16 a[8:11], v[100:103], v[132:135], a[8:11]\n"
"s_add_u32 s40, s53, %[sW1K]\n"
"s_add_u32 m0, s40, 0\n"
"s_add_u32 s40, s54, 0\n"
"buffer_load_dwordx4 v59, %[sW2], s40 offen nt lds\n"
"v_mfma_f32_16x16x32_bf16 a[12:15], v[100:103], v[136:139], a[12:15]\n"
"ds_read_b64_tr_b16 v[184:185], v61 offset:0\n"
"v_mfma_f32_16x16x32_bf16 a[16:19], v[100:103], v[140:143], a[16:19]\n"
"ds_read_b64_tr_b16 v[186:187], v61 offset:8192\n"
"v_mfma_f32_16x16x32_bf16 a[20:23], v[100:103], v[144:147], a[20:23]\n"
"s_add_u32 s40, s53, %[sW1K]\n"
"s_add_u32 m0, s40, 4096\n"
"s_add_u32 s40, s54, 32768\n"
"buffer_load_dwordx4 v59, %[sW2], s40 offen nt lds\n"
"v_mfma_f32_16x16x32_bf16 a[24:27], v[100:103], v[148:151], a[24:27]\n"
"ds_read_b64_tr_b16 v[188:189], v62 offset:0\n"
"v_mfma_f32_16x16x32_bf16 a[28:31], v[100:103], v[152:155], a[28:31]\n"
"v_mfma_f32_16x16x32_bf16 a[32:35], v[104:107], v[124:127], a[32:35]\n"
"ds_read_b64_tr_b16 v[190:191], v62 offset:8192\n"
"v_mfma_f32_16x16x32_bf16 a[36:39], v[104:107], v[128:131], a[36:39]\n"
"s_add_u32 s40, s53, %[sW1K]\n"
"s_add_u32 m0, s40, 8192\n"
"s_add_u32 s40, s54, 65536\n"
"buffer_load_dwordx4 v59, %[sW2], s40 offen nt lds\n"
"v_mfma_f32_16x16x32_bf16 a[40:43], v[104:107], v[132:135], a[40:43]\n"
"ds_read_b64_tr_b16 v[192:193], v63 offset:0\n"
"v_mfma_f32_16x16x32_bf16 a[44:47], v[104:107], v[136:139], a[44:47]\n"
"ds_read_b64_tr_b16 v[194:195], v63 offset:8192\n"
"v_mfma_f32_16x16x32_bf16 a[48:51], v[104:107], v[140:143], a[48:51]\n"
"s_add_u32 s40, s53, %[sW1K]\n"
"s_add_u32 m0, s40, 12288\n"
"s_add_u32 s40, s54, 98304\n"
"buffer_load_dwordx4 v59, %[sW2], s40 offen nt lds\n"
"v_mfma_f32_16x16x32_bf16 a[52:55], v[104:107], v[144:147], a[52:55]\n"
"ds_read_b64_tr_b16 v[196:197], v64 offset:0\n"
"v_mfma_f32_16x16x32_bf16 a[56:59], v[104:107], v[148:151], a[56:59]\n"
"ds_read_b64_tr_b16 v[198:199], v64 offset:8192\n"
"v_mfma_f32_16x16x32_bf16 a[60:63], v[104:107], v[152:155], a[60:63]\n"
"v_mfma_f32_16x16x32_bf16 a[64:67], v[108:111], v[124:127], a[64:67]\n"
"s_add_u32 s40, s53, %[sW3K]\n"
"s_add_u32 m0, s40, 16384\n"
"s_lshr_b32 s51, s52, 21\n"
"s_lshr_b32 s51, %[sFAR], s51\n"
"s_and_b32 s51, s51, 1\n"
"s_lshl_b32 s51, s51, 13\n"
"s_add_u32 s51, s51, s52\n"
"buffer_load_dwordx4 %[vL16], %[sWS], s51 offen offset:0 sc1 lds\n"
"v_mfma_f32_16x16x32_bf16 a[68:71], v[108:111], v[128:131], a[68:71]\n"
"ds_read_b64_tr_b16 v[200:201], v65 offset:0\n"
"v_mfma_f32_16x16x32_bf16 a[72:75], v[108:111], v[132:135], a[72:75]\n"
"ds_read_b64_tr_b16 v[202:203], v65 offset:8192\n"
"v_mfma_f32_16x16x32_bf16 a[76:79], v[108:111], v[136:139], a[76:79]\n"
"buffer_load_dwordx4 %[vL16], %[sWS], s51 offen offset:1024 sc1 lds\n"
"v_mfma_f32_16x16x32_bf16 a[80:83], v[108:111], v[140:143], a[80:83]\n"
"ds_read_b64_tr_b16 v[204:205], v66 offset:0\n"
"v_mfma_f32_16x16x32_bf16 a[84:87], v[108:111], v[144:147], a[84:87]\n"
"ds_read_b64_tr_b16 v[206:207], v66 offset:8192\n"
"v_mfma_f32_16x16x32_bf16 a[88:91], v[108:111], v[148:151], a[88:91]\n"
"v_mfma_f32_16x16x32_bf16 a[92:95], v[108:111], v[152:155], a[92:95]\n"
"buffer_load_dwordx4 %[vL16], %[sWS], s51 offen offset:2048 sc1 lds\n"
"v_mfma_f32_16x16x32_bf16 a[96:99], v[112:115], v[124:127], a[96:99]\n"
"ds_read_b64_tr_b16 v[208:209], v67 offset:0\n"
"v_mfma_f32_16x16x32_bf16 a[100:103], v[112:115], v[128:131], a[100:103]\n"
"ds_read_b64_tr_b16 v[210:211], v67 offset:8192\n"
"v_mfma_f32_16x16x32_bf16 a[104:107], v[112:115], v[132:135], a[104:107]\n"
"s_add_u32 s53, s53, 28672\n"
"s_cmp_ge_u32 s53, 114688\n"
"s_cselect_b32 s53, 0, s53\n"
"v_mfma_f32_16x16x32_bf16 a[108:111], v[112:115], v[136:139], a[108:111]\n"
"ds_read_b128 v[156:159], v58 offset:0\n"
"v_mfma_f32_16x16x32_bf16 a[112:115], v[112:115], v[140:143], a[112:115]\n"
"ds_read_b128 v[160:163], v58 offset:1024\n"
"v_mfma_f32_16x16x32_bf16 a[116:119], v[112:115], v[144:147], a[116:119]\n"
"ds_read_b128 v[164:167], v58 offset:2048\n"
"v_mfma_f32_16x16x32_bf16 a[120:123], v[112:115], v[148:151], a[120:123]\n"
"v_mfma_f32_16x16x32_bf16 a[124:127], v[112:115], v[152:155], a[124:127]\n"
"ds_read_b128 v[168:171], v58 offset:3072\n"
"v_mfma_f32_16x16x32_bf16 a[128:131], v[116:119], v[124:127], a[128:131]\n"
"ds_read_b128 v[172:175], v58 offset:4096\n"
"v_mfma_f32_16x16x32_bf16 a[132:135], v[116:119], v[128:131], a[132:135]\n"
"ds_read_b128 v[176:179], v58 offset:5120\n"
"v_mfma_f32_16x16x32_bf16 a[136:139], v[116:119], v[132:135], a[136:139]\n"
"s_add_u32 s44, s44, 28672\n"
"s_cmp_ge_u32 s44, 114688\n"
"s_cselect_b32 s45, 4294881280, s55\n"
"s_cselect_b32 s44, 0, s44\n"
"v_mfma_f32_16x16x32_bf16 a[140:143], v[116:119], v[136:139], a[140:143]\n"
"v_add_u32 v58, s45, v58\n"
"s_add_u32 s49, s49, 1\n"
"s_cmp_eq_u32 s49, s50\n"
"s_cselect_b32 s40, 3, 0\n"
"v_mfma_f32_16x16x32_bf16 a[144:147], v[116:119], v[140:143], a[144:147]\n"
"v_add_u32 v60, s45, v60\n"
"s_sub_u32 s49, s49, s40\n"
"s_add_u32 s42, s42, 1\n"
"v_mfma_f32_16x16x32_bf16 a[148:151], v[116:119], v[144:147], a[148:151]\n"
"v_add_u32 v61, s45, v61\n"
"s_cmp_eq_u32 s42, 3\n"
"s_cselect_b32 s40, 3, 0\n"
"s_sub_u32 s42, s42, s40\n"
"v_mfma_f32_16x16x32_bf16 a[152:155], v[116:119], v[148:151], a[152:155]\n"
"v_mfma_f32_16x16x32_bf16 a[156:159], v[116:119], v[152:155], a[156:159]\n"
"v_add_u32 v62, s45, v62\n"
"s_add_u32 s49, s49, s40\n"
"s_add_u32 s50, s50, s40\n"
"v_mfma_f32_16x16x32_bf16 a[160:163], v[120:123], v[124:127], a[160:163]\n"
"v_add_u32 v63, s45, v63\n"
"s_min_u32 s40, s49, 47\n"
"s_mul_i32 s54, s40, 131072\n"
"s_mul_i32 s52, s40, 98304\n"
"s_add_u32 s52, s52, %[sU2OFF]\n"
"v_mfma_f32_16x16x32_bf16 a[164:167], v[120:123], v[128:131], a[164:167]\n"
"v_add_u32 v64, s45, v64\n"
"v_mfma_f32_16x16x32_bf16 a[168:171], v[120:123], v[132:135], a[168:171]\n"
"v_add_u32 v65, s45, v65\n"
"v_mfma_f32_16x16x32_bf16 a[172:175], v[120:123], v[136:139], a[172:175]\n"
"v_add_u32 v66, s45, v66\n"
"v_mfma_f32_16x16x32_bf16 a[176:179], v[120:123], v[140:143], a[176:179]\n"
"v_add_u32 v67, s45, v67\n"
"v_mfma_f32_16x16x32_bf16 a[180:183], v[120:123], v[144:147], a[180:183]\n"
"v_mfma_f32_16x16x32_bf16 a[184:187], v[120:123], v[148:151], a[184:187]\n"
"v_mfma_f32_16x16x32_bf16 a[188:191], v[120:123], v[152:155], a[188:191]\n"
"s_waitcnt vmcnt(14) lgkmcnt(0)\n"
"s_barrier\n"
"v_mfma_f32_16x16x32_bf16 a[0:3], v[156:159], v[180:183], a[0:3]\n"
"ds_read_b64_tr_b16 v[124:125], v60 offset:0\n"
"v_mfma_f32_16x16x32_bf16 a[4:7], v[156:159], v[184:187], a[4:7]\n"
"ds_read_b64_tr_b16 v[126:127], v60 offset:8192\n"
"v_mfma_f32_16x16x32_bf16 a[8:11], v[156:159], v[188:191], a[8:11]\n"
"s_add_u32 s40, s53, %[sW1K]\n"
"s_add_u32 m0, s40, 0\n"
"s_add_u32 s40, s54, 0\n"
"buffer_load_dwordx4 v59, %[sW2], s40 offen nt lds\n"
"v_mfma_f32_16x16x32_bf16 a[12:15], v[156:159], v[192:195], a[12:15]\n"
"ds_read_b64_tr_b16 v[128:129], v61 offset:0\n"
"v_mfma_f32_16x16x32_bf16 a[16:19], v[156:159], v[196:199], a[16:19]\n"
"ds_read_b64_tr_b16 v[130:131], v61 offset:8192\n"
"v_mfma_f32_16x16x32_bf16 a[20:23], v[156:159], v[200:203], a[20:23]\n"
"s_add_u32 s40, s53, %[sW1K]\n"
"s_add_u32 m0, s40, 4096\n"
"s_add_u32 s40, s54, 32768\n"
"buffer_load_dwordx4 v59, %[sW2], s40 offen nt lds\n"
"v_mfma_f32_16x16x32_bf16 a[24:27], v[156:159], v[204:207], a[24:27]\n"
"ds_read_b64_tr_b16 v[132:133], v62 offset:0\n"
"v_mfma_f32_16x16x32_bf16 a[28:31], v[156:159], v[208:211], a[28:31]\n"
"ds_read_b64_tr_b16 v[134:135], v62 offset:8192\n"
"v_mfma_f32_16x16x32_bf16 a[32:35], v[160:163], v[180:183], a[32:35]\n"
"s_add_u32 s40, s53, %[sW1K]\n"
"s_add_u32 m0, s40, 8192\n"
"s_add_u32 s40, s54, 65536\n"
"buffer_load_dwordx4 v59, %[sW2], s40 offen nt lds\n"
"v_mfma_f32_16x16x32_bf16 a[36:39], v[160:163], v[184:187], a[36:39]\n"
"v_mfma_f32_16x16x32_bf16 a[40:43], v[160:163], v[188:191], a[40:43]\n"
"ds_read_b64_tr_b16 v[136:137], v63 offset:0\n"
"v_mfma_f32_16x16x32_bf16 a[44:47], v[160:163], v[192:195], a[44:47]\n"
"ds_read_b64_tr_b16 v[138:139], v63 offset:8192\n"
"v_mfma_f32_16x16x32_bf16 a[48:51], v[160:163], v[196:199], a[48:51]\n"
"s_add_u32 s40, s53, %[sW1K]\n"
"s_add_u32 m0, s40, 12288\n"
"s_add_u32 s40, s54, 98304\n"
"buffer_load_dwordx4 v59, %[sW2], s40 offen nt lds\n"
"v_mfma_f32_16x16x32_bf16 a[52:55], v[160:163], v[200:203], a[52:55]\n"
"ds_read_b64_tr_b16 v[140:141], v64 offset:0\n"
"v_mfma_f32_16x16x32_bf16 a[56:59], v[160:163], v[204:207], a[56:59]\n"
"ds_read_b64_tr_b16 v[142:143], v64 offset:8192\n"
"v_mfma_f32_16x16x32_bf16 a[60:63], v[160:163], v[208:211], a[60:63]\n"
"s_add_u32 s40, s53, %[sW3K]\n"
"s_add_u32 m0, s40, 16384\n"
"s_lshr_b32 s51, s52, 21\n"
"s_lshr_b32 s51, %[sFAR], s51\n"
"s_and_b32 s51, s51, 1\n"
"s_lshl_b32 s51, s51, 13\n"
"s_add_u32 s51, s51, s52\n"
"buffer_load_dwordx4 %[vL16], %[sWS], s51 offen offset:0 sc1 lds\n"
"v_mfma_f32_16x16x32_bf16 a[64:67], v[164:167], v[180:183], a[64:67]\n"
"ds_read_b64_tr_b16 v[144:145], v65 offset:0\n"
"v_mfma_f32_16x16x32_bf16 a[68:71], v[164:167], v[184:187], a[68:71]\n"
"ds_read_b64_tr_b16 v[146:147], v65 offset:8192\n"
"v_mfma_f32_16x16x32_bf16 a[72:75], v[164:167], v[188:191], a[72:75]\n"
"v_mfma_f32_16x16x32_bf16 a[76:79], v[164:167], v[192:195], a[76:79]\n"
"buffer_load_dwordx4 %[vL16], %[sWS], s51 offen offset:1024 sc1 lds\n"
"v_mfma_f32_16x16x32_bf16 a[80:83], v[164:167], v[196:199], a[80:83]\n"
"ds_read_b64_tr_b16 v[148:149], v66 offset:0\n"
"v_mfma_f32_16x16x32_bf16 a[84:87], v[164:167], v[200:203], a[84:87]\n"
"ds_read_b64_tr_b16 v[150:151], v66 offset:8192\n"
"v_mfma_f32_16x16x32_bf16 a[88:91], v[164:167], v[204:207], a[88:91]\n"
"buffer_load_dwordx4 %[vL16], %[sWS], s51 offen offset:2048 sc1 lds\n"
"v_mfma_f32_16x16x32_bf16 a[92:95], v[164:167], v[208:211], a[92:95]\n"
"ds_read_b64_tr_b16 v[152:153], v67 offset:0\n"
"v_mfma_f32_16x16x32_bf16 a[96:99], v[168:171], v[180:183], a[96:99]\n"
"ds_read_b64_tr_b16 v[154:155], v67 offset:8192\n"
"v_mfma_f32_16x16x32_bf16 a[100:103], v[168:171], v[184:187], a[100:103]\n"
"s_add_u32 s53, s53, 28672\n"
"s_cmp_ge_u32 s53, 114688\n"
"s_cselect_b32 s53, 0, s53\n"
"v_mfma_f32_16x16x32_bf16 a[104:107], v[168:171], v[188:191], a[104:107]\n"
"ds_read_b128 v[100:103], v58 offset:0\n"
"v_mfma_f32_16x16x32_bf16 a[108:111], v[168:171], v[192:195], a[108:111]\n"
"v_mfma_f32_16x16x32_bf16 a[112:115], v[168:171], v[196:199], a[112:115]\n"
"ds_read_b128 v[104:107], v58 offset:1024\n"
"v_mfma_f32_16x16x32_bf16 a[116:119], v[168:171], v[200:203], a[116:119]\n"
"ds_read_b128 v[108:111], v58 offset:2048\n"
"v_mfma_f32_16x16x32_bf16 a[120:123], v[168:171], v[204:207], a[120:123]\n"
"ds_read_b128 v[112:115], v58 offset:3072\n"
"v_mfma_f32_16x16x32_bf16 a[124:127], v[168:171], v[208:211], a[124:127]\n"
"ds_read_b128 v[116:119], v58 offset:4096\n"
"v_mfma_f32_16x16x32_bf16 a[128:131], v[172:175], v[180:183], a[128:131]\n"
"ds_read_b128 v[120:123], v58 offset:5120\n"
"v_mfma_f32_16x16x32_bf16 a[132:135], v[172:175], v[184:187], a[132:135]\n"
"s_add_u32 s44, s44, 28672\n"
"s_cmp_ge_u32 s44, 114688\n"
"s_cselect_b32 s45, 4294881280, s55\n"
"s_cselect_b32 s44, 0, s44\n"
"v_mfma_f32_16x16x32_bf16 a[136:139], v[172:175], v[188:191], a[136:139]\n"
"v_add_u32 v58, s45, v58\n"
"s_add_u32 s49, s49, 1\n"
"s_cmp_eq_u32 s49, s50\n"
"s_cselect_b32 s40, 3, 0\n"
"v_mfma_f32_16x16x32_bf16 a[140:143], v[172:175], v[192:195], a[140:143]\n"
"v_add_u32 v60, s45, v60\n"
"s_sub_u32 s49, s49, s40\n"
"s_add_u32 s42, s42, 1\n"
"v_mfma_f32_16x16x32_bf16 a[144:147], v[172:175], v[196:199], a[144:147]\n"
"v_mfma_f32_16x16x32_bf16 a[148:151], v[172:175], v[200:203], a[148:151]\n"
"v_add_u32 v61, s45, v61\n"
"s_cmp_eq_u32 s42, 3\n"
"s_cselect_b32 s40, 3, 0\n"
"s_sub_u32 s42, s42, s40\n"
"v_mfma_f32_16x16x32_bf16 a[152:155], v[172:175], v[204:207], a[152:155]\n"
"v_add_u32 v62, s45, v62\n"
"s_add_u32 s49, s49, s40\n"
"s_add_u32 s50, s50, s40\n"
"v_mfma_f32_16x16x32_bf16 a[156:159], v[172:175], v[208:211], a[156:159]\n"
"v_add_u32 v63, s45, v63\n"
"s_min_u32 s40, s49, 47\n"
"s_mul_i32 s54, s40, 131072\n"
"s_mul_i32 s52, s40, 98304\n"
"s_add_u32 s52, s52, %[sU2OFF]\n"
"v_mfma_f32_16x16x32_bf16 a[160:163], v[176:179], v[180:183], a[160:163]\n"
"v_add_u32 v64, s45, v64\n"
"v_mfma_f32_16x16x32_bf16 a[164:167], v[176:179], v[184:187], a[164:167]\n"
"v_add_u32 v65, s45, v65\n"
"v_mfma_f32_16x16x32_bf16 a[168:171], v[176:179], v[188:191], a[168:171]\n"
"v_add_u32 v66, s45, v66\n"
"v_mfma_f32_16x16x32_bf16 a[172:175], v[176:179], v[192:195], a[172:175]\n"
"v_add_u32 v67, s45, v67\n"
"v_mfma_f32_16x16x32_bf16 a[176:179], v[176:179], v[196:199], a[176:179]\n"
"s_sub_u32 s43, s43, 1\n"
"s_cmp_lg_u32 s43, 0\n"
"v_mfma_f32_16x16x32_bf16 a[180:183], v[176:179], v[200:203], a[180:183]\n"
"v_mfma_f32_16x16x32_bf16 a[184:187], v[176:179], v[204:207], a[184:187]\n"
"v_mfma_f32_16x16x32_bf16 a[188:191], v[176:179], v[208:211], a[188:191]\n"
"s_cbranch_scc1 LC_g2\n"
"s_waitcnt vmcnt(0) lgkmcnt(0)\n"
"s_barrier\n"
"s_mov_b32 s50, %[sYOFF]\n"
"s_lshr_b32 s48, s50, 21\n"
"s_lshr_b32 s48, %[sFAR], s48\n"
"s_and_b32 s48, s48, 1\n"
"s_lshl_b32 s48, s48, 13\n"
"s_add_u32 s48, s48, s50\n"
"v_accvgpr_read_b32 v128, a0\n"
"v_accvgpr_read_b32 v129, a1\n"
"v_accvgpr_read_b32 v130, a2\n"
"v_accvgpr_read_b32 v131, a3\n"
"v_accvgpr_read_b32 v132, a32\n"
"v_accvgpr_read_b32 v133, a33\n"
"v_accvgpr_read_b32 v134, a34\n"
"v_accvgpr_read_b32 v135, a35\n"
"v_cvt_pk_bf16_f32 v136, v128, v129\n"
"v_cvt_pk_bf16_f32 v137, v130, v131\n"
"v_cvt_pk_bf16_f32 v138, v132, v133\n"
"v_cvt_pk_bf16_f32 v139, v134, v135\n"
"buffer_store_dwordx4 v[136:139], %[vL16], %[sWS], s48 offen offset:0 sc1\n"
"v_accvgpr_read_b32 v144, a64\n"
"v_accvgpr_read_b32 v145, a65\n"
"v_accvgpr_read_b32 v146, a66\n"
"v_accvgpr_read_b32 v147, a67\n"
"v_accvgpr_read_b32 v148, a96\n"
"v_accvgpr_read_b32 v149, a97\n"
"v_accvgpr_read_b32 v150, a98\n"
"v_accvgpr_read_b32 v151, a99\n"
"v_cvt_pk_bf16_f32 v152, v144, v145\n"
"v_cvt_pk_bf16_f32 v153, v146, v147\n"
"v_cvt_pk_bf16_f32 v154, v148, v149\n"
"v_cvt_pk_bf16_f32 v155, v150, v151\n"
"buffer_store_dwordx4 v[152:155], %[vL16], %[sWS], s48 offen offset:1024 sc1\n"
"v_accvgpr_read_b32 v128, a128\n"
"v_accvgpr_read_b32 v129, a129\n"
"v_accvgpr_read_b32 v130, a130\n"
"v_accvgpr_read_b32 v131, a131\n"
"v_accvgpr_read_b32 v132, a160\n"
"v_accvgpr_read_b32 v133, a161\n"
"v_accvgpr_read_b32 v134, a162\n"
"v_accvgpr_read_b32 v135, a163\n"
"v_cvt_pk_bf16_f32 v136, v128, v129\n"
"v_cvt_pk_bf16_f32 v137, v130, v131\n"
"v_cvt_pk_bf16_f32 v138, v132, v133\n"
"v_cvt_pk_bf16_f32 v139, v134, v135\n"
"buffer_store_dwordx4 v[136:139], %[vL16], %[sWS], s48 offen offset:2048 sc1\n"
"s_add_u32 s50, s50, 49152\n"
"s_lshr_b32 s48, s50, 21\n"
"s_lshr_b32 s48, %[sFAR], s48\n"
"s_and_b32 s48, s48, 1\n"
"s_lshl_b32 s48, s48, 13\n"
"s_add_u32 s48, s48, s50\n"
"v_accvgpr_read_b32 v160, a4\n"
"v_accvgpr_read_b32 v161, a5\n"
"v_accvgpr_read_b32 v162, a6\n"
"v_accvgpr_read_b32 v163, a7\n"
"v_accvgpr_read_b32 v164, a36\n"
"v_accvgpr_read_b32 v165, a37\n"
"v_accvgpr_read_b32 v166, a38\n"
"v_accvgpr_read_b32 v167, a39\n"
"v_cvt_pk_bf16_f32 v168, v160, v161\n"
"v_cvt_pk_bf16_f32 v169, v162, v163\n"
"v_cvt_pk_bf16_f32 v170, v164, v165\n"
"v_cvt_pk_bf16_f32 v171, v166, v167\n"
"buffer_store_dwordx4 v[168:171], %[vL16], %[sWS], s48 offen offset:0 sc1\n"
"v_accvgpr_read_b32 v176, a68\n"
"v_accvgpr_read_b32 v177, a69\n"
"v_accvgpr_read_b32 v178, a70\n"
"v_accvgpr_read_b32 v179, a71\n"
"v_accvgpr_read_b32 v180, a100\n"
"v_accvgpr_read_b32 v181, a101\n"
"v_accvgpr_read_b32 v182, a102\n"
"v_accvgpr_read_b32 v183, a103\n"
"v_cvt_pk_bf16_f32 v184, v176, v177\n"
"v_cvt_pk_bf16_f32 v185, v178, v179\n"
"v_cvt_pk_bf16_f32 v186, v180, v181\n"
"v_cvt_pk_bf16_f32 v187, v182, v183\n"
"buffer_store_dwordx4 v[184:187], %[vL16], %[sWS], s48 offen offset:1024 sc1\n"
"v_accvgpr_read_b32 v160, a132\n"
"v_accvgpr_read_b32 v161, a133\n"
"v_accvgpr_read_b32 v162, a134\n"
"v_accvgpr_read_b32 v163, a135\n"
"v_accvgpr_read_b32 v164, a164\n"
"v_accvgpr_read_b32 v165, a165\n"
"v_accvgpr_read_b32 v166, a166\n"
"v_accvgpr_read_b32 v167, a167\n"
"v_cvt_pk_bf16_f32 v168, v160, v161\n"
"v_cvt_pk_bf16_f32 v169, v162, v163\n"
"v_cvt_pk_bf16_f32 v170, v164, v165\n"
"v_cvt_pk_bf16_f32 v171, v166, v167\n"
"buffer_store_dwordx4 v[168:171], %[vL16], %[sWS], s48 offen offset:2048 sc1\n"
"s_add_u32 s50, s50, 49152\n"
"s_lshr_b32 s48, s50, 21\n"
"s_lshr_b32 s48, %[sFAR], s48\n"
"s_and_b32 s48, s48, 1\n"
"s_lshl_b32 s48, s48, 13\n"
"s_add_u32 s48, s48, s50\n"
"v_accvgpr_read_b32 v128, a8\n"
"v_accvgpr_read_b32 v129, a9\n"
"v_accvgpr_read_b32 v130, a10\n"
"v_accvgpr_read_b32 v131, a11\n"
"v_accvgpr_read_b32 v132, a40\n"
"v_accvgpr_read_b32 v133, a41\n"
"v_accvgpr_read_b32 v134, a42\n"
"v_accvgpr_read_b32 v135, a43\n"
"v_cvt_pk_bf16_f32 v136, v128, v129\n"
"v_cvt_pk_bf16_f32 v137, v130, v131\n"
"v_cvt_pk_bf16_f32 v138, v132, v133\n"
"v_cvt_pk_bf16_f32 v139, v134, v135\n"
"buffer_store_dwordx4 v[136:139], %[vL16], %[sWS], s48 offen offset:0 sc1\n"
"v_accvgpr_read_b32 v144, a72\n"
"v_accvgpr_read_b32 v145, a73\n"
"v_accvgpr_read_b32 v146, a74\n"
"v_accvgpr_read_b32 v147, a75\n"
"v_accvgpr_read_b32 v148, a104\n"
"v_accvgpr_read_b32 v149, a105\n"
"v_accvgpr_read_b32 v150, a106\n"
"v_accvgpr_read_b32 v151, a107\n"
"v_cvt_pk_bf16_f32 v152, v144, v145\n"
"v_cvt_pk_bf16_f32 v153, v146, v147\n"
"v_cvt_pk_bf16_f32 v154, v148, v149\n"
"v_cvt_pk_bf16_f32 v155, v150, v151\n"
"buffer_store_dwordx4 v[152:155], %[vL16], %[sWS], s48 offen offset:1024 sc1\n"
"v_accvgpr_read_b32 v128, a136\n"
"v_accvgpr_read_b32 v129, a137\n"
"v_accvgpr_read_b32 v130, a138\n"
"v_accvgpr_read_b32 v131, a139\n"
"v_accvgpr_read_b32 v132, a168\n"
"v_accvgpr_read_b32 v133, a169\n"
"v_accvgpr_read_b32 v134, a170\n"
"v_accvgpr_read_b32 v135, a171\n"
"v_cvt_pk_bf16_f32 v136, v128, v129\n"
"v_cvt_pk_bf16_f32 v137, v130, v131\n"
"v_cvt_pk_bf16_f32 v138, v132, v133\n"
"v_cvt_pk_bf16_f32 v139, v134, v135\n"
"buffer_store_dwordx4 v[136:139], %[vL16], %[sWS], s48 offen offset:2048 sc1\n"
"s_add_u32 s50, s50, 49152\n"
"s_lshr_b32 s48, s50, 21\n"
"s_lshr_b32 s48, %[sFAR], s48\n"
"s_and_b32 s48, s48, 1\n"
"s_lshl_b32 s48, s48, 13\n"
"s_add_u32 s48, s48, s50\n"
"v_accvgpr_read_b32 v160, a12\n"
"v_accvgpr_read_b32 v161, a13\n"
"v_accvgpr_read_b32 v162, a14\n"
"v_accvgpr_read_b32 v163, a15\n"
"v_accvgpr_read_b32 v164, a44\n"
"v_accvgpr_read_b32 v165, a45\n"
"v_accvgpr_read_b32 v166, a46\n"
"v_accvgpr_read_b32 v167, a47\n"
"v_cvt_pk_bf16_f32 v168, v160, v161\n"
"v_cvt_pk_bf16_f32 v169, v162, v163\n"
"v_cvt_pk_bf16_f32 v170, v164, v165\n"
"v_cvt_pk_bf16_f32 v171, v166, v167\n"
"buffer_store_dwordx4 v[168:171], %[vL16], %[sWS], s48 offen offset:0 sc1\n"
"v_accvgpr_read_b32 v176, a76\n"
"v_accvgpr_read_b32 v177, a77\n"
"v_accvgpr_read_b32 v178, a78\n"
"v_accvgpr_read_b32 v179, a79\n"
"v_accvgpr_read_b32 v180, a108\n"
"v_accvgpr_read_b32 v181, a109\n"
"v_accvgpr_read_b32 v182, a110\n"
"v_accvgpr_read_b32 v183, a111\n"
"v_cvt_pk_bf16_f32 v184, v176, v177\n"
"v_cvt_pk_bf16_f32 v185, v178, v179\n"
"v_cvt_pk_bf16_f32 v186, v180, v181\n"
"v_cvt_pk_bf16_f32 v187, v182, v183\n"
"buffer_store_dwordx4 v[184:187], %[vL16], %[sWS], s48 offen offset:1024 sc1\n"
"v_accvgpr_read_b32 v160, a140\n"
"v_accvgpr_read_b32 v161, a141\n"
"v_accvgpr_read_b32 v162, a142\n"
"v_accvgpr_read_b32 v163, a143\n"
"v_accvgpr_read_b32 v164, a172\n"
"v_accvgpr_read_b32 v165, a173\n"
"v_accvgpr_read_b32 v166, a174\n"
"v_accvgpr_read_b32 v167, a175\n"
"v_cvt_pk_bf16_f32 v168, v160, v161\n"
"v_cvt_pk_bf16_f32 v169, v162, v163\n"
"v_cvt_pk_bf16_f32 v170, v164, v165\n"
"v_cvt_pk_bf16_f32 v171, v166, v167\n"
"buffer_store_dwordx4 v[168:171], %[vL16], %[sWS], s48 offen offset:2048 sc1\n"
"s_add_u32 s50, s50, 49152\n"
"s_lshr_b32 s48, s50, 21\n"
"s_lshr_b32 s48, %[sFAR], s48\n"
"s_and_b32 s48, s48, 1\n"
"s_lshl_b32 s48, s48, 13\n"
"s_add_u32 s48, s48, s50\n"
"v_accvgpr_read_b32 v128, a16\n"
"v_accvgpr_read_b32 v129, a17\n"
"v_accvgpr_read_b32 v130, a18\n"
"v_accvgpr_read_b32 v131, a19\n"
"v_accvgpr_read_b32 v132, a48\n"
"v_accvgpr_read_b32 v133, a49\n"
"v_accvgpr_read_b32 v134, a50\n"
"v_accvgpr_read_b32 v135, a51\n"
"v_cvt_pk_bf16_f32 v136, v128, v129\n"
"v_cvt_pk_bf16_f32 v137, v130, v131\n"
"v_cvt_pk_bf16_f32 v138, v132, v133\n"
"v_cvt_pk_bf16_f32 v139, v134, v135\n"
"buffer_store_dwordx4 v[136:139], %[vL16], %[sWS], s48 offen offset:0 sc1\n"
"v_accvgpr_read_b32 v144, a80\n"
"v_accvgpr_read_b32 v145, a81\n"
"v_accvgpr_read_b32 v146, a82\n"
"v_accvgpr_read_b32 v147, a83\n"
"v_accvgpr_read_b32 v148, a112\n"
"v_accvgpr_read_b32 v149, a113\n"
"v_accvgpr_read_b32 v150, a114\n"
"v_accvgpr_read_b32 v151, a115\n"
"v_cvt_pk_bf16_f32 v152, v144, v145\n"
"v_cvt_pk_bf16_f32 v153, v146, v147\n"
"v_cvt_pk_bf16_f32 v154, v148, v149\n"
"v_cvt_pk_bf16_f32 v155, v150, v151\n"
"buffer_store_dwordx4 v[152:155], %[vL16], %[sWS], s48 offen offset:1024 sc1\n"
"v_accvgpr_read_b32 v128, a144\n"
"v_accvgpr_read_b32 v129, a145\n"
"v_accvgpr_read_b32 v130, a146\n"
"v_accvgpr_read_b32 v131, a147\n"
"v_accvgpr_read_b32 v132, a176\n"
"v_accvgpr_read_b32 v133, a177\n"
"v_accvgpr_read_b32 v134, a178\n"
"v_accvgpr_read_b32 v135, a179\n"
"v_cvt_pk_bf16_f32 v136, v128, v129\n"
"v_cvt_pk_bf16_f32 v137, v130, v131\n"
"v_cvt_pk_bf16_f32 v138, v132, v133\n"
"v_cvt_pk_bf16_f32 v139, v134, v135\n"
"buffer_store_dwordx4 v[136:139], %[vL16], %[sWS], s48 offen offset:2048 sc1\n"
"s_add_u32 s50, s50, 49152\n"
"s_lshr_b32 s48, s50, 21\n"
"s_lshr_b32 s48, %[sFAR], s48\n"
"s_and_b32 s48, s48, 1\n"
"s_lshl_b32 s48, s48, 13\n"
"s_add_u32 s48, s48, s50\n"
"v_accvgpr_read_b32 v160, a20\n"
"v_accvgpr_read_b32 v161, a21\n"
"v_accvgpr_read_b32 v162, a22\n"
"v_accvgpr_read_b32 v163, a23\n"
"v_accvgpr_read_b32 v164, a52\n"
"v_accvgpr_read_b32 v165, a53\n"
"v_accvgpr_read_b32 v166, a54\n"
"v_accvgpr_read_b32 v167, a55\n"
"v_cvt_pk_bf16_f32 v168, v160, v161\n"
"v_cvt_pk_bf16_f32 v169, v162, v163\n"
"v_cvt_pk_bf16_f32 v170, v164, v165\n"
"v_cvt_pk_bf16_f32 v171, v166, v167\n"
"buffer_store_dwordx4 v[168:171], %[vL16], %[sWS], s48 offen offset:0 sc1\n"
"v_accvgpr_read_b32 v176, a84\n"
"v_accvgpr_read_b32 v177, a85\n"
"v_accvgpr_read_b32 v178, a86\n"
"v_accvgpr_read_b32 v179, a87\n"
"v_accvgpr_read_b32 v180, a116\n"
"v_accvgpr_read_b32 v181, a117\n"
"v_accvgpr_read_b32 v182, a118\n"
"v_accvgpr_read_b32 v183, a119\n"
"v_cvt_pk_bf16_f32 v184, v176, v177\n"
"v_cvt_pk_bf16_f32 v185, v178, v179\n"
"v_cvt_pk_bf16_f32 v186, v180, v181\n"
"v_cvt_pk_bf16_f32 v187, v182, v183\n"
"buffer_store_dwordx4 v[184:187], %[vL16], %[sWS], s48 offen offset:1024 sc1\n"
"v_accvgpr_read_b32 v160, a148\n"
"v_accvgpr_read_b32 v161, a149\n"
"v_accvgpr_read_b32 v162, a150\n"
"v_accvgpr_read_b32 v163, a151\n"
"v_accvgpr_read_b32 v164, a180\n"
"v_accvgpr_read_b32 v165, a181\n"
"v_accvgpr_read_b32 v166, a182\n"
"v_accvgpr_read_b32 v167, a183\n"
"v_cvt_pk_bf16_f32 v168, v160, v161\n"
"v_cvt_pk_bf16_f32 v169, v162, v163\n"
"v_cvt_pk_bf16_f32 v170, v164, v165\n"
"v_cvt_pk_bf16_f32 v171, v166, v167\n"
"buffer_store_dwordx4 v[168:171], %[vL16], %[sWS], s48 offen offset:2048 sc1\n"
"s_add_u32 s50, s50, 49152\n"
"s_lshr_b32 s48, s50, 21\n"
"s_lshr_b32 s48, %[sFAR], s48\n"
"s_and_b32 s48, s48, 1\n"
"s_lshl_b32 s48, s48, 13\n"
"s_add_u32 s48, s48, s50\n"
"v_accvgpr_read_b32 v128, a24\n"
"v_accvgpr_read_b32 v129, a25\n"
"v_accvgpr_read_b32 v130, a26\n"
"v_accvgpr_read_b32 v131, a27\n"
"v_accvgpr_read_b32 v132, a56\n"
"v_accvgpr_read_b32 v133, a57\n"
"v_accvgpr_read_b32 v134, a58\n"
"v_accvgpr_read_b32 v135, a59\n"
"v_cvt_pk_bf16_f32 v136, v128, v129\n"
"v_cvt_pk_bf16_f32 v137, v130, v131\n"
"v_cvt_pk_bf16_f32 v138, v132, v133\n"
"v_cvt_pk_bf16_f32 v139, v134, v135\n"
"buffer_store_dwordx4 v[136:139], %[vL16], %[sWS], s48 offen offset:0 sc1\n"
"v_accvgpr_read_b32 v144, a88\n"
"v_accvgpr_read_b32 v145, a89\n"
"v_accvgpr_read_b32 v146, a90\n"
"v_accvgpr_read_b32 v147, a91\n"
"v_accvgpr_read_b32 v148, a120\n"
"v_accvgpr_read_b32 v149, a121\n"
"v_accvgpr_read_b32 v150, a122\n"
"v_accvgpr_read_b32 v151, a123\n"
"v_cvt_pk_bf16_f32 v152, v144, v145\n"
"v_cvt_pk_bf16_f32 v153, v146, v147\n"
"v_cvt_pk_bf16_f32 v154, v148, v149\n"
"v_cvt_pk_bf16_f32 v155, v150, v151\n"
"buffer_store_dwordx4 v[152:155], %[vL16], %[sWS], s48 offen offset:1024 sc1\n"
"v_accvgpr_read_b32 v128, a152\n"
"v_accvgpr_read_b32 v129, a153\n"
"v_accvgpr_read_b32 v130, a154\n"
"v_accvgpr_read_b32 v131, a155\n"
"v_accvgpr_read_b32 v132, a184\n"
"v_accvgpr_read_b32 v133, a185\n"
"v_accvgpr_read_b32 v134, a186\n"
"v_accvgpr_read_b32 v135, a187\n"
"v_cvt_pk_bf16_f32 v136, v128, v129\n"
"v_cvt_pk_bf16_f32 v137, v130, v131\n"
"v_cvt_pk_bf16_f32 v138, v132, v133\n"
"v_cvt_pk_bf16_f32 v139, v134, v135\n"
"buffer_store_dwordx4 v[136:139], %[vL16], %[sWS], s48 offen offset:2048 sc1\n"
"s_add_u32 s50, s50, 49152\n"
"s_lshr_b32 s48, s50, 21\n"
"s_lshr_b32 s48, %[sFAR], s48\n"
"s_and_b32 s48, s48, 1\n"
"s_lshl_b32 s48, s48, 13\n"
"s_add_u32 s48, s48, s50\n"
"v_accvgpr_read_b32 v160, a28\n"
"v_accvgpr_read_b32 v161, a29\n"
"v_accvgpr_read_b32 v162, a30\n"
"v_accvgpr_read_b32 v163, a31\n"
"v_accvgpr_read_b32 v164, a60\n"
"v_accvgpr_read_b32 v165, a61\n"
"v_accvgpr_read_b32 v166, a62\n"
"v_accvgpr_read_b32 v167, a63\n"
"v_cvt_pk_bf16_f32 v168, v160, v161\n"
"v_cvt_pk_bf16_f32 v169, v162, v163\n"
"v_cvt_pk_bf16_f32 v170, v164, v165\n"
"v_cvt_pk_bf16_f32 v171, v166, v167\n"
"buffer_store_dwordx4 v[168:171], %[vL16], %[sWS], s48 offen offset:0 sc1\n"
"v_accvgpr_read_b32 v176, a92\n"
"v_accvgpr_read_b32 v177, a93\n"
"v_accvgpr_read_b32 v178, a94\n"
"v_accvgpr_read_b32 v179, a95\n"
"v_accvgpr_read_b32 v180, a124\n"
"v_accvgpr_read_b32 v181, a125\n"
"v_accvgpr_read_b32 v182, a126\n"
"v_accvgpr_read_b32 v183, a127\n"
"v_cvt_pk_bf16_f32 v184, v176, v177\n"
"v_cvt_pk_bf16_f32 v185, v178, v179\n"
"v_cvt_pk_bf16_f32 v186, v180, v181\n"
"v_cvt_pk_bf16_f32 v187, v182, v183\n"
"buffer_store_dwordx4 v[184:187], %[vL16], %[sWS], s48 offen offset:1024 sc1\n"
"v_accvgpr_read_b32 v160, a156\n"
"v_accvgpr_read_b32 v161, a157\n"
"v_accvgpr_read_b32 v162, a158\n"
"v_accvgpr_read_b32 v163, a159\n"
"v_accvgpr_read_b32 v164, a188\n"
"v_accvgpr_read_b32 v165, a189\n"
"v_accvgpr_read_b32 v166, a190\n"
"v_accvgpr_read_b32 v167, a191\n"
"v_cvt_pk_bf16_f32 v168, v160, v161\n"
"v_cvt_pk_bf16_f32 v169, v162, v163\n"
"v_cvt_pk_bf16_f32 v170, v164, v165\n"
"v_cvt_pk_bf16_f32 v171, v166, v167\n"
"buffer_store_dwordx4 v[168:171], %[vL16], %[sWS], s48 offen offset:2048 sc1\n"
"s_add_u32 s50, s50, 49152\n"
"s_waitcnt vmcnt(0)\n"
"s_barrier\n"
"v_mov_b32 v20, 1\n"
"s_mov_b32 s40, 1134592\n"
"buffer_store_dword v20, %[vFL], %[sWS], s40 offen sc1\n"
"s_branch LC_done\n"
"LC_tmo:\n"
"s_mov_b64 exec, 1\n"
"v_mov_b32 v20, 4\n"
"s_mov_b32 s40, 1065088\n"
"buffer_store_dword v20, off, %[sWS], s40 sc1\n"
"s_endpgm\n"
"LC_done:\n"
        :
        : [sWS] "s"(sWS), [sW2] "s"(sW2), [sW3K] "s"(sW3K), [sW1K] "s"(sW1K), [sU2OFF] "s"(sU2OFF), [sK0] "s"(rfl(dt * K0J + (tid & 0))), [sYOFF] "s"(sYOFF), [sFAR] "s"(wt_par(ws)),
          [vG] "v"(vG), [vT] "v"(vT), [vR] "v"(vR), [vPF] "v"(vPF), [vL16] "v"(vL16), [vFL] "v"(vFL)
        : "v20", "v21", "v22", "v23", "v24", "v25", "v26", "v27", "v28", "v29", "v30", "v31", "v32", "v33", "v34", "v35", "v36", "v37", "v38", "v39", "v40", "v41", "v42", "v43",
"v44", "v45", "v46", "v47", "v48", "v49", "v50", "v51", "v52", "v53", "v54", "v55", "v56", "v57", "v58", "v59", "v60", "v61", "v62", "v63", "v64", "v65", "v66", "v67",
"v68", "v69", "v70", "v71", "v72", "v73", "v74", "v75", "v76", "v77", "v78", "v79", "v80", "v81", "v82", "v83", "v84", "v85", "v86", "v87", "v88", "v89", "v90", "v91",
"v92", "v93", "v94", "v95", "v96", "v97", "v98", "v99", "v100", "v101", "v102", "v103", "v104", "v105", "v106", "v107", "v108", "v109", "v110", "v111", "v112", "v113", "v114", "v115",
"v116", "v117", "v118", "v119", "v120", "v121", "v122", "v123", "v124", "v125", "v126", "v127", "v128", "v129", "v130", "v131", "v132", "v133", "v134", "v135", "v136", "v137", "v138", "v139",
"v140", "v141", "v142", "v143", "v144", "v145", "v146", "v147", "v148", "v149", "v150", "v151", "v152", "v153", "v154", "v155", "v156", "v157", "v158", "v159", "v160", "v161", "v162", "v163",
"v164", "v165", "v166", "v167", "v168", "v169", "v170", "v171", "v172", "v173", "v174", "v175", "v176", "v177", "v178", "v179", "v180", "v181", "v182", "v183", "v184", "v185", "v186", "v187",
"v188", "v189", "v190", "v191", "v192", "v193", "v194", "v195", "v196", "v197", "v198", "v199", "v200", "v201", "v202", "v203", "v204", "v205", "v206", "v207", "v208", "v209", "v210", "v211",
"v212", "v213", "v214", "v215", "v216", "v217", "v218", "v219", "v220", "v221", "v222", "v223", "v224", "v225", "v226", "v227", "v228", "v229", "v230", "v231", "v232", "v233", "v234", "v235",
"v236", "v237", "v238", "v239", "v240", "v241", "v242", "v243", "v244", "v245", "v246", "v247", "v248", "v249", "v250", "v251", "v252", "v253", "v254", "v255", "a0", "a1", "a2", "a3",
"a4", "a5", "a6", "a7", "a8", "a9", "a10", "a11", "a12", "a13", "a14", "a15", "a16", "a17", "a18", "a19", "a20", "a21", "a22", "a23", "a24", "a25", "a26", "a27",
"a28", "a29", "a30", "a31", "a32", "a33", "a34", "a35", "a36", "a37", "a38", "a39", "a40", "a41", "a42", "a43", "a44", "a45", "a46", "a47", "a48", "a49", "a50", "a51",
"a52", "a53", "a54", "a55", "a56", "a57", "a58", "a59", "a60", "a61", "a62", "a63", "a64", "a65", "a66", "a67", "a68", "a69", "a70", "a71", "a72", "a73", "a74", "a75",
"a76", "a77", "a78", "a79", "a80", "a81", "a82", "a83", "a84", "a85", "a86", "a87", "a88", "a89", "a90", "a91", "a92", "a93", "a94", "a95", "a96", "a97", "a98", "a99",
"a100", "a101", "a102", "a103", "a104", "a105", "a106", "a107", "a108", "a109", "a110", "a111", "a112", "a113", "a114", "a115", "a116", "a117", "a118", "a119", "a120", "a121", "a122", "a123",
"a124", "a125", "a126", "a127", "a128", "a129", "a130", "a131", "a132", "a133", "a134", "a135", "a136", "a137", "a138", "a139", "a140", "a141", "a142", "a143", "a144", "a145", "a146", "a147",
"a148", "a149", "a150", "a151", "a152", "a153", "a154", "a155", "a156", "a157", "a158", "a159", "a160", "a161", "a162", "a163", "a164", "a165", "a166", "a167", "a168", "a169", "a170", "a171",
"a172", "a173", "a174", "a175", "a176", "a177", "a178", "a179", "a180", "a181", "a182", "a183", "a184", "a185", "a186", "a187", "a188", "a189", "a190", "a191", "a192", "a193", "a194", "a195",
"a196", "a197", "a198", "a199", "a200", "a201", "a202", "a203", "a204", "a205", "a206", "a207", "a208", "a209", "a210", "a211", "a212", "a213", "a214", "a215", "a216", "a217", "a218", "a219",
"a220", "a221", "a222", "a223", "a224", "a225", "a226", "a227", "a228", "a229", "a230", "a231", "a232", "a233", "a234", "a235", "a236", "a237", "a238", "a239", "a240", "a241", "a242", "a243",
"a244", "a245", "a246", "a247", "a248", "a249", "a250", "a251", "a252", "a253", "a254", "a255", "s40", "s41", "s42", "s43", "s44", "s45", "s46", "s47", "s48", "s49", "s50", "s51",
"s52", "s53", "s54", "s55", "s56", "s57", "vcc", "scc", "memory");
}

__device__ __forceinline__ int wt_off(int par, int i) {
    const unsigned o = WS_WT + i * 131072;
    return rfl(o + ((par >> ((o >> 21) & 31)) & 1) * 8192 + (i & 7) * 1024);
}
__device__ __forceinline__ void role_wt(char* ws, const void* wo, int par, int Jq, int n) {
    int tid = threadIdx.x; asm volatile("" : "+v"(tid));
    const int w = rfl(tid >> 6), l = tid & 63, g = l >> 4, c = l & 15;
    const v4i sWS = make_srd(ws, WS_B), sWO = make_srd(wo, 4096 * 1152);
    const unsigned vG = (w * 1024 + n * 128 + (l >> 3)) * 1152 + 128 * Jq + (l & 7) * 16;
    const unsigned vT = w * 32768 + (4 * g + (c >> 2)) * 128 + (c & 3) * 16, vL16 = l * 16;
    asm volatile(
"s_nop 4\n"
"s_mov_b32 s40, 0\n"
"s_add_u32 m0, %[sW32K], 0\n"
"s_nop 0\n"
"buffer_load_dwordx4 %[vG], %[sWO], s40 offen nt lds\n"
"s_mov_b32 s40, 9216\n"
"s_add_u32 m0, %[sW32K], 1024\n"
"s_nop 0\n"
"buffer_load_dwordx4 %[vG], %[sWO], s40 offen nt lds\n"
"s_mov_b32 s40, 18432\n"
"s_add_u32 m0, %[sW32K], 2048\n"
"s_nop 0\n"
"buffer_load_dwordx4 %[vG], %[sWO], s40 offen nt lds\n"
"s_mov_b32 s40, 27648\n"
"s_add_u32 m0, %[sW32K], 3072\n"
"s_nop 0\n"
"buffer_load_dwordx4 %[vG], %[sWO], s40 offen nt lds\n"
"s_mov_b32 s40, 36864\n"
"s_add_u32 m0, %[sW32K], 4096\n"
"s_nop 0\n"
"buffer_load_dwordx4 %[vG], %[sWO], s40 offen nt lds\n"
"s_mov_b32 s40, 46080\n"
"s_add_u32 m0, %[sW32K], 5120\n"
"s_nop 0\n"
"buffer_load_dwordx4 %[vG], %[sWO], s40 offen nt lds\n"
"s_mov_b32 s40, 55296\n"
"s_add_u32 m0, %[sW32K], 6144\n"
"s_nop 0\n"
"buffer_load_dwordx4 %[vG], %[sWO], s40 offen nt lds\n"
"s_mov_b32 s40, 64512\n"
"s_add_u32 m0, %[sW32K], 7168\n"
"s_nop 0\n"
"buffer_load_dwordx4 %[vG], %[sWO], s40 offen nt lds\n"
"s_mov_b32 s40, 73728\n"
"s_add_u32 m0, %[sW32K], 8192\n"
"s_nop 0\n"
"buffer_load_dwordx4 %[vG], %[sWO], s40 offen nt lds\n"
"s_mov_b32 s40, 82944\n"
"s_add_u32 m0, %[sW32K], 9216\n"
"s_nop 0\n"
"buffer_load_dwordx4 %[vG], %[sWO], s40 offen nt lds\n"
"s_mov_b32 s40, 92160\n"
"s_add_u32 m0, %[sW32K], 10240\n"
"s_nop 0\n"
"buffer_load_dwordx4 %[vG], %[sWO], s40 offen nt lds\n"
"s_mov_b32 s40, 101376\n"
"s_add_u32 m0, %[sW32K], 11264\n"
"s_nop 0\n"
"buffer_load_dwordx4 %[vG], %[sWO], s40 offen nt lds\n"
"s_mov_b32 s40, 110592\n"
"s_add_u32 m0, %[sW32K], 12288\n"
"s_nop 0\n"
"buffer_load_dwordx4 %[vG], %[sWO], s40 offen nt lds\n"
"s_mov_b32 s40, 119808\n"
"s_add_u32 m0, %[sW32K], 13312\n"
"s_nop 0\n"
"buffer_load_dwordx4 %[vG], %[sWO], s40 offen nt lds\n"
"s_mov_b32 s40, 129024\n"
"s_add_u32 m0, %[sW32K], 14336\n"
"s_nop 0\n"
"buffer_load_dwordx4 %[vG], %[sWO], s40 offen nt lds\n"
"s_mov_b32 s40, 138240\n"
"s_add_u32 m0, %[sW32K], 15360\n"
"s_nop 0\n"
"buffer_load_dwordx4 %[vG], %[sWO], s40 offen nt lds\n"
"s_waitcnt vmcnt(12)\n"
"ds_read_b64_tr_b16 v[32:33], %[vT] offset:0\n"
"ds_read_b64_tr_b16 v[34:35], %[vT] offset:2048\n"
"ds_read_b64_tr_b16 v[36:37], %[vT] offset:8\n"
"ds_read_b64_tr_b16 v[38:39], %[vT] offset:2056\n"
"ds_read_b64_tr_b16 v[40:41], %[vT] offset:64\n"
"ds_read_b64_tr_b16 v[42:43], %[vT] offset:2112\n"
"ds_read_b64_tr_b16 v[44:45], %[vT] offset:72\n"
"ds_read_b64_tr_b16 v[46:47], %[vT] offset:2120\n"
"s_waitcnt lgkmcnt(0)\n"
"s_xor_b32 s45, %[sD0], 0\n"
"buffer_store_dwordx4 v[32:35], %[vL16], %[sWS], s45 offen sc1\n"
"s_xor_b32 s45, %[sD0], 1024\n"
"buffer_store_dwordx4 v[36:39], %[vL16], %[sWS], s45 offen sc1\n"
"s_xor_b32 s45, %[sD1], 0\n"
"buffer_store_dwordx4 v[40:43], %[vL16], %[sWS], s45 offen sc1\n"
"s_xor_b32 s45, %[sD1], 1024\n"
"buffer_store_dwordx4 v[44:47], %[vL16], %[sWS], s45 offen sc1\n"
"s_waitcnt vmcnt(12)\n"
"ds_read_b64_tr_b16 v[48:49], %[vT] offset:4096\n"
"ds_read_b64_tr_b16 v[50:51], %[vT] offset:6144\n"
"ds_read_b64_tr_b16 v[52:53], %[vT] offset:4104\n"
"ds_read_b64_tr_b16 v[54:55], %[vT] offset:6152\n"
"ds_read_b64_tr_b16 v[56:57], %[vT] offset:4160\n"
"ds_read_b64_tr_b16 v[58:59], %[vT] offset:6208\n"
"ds_read_b64_tr_b16 v[60:61], %[vT] offset:4168\n"
"ds_read_b64_tr_b16 v[62:63], %[vT] offset:6216\n"
"s_waitcnt lgkmcnt(0)\n"
"s_xor_b32 s45, %[sD0], 2048\n"
"buffer_store_dwordx4 v[48:51], %[vL16], %[sWS], s45 offen sc1\n"
"s_xor_b32 s45, %[sD0], 3072\n"
"buffer_store_dwordx4 v[52:55], %[vL16], %[sWS], s45 offen sc1\n"
"s_xor_b32 s45, %[sD1], 2048\n"
"buffer_store_dwordx4 v[56:59], %[vL16], %[sWS], s45 offen sc1\n"
"s_xor_b32 s45, %[sD1], 3072\n"
"buffer_store_dwordx4 v[60:63], %[vL16], %[sWS], s45 offen sc1\n"
"s_waitcnt vmcnt(12)\n"
"ds_read_b64_tr_b16 v[64:65], %[vT] offset:8192\n"
"ds_read_b64_tr_b16 v[66:67], %[vT] offset:10240\n"
"ds_read_b64_tr_b16 v[68:69], %[vT] offset:8200\n"
"ds_read_b64_tr_b16 v[70:71], %[vT] offset:10248\n"
"ds_read_b64_tr_b16 v[72:73], %[vT] offset:8256\n"
"ds_read_b64_tr_b16 v[74:75], %[vT] offset:10304\n"
"ds_read_b64_tr_b16 v[76:77], %[vT] offset:8264\n"
"ds_read_b64_tr_b16 v[78:79], %[vT] offset:10312\n"
"s_waitcnt lgkmcnt(0)\n"
"s_xor_b32 s45, %[sD0], 4096\n"
"buffer_store_dwordx4 v[64:67], %[vL16], %[sWS], s45 offen sc1\n"
"s_xor_b32 s45, %[sD0], 5120\n"
"buffer_store_dwordx4 v[68:71], %[vL16], %[sWS], s45 offen sc1\n"
"s_xor_b32 s45, %[sD1], 4096\n"
"buffer_store_dwordx4 v[72:75], %[vL16], %[sWS], s45 offen sc1\n"
"s_xor_b32 s45, %[sD1], 5120\n"
"buffer_store_dwordx4 v[76:79], %[vL16], %[sWS], s45 offen sc1\n"
"s_waitcnt vmcnt(12)\n"
"ds_read_b64_tr_b16 v[80:81], %[vT] offset:12288\n"
"ds_read_b64_tr_b16 v[82:83], %[vT] offset:14336\n"
"ds_read_b64_tr_b16 v[84:85], %[vT] offset:12296\n"
"ds_read_b64_tr_b16 v[86:87], %[vT] offset:14344\n"
"ds_read_b64_tr_b16 v[88:89], %[vT] offset:12352\n"
"ds_read_b64_tr_b16 v[90:91], %[vT] offset:14400\n"
"ds_read_b64_tr_b16 v[92:93], %[vT] offset:12360\n"
"ds_read_b64_tr_b16 v[94:95], %[vT] offset:14408\n"
"s_waitcnt lgkmcnt(0)\n"
"s_xor_b32 s45, %[sD0], 6144\n"
"buffer_store_dwordx4 v[80:83], %[vL16], %[sWS], s45 offen sc1\n"
"s_xor_b32 s45, %[sD0], 7168\n"
"buffer_store_dwordx4 v[84:87], %[vL16], %[sWS], s45 offen sc1\n"
"s_xor_b32 s45, %[sD1], 6144\n"
"buffer_store_dwordx4 v[88:91], %[vL16], %[sWS], s45 offen sc1\n"
"s_xor_b32 s45, %[sD1], 7168\n"
"buffer_store_dwordx4 v[92:95], %[vL16], %[sWS], s45 offen sc1\n"
"s_nop 1\n"
        :
        : [sWS] "s"(sWS), [sWO] "s"(sWO), [sW32K] "s"(w * 32768), [sD0] "s"(wt_off(par, Jq * 8 + w) + n * 16384), [sD1] "s"(wt_off(par, Jq * 8 + 4 + w) + n * 16384),
          [vG] "v"(vG), [vT] "v"(vT), [vL16] "v"(vL16)
        : "v20", "v21", "v22", "v23", "v24", "v25", "v26", "v27", "v28", "v29", "v30", "v31", "v32", "v33", "v34", "v35", "v36", "v37", "v38", "v39", "v40", "v41", "v42", "v43",
"v44", "v45", "v46", "v47", "v48", "v49", "v50", "v51", "v52", "v53", "v54", "v55", "v56", "v57", "v58", "v59", "v60", "v61", "v62", "v63", "v64", "v65", "v66", "v67",
"v68", "v69", "v70", "v71", "v72", "v73", "v74", "v75", "v76", "v77", "v78", "v79", "v80", "v81", "v82", "v83", "v84", "v85", "v86", "v87", "v88", "v89", "v90", "v91",
"v92", "v93", "v94", "v95", "s40", "s41", "s42", "s43", "s44", "s45", "vcc", "scc", "memory");
}

__device__ __forceinline__ void role_o(char* ws, const void* xin, const void* b2, void* out, int xg, int r) {
    int tid = threadIdx.x; asm volatile("" : "+v"(tid));
    const int w = rfl(tid >> 6), l = tid & 63, g = l >> 4, c = l & 15, kq = r / 9, J = 2 * (r % 9);
    const v4i sWS = make_srd(ws, WS_B);
    const v4i sAD = make_srd(w == 3 ? (const char*)xin : ws, rfl(w == 3 ? 2359296u : WS_B)), sB2 = make_srd(b2, 4096), sOUT = make_srd(out, 2359296);
    const unsigned vPW = (l & 7) * 128;
    const unsigned vX = (16 * kq + c) * 1152 + 64 * J + 16 * g, vOUT = w < 2 ? vX + 64 * w : OOB;
    const unsigned vAD = w == 3 ? vX : (unsigned)((J % 6) * 1024 + l * 16), vAD2 = vAD + (w == 3 ? 64 : 1024), vB2 = w == 3 ? (unsigned)((16 * kq + c) * 2) : OOB;
    const unsigned vOL = WS_OT + kq * 131072 + w * 32768 + l * 16, vPX = (16 * w + c) * 128;
    const unsigned vRW = 131072 + w * 4096 + l * 16, vRR = 131072 + (w & 1) * 2048 + l * 16, vL16 = l * 16, vL128 = l * 128;
    const int sW32K = w * 32768, par = wt_par(ws);
    asm volatile(
"s_nop 4\n"
"s_mov_b32 s40, 1067008\n"
"s_mov_b64 exec, -1\n"
"s_mov_b32 s41, 0\n"
"LO_wt:\n"
"buffer_load_dword v20, %[vPW], %[sWS], s40 offen sc1\n"
"s_add_u32 s41, s41, 1\n"
"s_cmp_gt_u32 s41, 60000\n"
"s_cbranch_scc1 LO_tmo\n"
"s_waitcnt vmcnt(0)\n"
"v_cmp_gt_i32 vcc, 0, v20\n"
"s_cbranch_vccnz LO_wt\n"
"LO_wt_ok:\n"
"s_mov_b64 exec, -1\n"
"s_add_u32 s40, %[sROT], 0\n"
"s_and_b32 s40, s40, 7\n"
"s_lshl_b32 s60, s40, 12\n"
"s_lshl_b32 s42, s40, 14\n"
"s_add_u32 s44, s42, %[sSRC]\n"
"s_add_u32 s42, s42, %[sSRC2]\n"
"s_xor_b32 s45, s44, 0\n"
"buffer_load_dwordx4 a[0:3], %[vL16], %[sWS], s45 offen sc1\n"
"s_xor_b32 s45, s42, 0\n"
"buffer_load_dwordx4 v[128:131], %[vL16], %[sWS], s45 offen sc1\n"
"s_waitcnt vmcnt(10)\n"
"s_xor_b32 s45, s44, 1024\n"
"buffer_load_dwordx4 a[4:7], %[vL16], %[sWS], s45 offen sc1\n"
"s_xor_b32 s45, s42, 1024\n"
"s_add_u32 m0, %[sW32K], 0\n"
"s_nop 0\n"
"buffer_load_dwordx4 %[vL16], %[sWS], s45 offen sc1 lds\n"
"s_waitcnt vmcnt(10)\n"
"s_xor_b32 s45, s44, 2048\n"
"buffer_load_dwordx4 a[8:11], %[vL16], %[sWS], s45 offen sc1\n"
"s_xor_b32 s45, s42, 2048\n"
"buffer_load_dwordx4 v[132:135], %[vL16], %[sWS], s45 offen sc1\n"
"s_waitcnt vmcnt(10)\n"
"s_xor_b32 s45, s44, 3072\n"
"buffer_load_dwordx4 a[12:15], %[vL16], %[sWS], s45 offen sc1\n"
"s_xor_b32 s45, s42, 3072\n"
"s_add_u32 m0, %[sW32K], 1024\n"
"s_nop 0\n"
"buffer_load_dwordx4 %[vL16], %[sWS], s45 offen sc1 lds\n"
"s_waitcnt vmcnt(10)\n"
"s_xor_b32 s45, s44, 4096\n"
"buffer_load_dwordx4 a[16:19], %[vL16], %[sWS], s45 offen sc1\n"
"s_xor_b32 s45, s42, 4096\n"
"buffer_load_dwordx4 v[136:139], %[vL16], %[sWS], s45 offen sc1\n"
"s_waitcnt vmcnt(10)\n"
"s_xor_b32 s45, s44, 5120\n"
"buffer_load_dwordx4 a[20:23], %[vL16], %[sWS], s45 offen sc1\n"
"s_xor_b32 s45, s42, 5120\n"
"s_add_u32 m0, %[sW32K], 2048\n"
"s_nop 0\n"
"buffer_load_dwordx4 %[vL16], %[sWS], s45 offen sc1 lds\n"
"s_waitcnt vmcnt(10)\n"
"s_xor_b32 s45, s44, 6144\n"
"buffer_load_dwordx4 a[24:27], %[vL16], %[sWS], s45 offen sc1\n"
"s_xor_b32 s45, s42, 6144\n"
"buffer_load_dwordx4 v[140:143], %[vL16], %[sWS], s45 offen sc1\n"
"s_waitcnt vmcnt(10)\n"
"s_xor_b32 s45, s44, 7168\n"
"buffer_load_dwordx4 a[28:31], %[vL16], %[sWS], s45 offen sc1\n"
"s_xor_b32 s45, s42, 7168\n"
"s_add_u32 m0, %[sW32K], 3072\n"
"s_nop 0\n"
"buffer_load_dwordx4 %[vL16], %[sWS], s45 offen sc1 lds\n"
"s_waitcnt vmcnt(10)\n"
"s_add_u32 s40, %[sROT], 1\n"
"s_and_b32 s40, s40, 7\n"
"s_lshl_b32 s61, s40, 12\n"
"s_lshl_b32 s42, s40, 14\n"
"s_add_u32 s44, s42, %[sSRC]\n"
"s_add_u32 s42, s42, %[sSRC2]\n"
"s_xor_b32 s45, s44, 0\n"
"buffer_load_dwordx4 a[32:35], %[vL16], %[sWS], s45 offen sc1\n"
"s_xor_b32 s45, s42, 0\n"
"buffer_load_dwordx4 v[144:147], %[vL16], %[sWS], s45 offen sc1\n"
"s_waitcnt vmcnt(10)\n"
"s_xor_b32 s45, s44, 1024\n"
"buffer_load_dwordx4 a[36:39], %[vL16], %[sWS], s45 offen sc1\n"
"s_xor_b32 s45, s42, 1024\n"
"s_add_u32 m0, %[sW32K], 4096\n"
"s_nop 0\n"
"buffer_load_dwordx4 %[vL16], %[sWS], s45 offen sc1 lds\n"
"s_waitcnt vmcnt(10)\n"
"s_xor_b32 s45, s44, 2048\n"
"buffer_load_dwordx4 a[40:43], %[vL16], %[sWS], s45 offen sc1\n"
"s_xor_b32 s45, s42, 2048\n"
"buffer_load_dwordx4 v[148:151], %[vL16], %[sWS], s45 offen sc1\n"
"s_waitcnt vmcnt(10)\n"
"s_xor_b32 s45, s44, 3072\n"
"buffer_load_dwordx4 a[44:47], %[vL16], %[sWS], s45 offen sc1\n"
"s_xor_b32 s45, s42, 3072\n"
"s_add_u32 m0, %[sW32K], 5120\n"
"s_nop 0\n"
"buffer_load_dwordx4 %[vL16], %[sWS], s45 offen sc1 lds\n"
"s_waitcnt vmcnt(10)\n"
"s_xor_b32 s45, s44, 4096\n"
"buffer_load_dwordx4 a[48:51], %[vL16], %[sWS], s45 offen sc1\n"
"s_xor_b32 s45, s42, 4096\n"
"buffer_load_dwordx4 v[152:155], %[vL16], %[sWS], s45 offen sc1\n"
"s_waitcnt vmcnt(10)\n"
"s_xor_b32 s45, s44, 5120\n"
"buffer_load_dwordx4 a[52:55], %[vL16], %[sWS], s45 offen sc1\n"
"s_xor_b32 s45, s42, 5120\n"
"s_add_u32 m0, %[sW32K], 6144\n"
"s_nop 0\n"
"buffer_load_dwordx4 %[vL16], %[sWS], s45 offen sc1 lds\n"
"s_waitcnt vmcnt(10)\n"
"s_xor_b32 s45, s44, 6144\n"
"buffer_load_dwordx4 a[56:59], %[vL16], %[sWS], s45 offen sc1\n"
"s_xor_b32 s45, s42, 6144\n"
"buffer_load_dwordx4 v[156:159], %[vL16], %[sWS], s45 offen sc1\n"
"s_waitcnt vmcnt(10)\n"
"s_xor_b32 s45, s44, 7168\n"
"buffer_load_dwordx4 a[60:63], %[vL16], %[sWS], s45 offen sc1\n"
"s_xor_b32 s45, s42, 7168\n"
"s_add_u32 m0, %[sW32K], 7168\n"
"s_nop 0\n"
"buffer_load_dwordx4 %[vL16], %[sWS], s45 offen sc1 lds\n"
"s_waitcnt vmcnt(10)\n"
"s_add_u32 s40, %[sROT], 2\n"
"s_and_b32 s40, s40, 7\n"
"s_lshl_b32 s62, s40, 12\n"
"s_lshl_b32 s42, s40, 14\n"
"s_add_u32 s44, s42, %[sSRC]\n"
"s_add_u32 s42, s42, %[sSRC2]\n"
"s_xor_b32 s45, s44, 0\n"
"buffer_load_dwordx4 a[64:67], %[vL16], %[sWS], s45 offen sc1\n"
"s_xor_b32 s45, s42, 0\n"
"buffer_load_dwordx4 v[160:163], %[vL16], %[sWS], s45 offen sc1\n"
"s_waitcnt vmcnt(10)\n"
"s_xor_b32 s45, s44, 1024\n"
"buffer_load_dwordx4 a[68:71], %[vL16], %[sWS], s45 offen sc1\n"
"s_xor_b32 s45, s42, 1024\n"
"s_add_u32 m0, %[sW32K], 8192\n"
"s_nop 0\n"
"buffer_load_dwordx4 %[vL16], %[sWS], s45 offen sc1 lds\n"
"s_waitcnt vmcnt(10)\n"
"s_xor_b32 s45, s44, 2048\n"
"buffer_load_dwordx4 a[72:75], %[vL16], %[sWS], s45 offen sc1\n"
"s_xor_b32 s45, s42, 2048\n"
"buffer_load_dwordx4 v[164:167], %[vL16], %[sWS], s45 offen sc1\n"
"s_waitcnt vmcnt(10)\n"
"s_xor_b32 s45, s44, 3072\n"
"buffer_load_dwordx4 a[76:79], %[vL16], %[sWS], s45 offen sc1\n"
"s_xor_b32 s45, s42, 3072\n"
"s_add_u32 m0, %[sW32K], 9216\n"
"s_nop 0\n"
"buffer_load_dwordx4 %[vL16], %[sWS], s45 offen sc1 lds\n"
"s_waitcnt vmcnt(10)\n"
"s_xor_b32 s45, s44, 4096\n"
"buffer_load_dwordx4 a[80:83], %[vL16], %[sWS], s45 offen sc1\n"
"s_xor_b32 s45, s42, 4096\n"
"buffer_load_dwordx4 v[168:171], %[vL16], %[sWS], s45 offen sc1\n"
"s_waitcnt vmcnt(10)\n"
"s_xor_b32 s45, s44, 5120\n"
"buffer_load_dwordx4 a[84:87], %[vL16], %[sWS], s45 offen sc1\n"
"s_xor_b32 s45, s42, 5120\n"
"s_add_u32 m0, %[sW32K], 10240\n"
"s_nop 0\n"
"buffer_load_dwordx4 %[vL16], %[sWS], s45 offen sc1 lds\n"
"s_waitcnt vmcnt(10)\n"
"s_xor_b32 s45, s44, 6144\n"
"buffer_load_dwordx4 a[88:91], %[vL16], %[sWS], s45 offen sc1\n"
"s_xor_b32 s45, s42, 6144\n"
"buffer_load_dwordx4 v[172:175], %[vL16], %[sWS], s45 offen sc1\n"
"s_waitcnt vmcnt(10)\n"
"s_xor_b32 s45, s44, 7168\n"
"buffer_load_dwordx4 a[92:95], %[vL16], %[sWS], s45 offen sc1\n"
"s_xor_b32 s45, s42, 7168\n"
"s_add_u32 m0, %[sW32K], 11264\n"
"s_nop 0\n"
"buffer_load_dwordx4 %[vL16], %[sWS], s45 offen sc1 lds\n"
"s_waitcnt vmcnt(10)\n"
"s_add_u32 s40, %[sROT], 3\n"
"s_and_b32 s40, s40, 7\n"
"s_lshl_b32 s63, s40, 12\n"
"s_lshl_b32 s42, s40, 14\n"
"s_add_u32 s44, s42, %[sSRC]\n"
"s_add_u32 s42, s42, %[sSRC2]\n"
"s_xor_b32 s45, s44, 0\n"
"buffer_load_dwordx4 a[96:99], %[vL16], %[sWS], s45 offen sc1\n"
"s_xor_b32 s45, s42, 0\n"
"buffer_load_dwordx4 v[176:179], %[vL16], %[sWS], s45 offen sc1\n"
"s_waitcnt vmcnt(10)\n"
"s_xor_b32 s45, s44, 1024\n"
"buffer_load_dwordx4 a[100:103], %[vL16], %[sWS], s45 offen sc1\n"
"s_xor_b32 s45, s42, 1024\n"
"s_add_u32 m0, %[sW32K], 12288\n"
"s_nop 0\n"
"buffer_load_dwordx4 %[vL16], %[sWS], s45 offen sc1 lds\n"
"s_waitcnt vmcnt(10)\n"
"s_xor_b32 s45, s44, 2048\n"
"buffer_load_dwordx4 a[104:107], %[vL16], %[sWS], s45 offen sc1\n"
"s_xor_b32 s45, s42, 2048\n"
"buffer_load_dwordx4 v[180:183], %[vL16], %[sWS], s45 offen sc1\n"
"s_waitcnt vmcnt(10)\n"
"s_xor_b32 s45, s44, 3072\n"
"buffer_load_dwordx4 a[108:111], %[vL16], %[sWS], s45 offen sc1\n"
"s_xor_b32 s45, s42, 3072\n"
"s_add_u32 m0, %[sW32K], 13312\n"
"s_nop 0\n"
"buffer_load_dwordx4 %[vL16], %[sWS], s45 offen sc1 lds\n"
"s_waitcnt vmcnt(10)\n"
"s_xor_b32 s45, s44, 4096\n"
"buffer_load_dwordx4 a[112:115], %[vL16], %[sWS], s45 offen sc1\n"
"s_xor_b32 s45, s42, 4096\n"
"buffer_load_dwordx4 v[184:187], %[vL16], %[sWS], s45 offen sc1\n"
"s_waitcnt vmcnt(10)\n"
"s_xor_b32 s45, s44, 5120\n"
"buffer_load_dwordx4 a[116:119], %[vL16], %[sWS], s45 offen sc1\n"
"s_xor_b32 s45, s42, 5120\n"
"s_add_u32 m0, %[sW32K], 14336\n"
"s_nop 0\n"
"buffer_load_dwordx4 %[vL16], %[sWS], s45 offen sc1 lds\n"
"s_waitcnt vmcnt(10)\n"
"s_xor_b32 s45, s44, 6144\n"
"buffer_load_dwordx4 a[120:123], %[vL16], %[sWS], s45 offen sc1\n"
"s_xor_b32 s45, s42, 6144\n"
"buffer_load_dwordx4 v[188:191], %[vL16], %[sWS], s45 offen sc1\n"
"s_waitcnt vmcnt(10)\n"
"s_xor_b32 s45, s44, 7168\n"
"buffer_load_dwordx4 a[124:127], %[vL16], %[sWS], s45 offen sc1\n"
"s_xor_b32 s45, s42, 7168\n"
"s_add_u32 m0, %[sW32K], 15360\n"
"s_nop 0\n"
"buffer_load_dwordx4 %[vL16], %[sWS], s45 offen sc1 lds\n"
"s_waitcnt vmcnt(10)\n"
"s_add_u32 s40, %[sROT], 4\n"
"s_and_b32 s40, s40, 7\n"
"s_lshl_b32 s64, s40, 12\n"
"s_lshl_b32 s42, s40, 14\n"
"s_add_u32 s44, s42, %[sSRC]\n"
"s_add_u32 s42, s42, %[sSRC2]\n"
"s_xor_b32 s45, s44, 0\n"
"buffer_load_dwordx4 a[128:131], %[vL16], %[sWS], s45 offen sc1\n"
"s_xor_b32 s45, s42, 0\n"
"buffer_load_dwordx4 v[192:195], %[vL16], %[sWS], s45 offen sc1\n"
"s_waitcnt vmcnt(10)\n"
"s_xor_b32 s45, s44, 1024\n"
"buffer_load_dwordx4 a[132:135], %[vL16], %[sWS], s45 offen sc1\n"
"s_xor_b32 s45, s42, 1024\n"
"s_add_u32 m0, %[sW32K], 16384\n"
"s_nop 0\n"
"buffer_load_dwordx4 %[vL16], %[sWS], s45 offen sc1 lds\n"
"s_waitcnt vmcnt(10)\n"
"s_xor_b32 s45, s44, 2048\n"
"buffer_load_dwordx4 a[136:139], %[vL16], %[sWS], s45 offen sc1\n"
"s_xor_b32 s45, s42, 2048\n"
"buffer_load_dwordx4 v[196:199], %[vL16], %[sWS], s45 offen sc1\n"
"s_waitcnt vmcnt(10)\n"
"s_xor_b32 s45, s44, 3072\n"
"buffer_load_dwordx4 a[140:143], %[vL16], %[sWS], s45 offen sc1\n"
"s_xor_b32 s45, s42, 3072\n"
"s_add_u32 m0, %[sW32K], 17408\n"
"s_nop 0\n"
"buffer_load_dwordx4 %[vL16], %[sWS], s45 offen sc1 lds\n"
"s_waitcnt vmcnt(10)\n"
"s_xor_b32 s45, s44, 4096\n"
"buffer_load_dwordx4 a[144:147], %[vL16], %[sWS], s45 offen sc1\n"
"s_xor_b32 s45, s42, 4096\n"
"buffer_load_dwordx4 v[200:203], %[vL16], %[sWS], s45 offen sc1\n"
"s_waitcnt vmcnt(10)\n"
"s_xor_b32 s45, s44, 5120\n"
"buffer_load_dwordx4 a[148:151], %[vL16], %[sWS], s45 offen sc1\n"
"s_xor_b32 s45, s42, 5120\n"
"s_add_u32 m0, %[sW32K], 18432\n"
"s_nop 0\n"
"buffer_load_dwordx4 %[vL16], %[sWS], s45 offen sc1 lds\n"
"s_waitcnt vmcnt(10)\n"
"s_xor_b32 s45, s44, 6144\n"
"buffer_load_dwordx4 a[152:155], %[vL16], %[sWS], s45 offen sc1\n"
"s_xor_b32 s45, s42, 6144\n"
"buffer_load_dwordx4 v[204:207], %[vL16], %[sWS], s45 offen sc1\n"
"s_waitcnt vmcnt(10)\n"
"s_xor_b32 s45, s44, 7168\n"
"buffer_load_dwordx4 a[156:159], %[vL16], %[sWS], s45 offen sc1\n"
"s_xor_b32 s45, s42, 7168\n"
"s_add_u32 m0, %[sW32K], 19456\n"
"s_nop 0\n"
"buffer_load_dwordx4 %[vL16], %[sWS], s45 offen sc1 lds\n"
"s_waitcnt vmcnt(10)\n"
"s_add_u32 s40, %[sROT], 5\n"
"s_and_b32 s40, s40, 7\n"
"s_lshl_b32 s65, s40, 12\n"
"s_lshl_b32 s42, s40, 14\n"
"s_add_u32 s44, s42, %[sSRC]\n"
"s_add_u32 s42, s42, %[sSRC2]\n"
"s_xor_b32 s45, s44, 0\n"
"buffer_load_dwordx4 a[160:163], %[vL16], %[sWS], s45 offen sc1\n"
"s_xor_b32 s45, s42, 0\n"
"buffer_load_dwordx4 v[208:211], %[vL16], %[sWS], s45 offen sc1\n"
"s_waitcnt vmcnt(10)\n"
"s_xor_b32 s45, s44, 1024\n"
"buffer_load_dwordx4 a[164:167], %[vL16], %[sWS], s45 offen sc1\n"
"s_xor_b32 s45, s42, 1024\n"
"s_add_u32 m0, %[sW32K], 20480\n"
"s_nop 0\n"
"buffer_load_dwordx4 %[vL16], %[sWS], s45 offen sc1 lds\n"
"s_waitcnt vmcnt(10)\n"
"s_xor_b32 s45, s44, 2048\n"
"buffer_load_dwordx4 a[168:171], %[vL16], %[sWS], s45 offen sc1\n"
"s_xor_b32 s45, s42, 2048\n"
"buffer_load_dwordx4 v[212:215], %[vL16], %[sWS], s45 offen sc1\n"
"s_waitcnt vmcnt(10)\n"
"s_xor_b32 s45, s44, 3072\n"
"buffer_load_dwordx4 a[172:175], %[vL16], %[sWS], s45 offen sc1\n"
"s_xor_b32 s45, s42, 3072\n"
"s_add_u32 m0, %[sW32K], 21504\n"
"s_nop 0\n"
"buffer_load_dwordx4 %[vL16], %[sWS], s45 offen sc1 lds\n"
"s_waitcnt vmcnt(10)\n"
"s_xor_b32 s45, s44, 4096\n"
"buffer_load_dwordx4 a[176:179], %[vL16], %[sWS], s45 offen sc1\n"
"s_xor_b32 s45, s42, 4096\n"
"buffer_load_dwordx4 v[216:219], %[vL16], %[sWS], s45 offen sc1\n"
"s_waitcnt vmcnt(10)\n"
"s_xor_b32 s45, s44, 5120\n"
"buffer_load_dwordx4 a[180:183], %[vL16], %[sWS], s45 offen sc1\n"
"s_xor_b32 s45, s42, 5120\n"
"s_add_u32 m0, %[sW32K], 22528\n"
"s_nop 0\n"
"buffer_load_dwordx4 %[vL16], %[sWS], s45 offen sc1 lds\n"
"s_waitcnt vmcnt(10)\n"
"s_xor_b32 s45, s44, 6144\n"
"buffer_load_dwordx4 a[184:187], %[vL16], %[sWS], s45 offen sc1\n"
"s_xor_b32 s45, s42, 6144\n"
"buffer_load_dwordx4 v[220:223], %[vL16], %[sWS], s45 offen sc1\n"
"s_waitcnt vmcnt(10)\n"
"s_xor_b32 s45, s44, 7168\n"
"buffer_load_dwordx4 a[188:191], %[vL16], %[sWS], s45 offen sc1\n"
"s_xor_b32 s45, s42, 7168\n"
"s_add_u32 m0, %[sW32K], 23552\n"
"s_nop 0\n"
"buffer_load_dwordx4 %[vL16], %[sWS], s45 offen sc1 lds\n"
"s_waitcnt vmcnt(10)\n"
"s_add_u32 s40, %[sROT], 6\n"
"s_and_b32 s40, s40, 7\n"
"s_lshl_b32 s66, s40, 12\n"
"s_lshl_b32 s42, s40, 14\n"
"s_add_u32 s44, s42, %[sSRC]\n"
"s_add_u32 s42, s42, %[sSRC2]\n"
"s_xor_b32 s45, s44, 0\n"
"buffer_load_dwordx4 a[192:195], %[vL16], %[sWS], s45 offen sc1\n"
"s_xor_b32 s45, s42, 0\n"
"buffer_load_dwordx4 v[224:227], %[vL16], %[sWS], s45 offen sc1\n"
"s_waitcnt vmcnt(10)\n"
"s_xor_b32 s45, s44, 1024\n"
"buffer_load_dwordx4 a[196:199], %[vL16], %[sWS], s45 offen sc1\n"
"s_xor_b32 s45, s42, 1024\n"
"s_add_u32 m0, %[sW32K], 24576\n"
"s_nop 0\n"
"buffer_load_dwordx4 %[vL16], %[sWS], s45 offen sc1 lds\n"
"s_waitcnt vmcnt(10)\n"
"s_xor_b32 s45, s44, 2048\n"
"buffer_load_dwordx4 a[200:203], %[vL16], %[sWS], s45 offen sc1\n"
"s_xor_b32 s45, s42, 2048\n"
"buffer_load_dwordx4 v[228:231], %[vL16], %[sWS], s45 offen sc1\n"
"s_waitcnt vmcnt(10)\n"
"s_xor_b32 s45, s44, 3072\n"
"buffer_load_dwordx4 a[204:207], %[vL16], %[sWS], s45 offen sc1\n"
"s_xor_b32 s45, s42, 3072\n"
"s_add_u32 m0, %[sW32K], 25600\n"
"s_nop 0\n"
"buffer_load_dwordx4 %[vL16], %[sWS], s45 offen sc1 lds\n"
"s_waitcnt vmcnt(10)\n"
"s_xor_b32 s45, s44, 4096\n"
"buffer_load_dwordx4 a[208:211], %[vL16], %[sWS], s45 offen sc1\n"
"s_xor_b32 s45, s42, 4096\n"
"buffer_load_dwordx4 v[232:235], %[vL16], %[sWS], s45 offen sc1\n"
"s_waitcnt vmcnt(10)\n"
"s_xor_b32 s45, s44, 5120\n"
"buffer_load_dwordx4 a[212:215], %[vL16], %[sWS], s45 offen sc1\n"
"s_xor_b32 s45, s42, 5120\n"
"s_add_u32 m0, %[sW32K], 26624\n"
"s_nop 0\n"
"buffer_load_dwordx4 %[vL16], %[sWS], s45 offen sc1 lds\n"
"s_waitcnt vmcnt(10)\n"
"s_xor_b32 s45, s44, 6144\n"
"buffer_load_dwordx4 a[216:219], %[vL16], %[sWS], s45 offen sc1\n"
"s_xor_b32 s45, s42, 6144\n"
"buffer_load_dwordx4 v[236:239], %[vL16], %[sWS], s45 offen sc1\n"
"s_waitcnt vmcnt(10)\n"
"s_xor_b32 s45, s44, 7168\n"
"buffer_load_dwordx4 a[220:223], %[vL16], %[sWS], s45 offen sc1\n"
"s_xor_b32 s45, s42, 7168\n"
"s_add_u32 m0, %[sW32K], 27648\n"
"s_nop 0\n"
"buffer_load_dwordx4 %[vL16], %[sWS], s45 offen sc1 lds\n"
"s_waitcnt vmcnt(10)\n"
"s_add_u32 s40, %[sROT], 7\n"
"s_and_b32 s40, s40, 7\n"
"s_lshl_b32 s67, s40, 12\n"
"s_lshl_b32 s42, s40, 14\n"
"s_add_u32 s44, s42, %[sSRC]\n"
"s_add_u32 s42, s42, %[sSRC2]\n"
"s_xor_b32 s45, s44, 0\n"
"buffer_load_dwordx4 a[224:227], %[vL16], %[sWS], s45 offen sc1\n"
"s_xor_b32 s45, s42, 0\n"
"buffer_load_dwordx4 v[240:243], %[vL16], %[sWS], s45 offen sc1\n"
"s_waitcnt vmcnt(10)\n"
"s_xor_b32 s45, s44, 1024\n"
"buffer_load_dwordx4 a[228:231], %[vL16], %[sWS], s45 offen sc1\n"
"s_xor_b32 s45, s42, 1024\n"
"s_add_u32 m0, %[sW32K], 28672\n"
"s_nop 0\n"
"buffer_load_dwordx4 %[vL16], %[sWS], s45 offen sc1 lds\n"
"s_waitcnt vmcnt(10)\n"
"s_xor_b32 s45, s44, 2048\n"
"buffer_load_dwordx4 a[232:235], %[vL16], %[sWS], s45 offen sc1\n"
"s_xor_b32 s45, s42, 2048\n"
"buffer_load_dwordx4 v[244:247], %[vL16], %[sWS], s45 offen sc1\n"
"s_waitcnt vmcnt(10)\n"
"s_xor_b32 s45, s44, 3072\n"
"buffer_load_dwordx4 a[236:239], %[vL16], %[sWS], s45 offen sc1\n"
"s_xor_b32 s45, s42, 3072\n"
"s_add_u32 m0, %[sW32K], 29696\n"
"s_nop 0\n"
"buffer_load_dwordx4 %[vL16], %[sWS], s45 offen sc1 lds\n"
"s_waitcnt vmcnt(10)\n"
"s_xor_b32 s45, s44, 4096\n"
"buffer_load_dwordx4 a[240:243], %[vL16], %[sWS], s45 offen sc1\n"
"s_xor_b32 s45, s42, 4096\n"
"buffer_load_dwordx4 v[248:251], %[vL16], %[sWS], s45 offen sc1\n"
"s_waitcnt vmcnt(10)\n"
"s_xor_b32 s45, s44, 5120\n"
"buffer_load_dwordx4 a[244:247], %[vL16], %[sWS], s45 offen sc1\n"
"s_xor_b32 s45, s42, 5120\n"
"s_add_u32 m0, %[sW32K], 30720\n"
"s_nop 0\n"
"buffer_load_dwordx4 %[vL16], %[sWS], s45 offen sc1 lds\n"
"s_waitcnt vmcnt(10)\n"
"s_xor_b32 s45, s44, 6144\n"
"buffer_load_dwordx4 a[248:251], %[vL16], %[sWS], s45 offen sc1\n"
"s_xor_b32 s45, s42, 6144\n"
"buffer_load_dwordx4 v[252:255], %[vL16], %[sWS], s45 offen sc1\n"
"s_waitcnt vmcnt(10)\n"
"s_xor_b32 s45, s44, 7168\n"
"buffer_load_dwordx4 a[252:255], %[vL16], %[sWS], s45 offen sc1\n"
"s_xor_b32 s45, s42, 7168\n"
"s_add_u32 m0, %[sW32K], 31744\n"
"s_nop 0\n"
"buffer_load_dwordx4 %[vL16], %[sWS], s45 offen sc1 lds\n"
"s_waitcnt vmcnt(10)\n"
"s_mov_b32 s40, 1134592\n"
"s_mov_b64 exec, -1\n"
"s_mov_b32 s41, 0\n"
"LO_b2a:\n"
"buffer_load_dword v20, %[vL128], %[sWS], s40 offen sc1\n"
"s_add_u32 s41, s41, 1\n"
"s_cmp_gt_u32 s41, 60000\n"
"s_cbranch_scc1 LO_tmo\n"
"s_waitcnt vmcnt(0)\n"
"v_cmp_gt_i32 vcc, 0, v20\n"
"s_cbranch_vccnz LO_b2a\n"
"LO_b2a_ok:\n"
"s_mov_b64 exec, -1\n"
"s_mov_b32 s40, 1142784\n"
"s_mov_b64 exec, 0xff\n"
"s_mov_b32 s41, 0\n"
"LO_b2b:\n"
"buffer_load_dword v20, %[vL128], %[sWS], s40 offen sc1\n"
"s_add_u32 s41, s41, 1\n"
"s_cmp_gt_u32 s41, 60000\n"
"s_cbranch_scc1 LO_tmo\n"
"s_waitcnt vmcnt(0)\n"
"v_cmp_gt_i32 vcc, 0, v20\n"
"s_cbranch_vccnz LO_b2b\n"
"LO_b2b_ok:\n"
"s_mov_b64 exec, -1\n"
"s_mov_b32 s49, 0\n"
"s_mov_b32 s43, %[sSEG]\n"
"s_mov_b32 s47, 1097728\n"
"v_add_u32 v31, %[sW32K], %[vL16]\n"
"LO_task:\n"
"s_add_u32 s51, s43, 1\n"
"s_mov_b64 exec, 0xffff\n"
"s_mov_b32 s41, 0\n"
"s_branch LO_xo_go\n"
"LO_xo:\n"
"s_sleep 2\n"
"LO_xo_go:\n"
"buffer_load_dword v20, %[vPX], %[sWS], s47 offen sc1\n"
"s_add_u32 s41, s41, 1\n"
"s_cmp_gt_u32 s41, 60000\n"
"s_cbranch_scc1 LO_tmo\n"
"s_waitcnt vmcnt(0)\n"
"v_cmp_gt_i32 vcc, s51, v20\n"
"s_cbranch_vccnz LO_xo\n"
"LO_xo_ok:\n"
"s_mov_b64 exec, -1\n"
"s_mul_i32 s48, s43, 36864\n"
"s_lshl_b32 s50, s43, 6\n"
"s_mul_i32 s52, s43, 98304\n"
"s_add_u32 s52, s52, %[sYB]\n"
"s_lshr_b32 s53, s52, 21\n"
"s_lshr_b32 s53, %[sFAR], s53\n"
"s_and_b32 s53, s53, 1\n"
"s_lshl_b32 s53, s53, 13\n"
"s_add_u32 s53, s53, s52\n"
"s_cmp_eq_u32 %[sW32K], 98304\n"
"s_cselect_b32 s53, s48, s53\n"
"buffer_load_dwordx4 v[20:23], %[vAD], %[sAD], s53 offen sc1\n"
"buffer_load_dwordx4 v[24:27], %[vAD2], %[sAD], s53 offen sc1\n"
"buffer_load_ushort v28, %[vB2], %[sB2], s50 offen\n"
"buffer_load_dword v29, %[vPX], %[sWS], s47 offen sc1\n"
"s_lshl_b32 s46, s43, 18\n"
"v_add_u32 v30, s46, %[vOL]\n"
"buffer_load_dwordx4 v[64:67], v30, %[sWS], s60 offen offset:0 sc1\n"
"s_nop 3\n"
"buffer_load_dwordx4 v[68:71], v30, %[sWS], s60 offen offset:1024 sc1\n"
"s_nop 3\n"
"buffer_load_dwordx4 v[72:75], v30, %[sWS], s60 offen offset:2048 sc1\n"
"s_nop 3\n"
"buffer_load_dwordx4 v[76:79], v30, %[sWS], s60 offen offset:3072 sc1\n"
"s_nop 3\n"
"buffer_load_dwordx4 v[80:83], v30, %[sWS], s61 offen offset:0 sc1\n"
"s_nop 3\n"
"buffer_load_dwordx4 v[84:87], v30, %[sWS], s61 offen offset:1024 sc1\n"
"s_nop 3\n"
"buffer_load_dwordx4 v[88:91], v30, %[sWS], s61 offen offset:2048 sc1\n"
"s_nop 3\n"
"buffer_load_dwordx4 v[92:95], v30, %[sWS], s61 offen offset:3072 sc1\n"
"s_nop 3\n"
"buffer_load_dwordx4 v[96:99], v30, %[sWS], s62 offen offset:0 sc1\n"
"s_nop 3\n"
"buffer_load_dwordx4 v[100:103], v30, %[sWS], s62 offen offset:1024 sc1\n"
"s_nop 3\n"
"buffer_load_dwordx4 v[104:107], v30, %[sWS], s62 offen offset:2048 sc1\n"
"s_nop 3\n"
"buffer_load_dwordx4 v[108:111], v30, %[sWS], s62 offen offset:3072 sc1\n"
"s_nop 3\n"
"buffer_load_dwordx4 v[112:115], v30, %[sWS], s63 offen offset:0 sc1\n"
"s_nop 3\n"
"buffer_load_dwordx4 v[116:119], v30, %[sWS], s63 offen offset:1024 sc1\n"
"s_nop 3\n"
"buffer_load_dwordx4 v[120:123], v30, %[sWS], s63 offen offset:2048 sc1\n"
"s_nop 3\n"
"buffer_load_dwordx4 v[124:127], v30, %[sWS], s63 offen offset:3072 sc1\n"
"s_nop 3\n"
"s_waitcnt vmcnt(16)\n"
"LO_run:\n"
"s_mov_b32 s45, s48\n"
"s_add_u32 s44, s43, 4\n"
"s_add_u32 s51, s44, 1\n"
"v_cmp_gt_i32 vcc, s51, v29\n"
"ds_read_b128 v[48:51], v31 offset:0\n"
"ds_read_b128 v[52:55], v31 offset:1024\n"
"ds_read_b128 v[56:59], v31 offset:2048\n"
"v_lshlrev_b32 v28, 16, v28\n"
"v_lshlrev_b32 v32, 16, v20\n"
"v_and_b32 v33, 0xffff0000, v20\n"
"v_lshlrev_b32 v34, 16, v21\n"
"v_and_b32 v35, 0xffff0000, v21\n"
"v_lshlrev_b32 v36, 16, v22\n"
"v_and_b32 v37, 0xffff0000, v22\n"
"v_lshlrev_b32 v38, 16, v23\n"
"v_and_b32 v39, 0xffff0000, v23\n"
"v_lshlrev_b32 v40, 16, v24\n"
"v_and_b32 v41, 0xffff0000, v24\n"
"v_lshlrev_b32 v42, 16, v25\n"
"v_and_b32 v43, 0xffff0000, v25\n"
"v_lshlrev_b32 v44, 16, v26\n"
"v_and_b32 v45, 0xffff0000, v26\n"
"v_lshlrev_b32 v46, 16, v27\n"
"v_and_b32 v47, 0xffff0000, v27\n"
"v_pk_add_f32 v[32:33], v[32:33], v[28:29] op_sel_hi:[1,0]\n"
"v_pk_add_f32 v[34:35], v[34:35], v[28:29] op_sel_hi:[1,0]\n"
"v_pk_add_f32 v[36:37], v[36:37], v[28:29] op_sel_hi:[1,0]\n"
"v_pk_add_f32 v[38:39], v[38:39], v[28:29] op_sel_hi:[1,0]\n"
"v_pk_add_f32 v[40:41], v[40:41], v[28:29] op_sel_hi:[1,0]\n"
"v_pk_add_f32 v[42:43], v[42:43], v[28:29] op_sel_hi:[1,0]\n"
"v_pk_add_f32 v[44:45], v[44:45], v[28:29] op_sel_hi:[1,0]\n"
"v_pk_add_f32 v[46:47], v[46:47], v[28:29] op_sel_hi:[1,0]\n"
"s_cbranch_vccnz LO_last\n"
"s_mul_i32 s48, s44, 36864\n"
"s_lshl_b32 s50, s44, 6\n"
"s_mul_i32 s52, s44, 98304\n"
"s_add_u32 s52, s52, %[sYB]\n"
"s_lshr_b32 s53, s52, 21\n"
"s_lshr_b32 s53, %[sFAR], s53\n"
"s_and_b32 s53, s53, 1\n"
"s_lshl_b32 s53, s53, 13\n"
"s_add_u32 s53, s53, s52\n"
"s_cmp_eq_u32 %[sW32K], 98304\n"
"s_cselect_b32 s53, s48, s53\n"
"buffer_load_dwordx4 v[20:23], %[vAD], %[sAD], s53 offen sc1\n"
"buffer_load_dwordx4 v[24:27], %[vAD2], %[sAD], s53 offen sc1\n"
"buffer_load_ushort v28, %[vB2], %[sB2], s50 offen\n"
"buffer_load_dword v29, %[vPX], %[sWS], s47 offen sc1\n"
"s_waitcnt vmcnt(19) lgkmcnt(2)\n"
"v_mfma_f32_16x16x32_bf16 v[44:47], v[48:51], v[64:67], v[44:47]\n"
"v_mfma_f32_16x16x32_bf16 v[32:35], a[0:3], v[64:67], v[32:35]\n"
"ds_read_b128 v[60:63], v31 offset:3072\n"
"v_mfma_f32_16x16x32_bf16 v[36:39], a[4:7], v[64:67], v[36:39]\n"
"v_mfma_f32_16x16x32_bf16 v[40:43], v[128:131], v[64:67], v[40:43]\n"
"buffer_load_dwordx4 v[64:67], v30, %[sWS], s64 offen offset:0 sc1\n"
"s_waitcnt vmcnt(19) lgkmcnt(2)\n"
"v_mfma_f32_16x16x32_bf16 v[44:47], v[52:55], v[68:71], v[44:47]\n"
"v_mfma_f32_16x16x32_bf16 v[32:35], a[8:11], v[68:71], v[32:35]\n"
"ds_read_b128 v[48:51], v31 offset:4096\n"
"v_mfma_f32_16x16x32_bf16 v[36:39], a[12:15], v[68:71], v[36:39]\n"
"v_mfma_f32_16x16x32_bf16 v[40:43], v[132:135], v[68:71], v[40:43]\n"
"buffer_load_dwordx4 v[68:71], v30, %[sWS], s64 offen offset:1024 sc1\n"
"s_waitcnt vmcnt(19) lgkmcnt(2)\n"
"v_mfma_f32_16x16x32_bf16 v[44:47], v[56:59], v[72:75], v[44:47]\n"
"v_mfma_f32_16x16x32_bf16 v[32:35], a[16:19], v[72:75], v[32:35]\n"
"ds_read_b128 v[52:55], v31 offset:5120\n"
"v_mfma_f32_16x16x32_bf16 v[36:39], a[20:23], v[72:75], v[36:39]\n"
"v_mfma_f32_16x16x32_bf16 v[40:43], v[136:139], v[72:75], v[40:43]\n"
"buffer_load_dwordx4 v[72:75], v30, %[sWS], s64 offen offset:2048 sc1\n"
"s_waitcnt vmcnt(19) lgkmcnt(2)\n"
"v_mfma_f32_16x16x32_bf16 v[44:47], v[60:63], v[76:79], v[44:47]\n"
"v_mfma_f32_16x16x32_bf16 v[32:35], a[24:27], v[76:79], v[32:35]\n"
"ds_read_b128 v[56:59], v31 offset:6144\n"
"v_mfma_f32_16x16x32_bf16 v[36:39], a[28:31], v[76:79], v[36:39]\n"
"v_mfma_f32_16x16x32_bf16 v[40:43], v[140:143], v[76:79], v[40:43]\n"
"buffer_load_dwordx4 v[76:79], v30, %[sWS], s64 offen offset:3072 sc1\n"
"s_waitcnt vmcnt(19) lgkmcnt(2)\n"
"v_mfma_f32_16x16x32_bf16 v[44:47], v[48:51], v[80:83], v[44:47]\n"
"v_mfma_f32_16x16x32_bf16 v[32:35], a[32:35], v[80:83], v[32:35]\n"
"ds_read_b128 v[60:63], v31 offset:7168\n"
"v_mfma_f32_16x16x32_bf16 v[36:39], a[36:39], v[80:83], v[36:39]\n"
"v_mfma_f32_16x16x32_bf16 v[40:43], v[144:147], v[80:83], v[40:43]\n"
"buffer_load_dwordx4 v[80:83], v30, %[sWS], s65 offen offset:0 sc1\n"
"s_waitcnt vmcnt(19) lgkmcnt(2)\n"
"v_mfma_f32_16x16x32_bf16 v[44:47], v[52:55], v[84:87], v[44:47]\n"
"v_mfma_f32_16x16x32_bf16 v[32:35], a[40:43], v[84:87], v[32:35]\n"
"ds_read_b128 v[48:51], v31 offset:8192\n"
"v_mfma_f32_16x16x32_bf16 v[36:39], a[44:47], v[84:87], v[36:39]\n"
"v_mfma_f32_16x16x32_bf16 v[40:43], v[148:151], v[84:87], v[40:43]\n"
"buffer_load_dwordx4 v[84:87], v30, %[sWS], s65 offen offset:1024 sc1\n"
"s_waitcnt vmcnt(19) lgkmcnt(2)\n"
"v_mfma_f32_16x16x32_bf16 v[44:47], v[56:59], v[88:91], v[44:47]\n"
"v_mfma_f32_16x16x32_bf16 v[32:35], a[48:51], v[88:91], v[32:35]\n"
"ds_read_b128 v[52:55], v31 offset:9216\n"
"v_mfma_f32_16x16x32_bf16 v[36:39], a[52:55], v[88:91], v[36:39]\n"
"v_mfma_f32_16x16x32_bf16 v[40:43], v[152:155], v[88:91], v[40:43]\n"
"buffer_load_dwordx4 v[88:91], v30, %[sWS], s65 offen offset:2048 sc1\n"
"s_waitcnt vmcnt(19) lgkmcnt(2)\n"
"v_mfma_f32_16x16x32_bf16 v[44:47], v[60:63], v[92:95], v[44:47]\n"
"v_mfma_f32_16x16x32_bf16 v[32:35], a[56:59], v[92:95], v[32:35]\n"
"ds_read_b128 v[56:59], v31 offset:10240\n"
"v_mfma_f32_16x16x32_bf16 v[36:39], a[60:63], v[92:95], v[36:39]\n"
"v_mfma_f32_16x16x32_bf16 v[40:43], v[156:159], v[92:95], v[40:43]\n"
"buffer_load_dwordx4 v[92:95], v30, %[sWS], s65 offen offset:3072 sc1\n"
"s_waitcnt vmcnt(19) lgkmcnt(2)\n"
"v_mfma_f32_16x16x32_bf16 v[44:47], v[48:51], v[96:99], v[44:47]\n"
"v_mfma_f32_16x16x32_bf16 v[32:35], a[64:67], v[96:99], v[32:35]\n"
"ds_read_b128 v[60:63], v31 offset:11264\n"
"v_mfma_f32_16x16x32_bf16 v[36:39], a[68:71], v[96:99], v[36:39]\n"
"v_mfma_f32_16x16x32_bf16 v[40:43], v[160:163], v[96:99], v[40:43]\n"
"buffer_load_dwordx4 v[96:99], v30, %[sWS], s66 offen offset:0 sc1\n"
"s_waitcnt vmcnt(19) lgkmcnt(2)\n"
"v_mfma_f32_16x16x32_bf16 v[44:47], v[52:55], v[100:103], v[44:47]\n"
"v_mfma_f32_16x16x32_bf16 v[32:35], a[72:75], v[100:103], v[32:35]\n"
"ds_read_b128 v[48:51], v31 offset:12288\n"
"v_mfma_f32_16x16x32_bf16 v[36:39], a[76:79], v[100:103], v[36:39]\n"
"v_mfma_f32_16x16x32_bf16 v[40:43], v[164:167], v[100:103], v[40:43]\n"
"buffer_load_dwordx4 v[100:103], v30, %[sWS], s66 offen offset:1024 sc1\n"
"s_waitcnt vmcnt(19) lgkmcnt(2)\n"
"v_mfma_f32_16x16x32_bf16 v[44:47], v[56:59], v[104:107], v[44:47]\n"
"v_mfma_f32_16x16x32_bf16 v[32:35], a[80:83], v[104:107], v[32:35]\n"
"ds_read_b128 v[52:55], v31 offset:13312\n"
"v_mfma_f32_16x16x32_bf16 v[36:39], a[84:87], v[104:107], v[36:39]\n"
"v_mfma_f32_16x16x32_bf16 v[40:43], v[168:171], v[104:107], v[40:43]\n"
"buffer_load_dwordx4 v[104:107], v30, %[sWS], s66 offen offset:2048 sc1\n"
"s_waitcnt vmcnt(19) lgkmcnt(2)\n"
"v_mfma_f32_16x16x32_bf16 v[44:47], v[60:63], v[108:111], v[44:47]\n"
"v_mfma_f32_16x16x32_bf16 v[32:35], a[88:91], v[108:111], v[32:35]\n"
"ds_read_b128 v[56:59], v31 offset:14336\n"
"v_mfma_f32_16x16x32_bf16 v[36:39], a[92:95], v[108:111], v[36:39]\n"
"v_mfma_f32_16x16x32_bf16 v[40:43], v[172:175], v[108:111], v[40:43]\n"
"buffer_load_dwordx4 v[108:111], v30, %[sWS], s66 offen offset:3072 sc1\n"
"s_waitcnt vmcnt(19) lgkmcnt(2)\n"
"v_mfma_f32_16x16x32_bf16 v[44:47], v[48:51], v[112:115], v[44:47]\n"
"v_mfma_f32_16x16x32_bf16 v[32:35], a[96:99], v[112:115], v[32:35]\n"
"ds_read_b128 v[60:63], v31 offset:15360\n"
"v_mfma_f32_16x16x32_bf16 v[36:39], a[100:103], v[112:115], v[36:39]\n"
"v_mfma_f32_16x16x32_bf16 v[40:43], v[176:179], v[112:115], v[40:43]\n"
"buffer_load_dwordx4 v[112:115], v30, %[sWS], s67 offen offset:0 sc1\n"
"s_waitcnt vmcnt(19) lgkmcnt(2)\n"
"v_mfma_f32_16x16x32_bf16 v[44:47], v[52:55], v[116:119], v[44:47]\n"
"v_mfma_f32_16x16x32_bf16 v[32:35], a[104:107], v[116:119], v[32:35]\n"
"ds_read_b128 v[48:51], v31 offset:16384\n"
"v_mfma_f32_16x16x32_bf16 v[36:39], a[108:111], v[116:119], v[36:39]\n"
"v_mfma_f32_16x16x32_bf16 v[40:43], v[180:183], v[116:119], v[40:43]\n"
"buffer_load_dwordx4 v[116:119], v30, %[sWS], s67 offen offset:1024 sc1\n"
"s_waitcnt vmcnt(19) lgkmcnt(2)\n"
"v_mfma_f32_16x16x32_bf16 v[44:47], v[56:59], v[120:123], v[44:47]\n"
"v_mfma_f32_16x16x32_bf16 v[32:35], a[112:115], v[120:123], v[32:35]\n"
"ds_read_b128 v[52:55], v31 offset:17408\n"
"v_mfma_f32_16x16x32_bf16 v[36:39], a[116:119], v[120:123], v[36:39]\n"
"v_mfma_f32_16x16x32_bf16 v[40:43], v[184:187], v[120:123], v[40:43]\n"
"buffer_load_dwordx4 v[120:123], v30, %[sWS], s67 offen offset:2048 sc1\n"
"s_waitcnt vmcnt(19) lgkmcnt(2)\n"
"v_mfma_f32_16x16x32_bf16 v[44:47], v[60:63], v[124:127], v[44:47]\n"
"v_mfma_f32_16x16x32_bf16 v[32:35], a[120:123], v[124:127], v[32:35]\n"
"ds_read_b128 v[56:59], v31 offset:18432\n"
"v_mfma_f32_16x16x32_bf16 v[36:39], a[124:127], v[124:127], v[36:39]\n"
"v_mfma_f32_16x16x32_bf16 v[40:43], v[188:191], v[124:127], v[40:43]\n"
"buffer_load_dwordx4 v[124:127], v30, %[sWS], s67 offen offset:3072 sc1\n"
"s_waitcnt vmcnt(15) lgkmcnt(2)\n"
"v_mfma_f32_16x16x32_bf16 v[44:47], v[48:51], v[64:67], v[44:47]\n"
"v_mfma_f32_16x16x32_bf16 v[32:35], a[128:131], v[64:67], v[32:35]\n"
"ds_read_b128 v[60:63], v31 offset:19456\n"
"v_mfma_f32_16x16x32_bf16 v[36:39], a[132:135], v[64:67], v[36:39]\n"
"s_lshl_b32 s46, s44, 18\n"
"v_add_u32 v30, s46, %[vOL]\n"
"v_mfma_f32_16x16x32_bf16 v[40:43], v[192:195], v[64:67], v[40:43]\n"
"buffer_load_dwordx4 v[64:67], v30, %[sWS], s60 offen offset:0 sc1\n"
"s_waitcnt vmcnt(15) lgkmcnt(2)\n"
"v_mfma_f32_16x16x32_bf16 v[44:47], v[52:55], v[68:71], v[44:47]\n"
"v_mfma_f32_16x16x32_bf16 v[32:35], a[136:139], v[68:71], v[32:35]\n"
"ds_read_b128 v[48:51], v31 offset:20480\n"
"v_mfma_f32_16x16x32_bf16 v[36:39], a[140:143], v[68:71], v[36:39]\n"
"v_mfma_f32_16x16x32_bf16 v[40:43], v[196:199], v[68:71], v[40:43]\n"
"buffer_load_dwordx4 v[68:71], v30, %[sWS], s60 offen offset:1024 sc1\n"
"s_waitcnt vmcnt(15) lgkmcnt(2)\n"
"v_mfma_f32_16x16x32_bf16 v[44:47], v[56:59], v[72:75], v[44:47]\n"
"v_mfma_f32_16x16x32_bf16 v[32:35], a[144:147], v[72:75], v[32:35]\n"
"ds_read_b128 v[52:55], v31 offset:21504\n"
"v_mfma_f32_16x16x32_bf16 v[36:39], a[148:151], v[72:75], v[36:39]\n"
"v_mfma_f32_16x16x32_bf16 v[40:43], v[200:203], v[72:75], v[40:43]\n"
"buffer_load_dwordx4 v[72:75], v30, %[sWS], s60 offen offset:2048 sc1\n"
"s_waitcnt vmcnt(15) lgkmcnt(2)\n"
"v_mfma_f32_16x16x32_bf16 v[44:47], v[60:63], v[76:79], v[44:47]\n"
"v_mfma_f32_16x16x32_bf16 v[32:35], a[152:155], v[76:79], v[32:35]\n"
"ds_read_b128 v[56:59], v31 offset:22528\n"
"v_mfma_f32_16x16x32_bf16 v[36:39], a[156:159], v[76:79], v[36:39]\n"
"v_mfma_f32_16x16x32_bf16 v[40:43], v[204:207], v[76:79], v[40:43]\n"
"buffer_load_dwordx4 v[76:79], v30, %[sWS], s60 offen offset:3072 sc1\n"
"s_waitcnt vmcnt(15) lgkmcnt(2)\n"
"v_mfma_f32_16x16x32_bf16 v[44:47], v[48:51], v[80:83], v[44:47]\n"
"v_mfma_f32_16x16x32_bf16 v[32:35], a[160:163], v[80:83], v[32:35]\n"
"ds_read_b128 v[60:63], v31 offset:23552\n"
"v_mfma_f32_16x16x32_bf16 v[36:39], a[164:167], v[80:83], v[36:39]\n"
"v_mfma_f32_16x16x32_bf16 v[40:43], v[208:211], v[80:83], v[40:43]\n"
"buffer_load_dwordx4 v[80:83], v30, %[sWS], s61 offen offset:0 sc1\n"
"s_waitcnt vmcnt(15) lgkmcnt(2)\n"
"v_mfma_f32_16x16x32_bf16 v[44:47], v[52:55], v[84:87], v[44:47]\n"
"v_mfma_f32_16x16x32_bf16 v[32:35], a[168:171], v[84:87], v[32:35]\n"
"ds_read_b128 v[48:51], v31 offset:24576\n"
"v_mfma_f32_16x16x32_bf16 v[36:39], a[172:175], v[84:87], v[36:39]\n"
"v_mfma_f32_16x16x32_bf16 v[40:43], v[212:215], v[84:87], v[40:43]\n"
"buffer_load_dwordx4 v[84:87], v30, %[sWS], s61 offen offset:1024 sc1\n"
"s_waitcnt vmcnt(15) lgkmcnt(2)\n"
"v_mfma_f32_16x16x32_bf16 v[44:47], v[56:59], v[88:91], v[44:47]\n"
"v_mfma_f32_16x16x32_bf16 v[32:35], a[176:179], v[88:91], v[32:35]\n"
"ds_read_b128 v[52:55], v31 offset:25600\n"
"v_mfma_f32_16x16x32_bf16 v[36:39], a[180:183], v[88:91], v[36:39]\n"
"v_mfma_f32_16x16x32_bf16 v[40:43], v[216:219], v[88:91], v[40:43]\n"
"buffer_load_dwordx4 v[88:91], v30, %[sWS], s61 offen offset:2048 sc1\n"
"s_waitcnt vmcnt(15) lgkmcnt(2)\n"
"v_mfma_f32_16x16x32_bf16 v[44:47], v[60:63], v[92:95], v[44:47]\n"
"v_mfma_f32_16x16x32_bf16 v[32:35], a[184:187], v[92:95], v[32:35]\n"
"ds_read_b128 v[56:59], v31 offset:26624\n"
"v_mfma_f32_16x16x32_bf16 v[36:39], a[188:191], v[92:95], v[36:39]\n"
"v_mfma_f32_16x16x32_bf16 v[40:43], v[220:223], v[92:95], v[40:43]\n"
"buffer_load_dwordx4 v[92:95], v30, %[sWS], s61 offen offset:3072 sc1\n"
"s_waitcnt vmcnt(15) lgkmcnt(2)\n"
"v_mfma_f32_16x16x32_bf16 v[44:47], v[48:51], v[96:99], v[44:47]\n"
"v_mfma_f32_16x16x32_bf16 v[32:35], a[192:195], v[96:99], v[32:35]\n"
"ds_read_b128 v[60:63], v31 offset:27648\n"
"v_mfma_f32_16x16x32_bf16 v[36:39], a[196:199], v[96:99], v[36:39]\n"
"v_mfma_f32_16x16x32_bf16 v[40:43], v[224:227], v[96:99], v[40:43]\n"
"buffer_load_dwordx4 v[96:99], v30, %[sWS], s62 offen offset:0 sc1\n"
"s_waitcnt vmcnt(15) lgkmcnt(2)\n"
"v_mfma_f32_16x16x32_bf16 v[44:47], v[52:55], v[100:103], v[44:47]\n"
"v_mfma_f32_16x16x32_bf16 v[32:35], a[200:203], v[100:103], v[32:35]\n"
"ds_read_b128 v[48:51], v31 offset:28672\n"
"v_mfma_f32_16x16x32_bf16 v[36:39], a[204:207], v[100:103], v[36:39]\n"
"v_mfma_f32_16x16x32_bf16 v[40:43], v[228:231], v[100:103], v[40:43]\n"
"buffer_load_dwordx4 v[100:103], v30, %[sWS], s62 offen offset:1024 sc1\n"
"s_waitcnt vmcnt(15) lgkmcnt(2)\n"
"v_mfma_f32_16x16x32_bf16 v[44:47], v[56:59], v[104:107], v[44:47]\n"
"v_mfma_f32_16x16x32_bf16 v[32:35], a[208:211], v[104:107], v[32:35]\n"
"ds_read_b128 v[52:55], v31 offset:29696\n"
"v_mfma_f32_16x16x32_bf16 v[36:39], a[212:215], v[104:107], v[36:39]\n"
"v_mfma_f32_16x16x32_bf16 v[40:43], v[232:235], v[104:107], v[40:43]\n"
"buffer_load_dwordx4 v[104:107], v30, %[sWS], s62 offen offset:2048 sc1\n"
"s_waitcnt vmcnt(15) lgkmcnt(2)\n"
"v_mfma_f32_16x16x32_bf16 v[44:47], v[60:63], v[108:111], v[44:47]\n"
"v_mfma_f32_16x16x32_bf16 v[32:35], a[216:219], v[108:111], v[32:35]\n"
"ds_read_b128 v[56:59], v31 offset:30720\n"
"v_mfma_f32_16x16x32_bf16 v[36:39], a[220:223], v[108:111], v[36:39]\n"
"v_mfma_f32_16x16x32_bf16 v[40:43], v[236:239], v[108:111], v[40:43]\n"
"buffer_load_dwordx4 v[108:111], v30, %[sWS], s62 offen offset:3072 sc1\n"
"s_waitcnt vmcnt(15) lgkmcnt(2)\n"
"v_mfma_f32_16x16x32_bf16 v[44:47], v[48:51], v[112:115], v[44:47]\n"
"v_mfma_f32_16x16x32_bf16 v[32:35], a[224:227], v[112:115], v[32:35]\n"
"ds_read_b128 v[60:63], v31 offset:31744\n"
"v_mfma_f32_16x16x32_bf16 v[36:39], a[228:231], v[112:115], v[36:39]\n"
"v_mfma_f32_16x16x32_bf16 v[40:43], v[240:243], v[112:115], v[40:43]\n"
"buffer_load_dwordx4 v[112:115], v30, %[sWS], s63 offen offset:0 sc1\n"
"s_waitcnt vmcnt(15) lgkmcnt(2)\n"
"v_mfma_f32_16x16x32_bf16 v[44:47], v[52:55], v[116:119], v[44:47]\n"
"v_mfma_f32_16x16x32_bf16 v[32:35], a[232:235], v[116:119], v[32:35]\n"
"v_mfma_f32_16x16x32_bf16 v[36:39], a[236:239], v[116:119], v[36:39]\n"
"v_mfma_f32_16x16x32_bf16 v[40:43], v[244:247], v[116:119], v[40:43]\n"
"buffer_load_dwordx4 v[116:119], v30, %[sWS], s63 offen offset:1024 sc1\n"
"s_waitcnt vmcnt(15) lgkmcnt(1)\n"
"v_mfma_f32_16x16x32_bf16 v[44:47], v[56:59], v[120:123], v[44:47]\n"
"v_mfma_f32_16x16x32_bf16 v[32:35], a[240:243], v[120:123], v[32:35]\n"
"v_mfma_f32_16x16x32_bf16 v[36:39], a[244:247], v[120:123], v[36:39]\n"
"v_mfma_f32_16x16x32_bf16 v[40:43], v[248:251], v[120:123], v[40:43]\n"
"buffer_load_dwordx4 v[120:123], v30, %[sWS], s63 offen offset:2048 sc1\n"
"s_waitcnt vmcnt(15) lgkmcnt(0)\n"
"v_mfma_f32_16x16x32_bf16 v[44:47], v[60:63], v[124:127], v[44:47]\n"
"v_mfma_f32_16x16x32_bf16 v[32:35], a[248:251], v[124:127], v[32:35]\n"
"v_mfma_f32_16x16x32_bf16 v[36:39], a[252:255], v[124:127], v[36:39]\n"
"v_mfma_f32_16x16x32_bf16 v[40:43], v[252:255], v[124:127], v[40:43]\n"
"buffer_load_dwordx4 v[124:127], v30, %[sWS], s63 offen offset:3072 sc1\n"
"s_barrier\n"
"s_nop 3\n"
"ds_write_b128 %[vRW], v[32:35] offset:0\n"
"ds_write_b128 %[vRW], v[36:39] offset:1024\n"
"ds_write_b128 %[vRW], v[40:43] offset:2048\n"
"ds_write_b128 %[vRW], v[44:47] offset:3072\n"
"s_cmp_gt_u32 %[sW32K], 32768\n"
"s_waitcnt lgkmcnt(0)\n"
"s_barrier\n"
"s_cbranch_scc1 LO_f1\n"
"ds_read_b128 v[32:35], %[vRR] offset:0\n"
"ds_read_b128 v[36:39], %[vRR] offset:1024\n"
"ds_read_b128 v[40:43], %[vRR] offset:4096\n"
"ds_read_b128 v[44:47], %[vRR] offset:5120\n"
"ds_read_b128 v[48:51], %[vRR] offset:8192\n"
"ds_read_b128 v[52:55], %[vRR] offset:9216\n"
"ds_read_b128 v[56:59], %[vRR] offset:12288\n"
"ds_read_b128 v[60:63], %[vRR] offset:13312\n"
"s_waitcnt lgkmcnt(0)\n"
"v_pk_add_f32 v[32:33], v[32:33], v[40:41]\n"
"v_pk_add_f32 v[34:35], v[34:35], v[42:43]\n"
"v_pk_add_f32 v[36:37], v[36:37], v[44:45]\n"
"v_pk_add_f32 v[38:39], v[38:39], v[46:47]\n"
"v_pk_add_f32 v[32:33], v[32:33], v[48:49]\n"
"v_pk_add_f32 v[34:35], v[34:35], v[50:51]\n"
"v_pk_add_f32 v[36:37], v[36:37], v[52:53]\n"
"v_pk_add_f32 v[38:39], v[38:39], v[54:55]\n"
"v_pk_add_f32 v[32:33], v[32:33], v[56:57]\n"
"v_pk_add_f32 v[34:35], v[34:35], v[58:59]\n"
"v_pk_add_f32 v[36:37], v[36:37], v[60:61]\n"
"v_pk_add_f32 v[38:39], v[38:39], v[62:63]\n"
"v_cvt_pk_bf16_f32 v16, v32, v33\n"
"v_cvt_pk_bf16_f32 v17, v34, v35\n"
"v_cvt_pk_bf16_f32 v18, v36, v37\n"
"v_cvt_pk_bf16_f32 v19, v38, v39\n"
"LO_f1:\n"
"buffer_store_dwordx4 v[16:19], %[vOUT], %[sOUT], s45 offen\n"
"s_mov_b32 s43, s44\n"
"s_branch LO_run\n"
"LO_last:\n"
"s_waitcnt vmcnt(15) lgkmcnt(2)\n"
"v_mfma_f32_16x16x32_bf16 v[44:47], v[48:51], v[64:67], v[44:47]\n"
"v_mfma_f32_16x16x32_bf16 v[32:35], a[0:3], v[64:67], v[32:35]\n"
"ds_read_b128 v[60:63], v31 offset:3072\n"
"v_mfma_f32_16x16x32_bf16 v[36:39], a[4:7], v[64:67], v[36:39]\n"
"v_mfma_f32_16x16x32_bf16 v[40:43], v[128:131], v[64:67], v[40:43]\n"
"buffer_load_dwordx4 v[64:67], v30, %[sWS], s64 offen offset:0 sc1\n"
"s_waitcnt vmcnt(15) lgkmcnt(2)\n"
"v_mfma_f32_16x16x32_bf16 v[44:47], v[52:55], v[68:71], v[44:47]\n"
"v_mfma_f32_16x16x32_bf16 v[32:35], a[8:11], v[68:71], v[32:35]\n"
"ds_read_b128 v[48:51], v31 offset:4096\n"
"v_mfma_f32_16x16x32_bf16 v[36:39], a[12:15], v[68:71], v[36:39]\n"
"v_mfma_f32_16x16x32_bf16 v[40:43], v[132:135], v[68:71], v[40:43]\n"
"buffer_load_dwordx4 v[68:71], v30, %[sWS], s64 offen offset:1024 sc1\n"
"s_waitcnt vmcnt(15) lgkmcnt(2)\n"
"v_mfma_f32_16x16x32_bf16 v[44:47], v[56:59], v[72:75], v[44:47]\n"
"v_mfma_f32_16x16x32_bf16 v[32:35], a[16:19], v[72:75], v[32:35]\n"
"ds_read_b128 v[52:55], v31 offset:5120\n"
"v_mfma_f32_16x16x32_bf16 v[36:39], a[20:23], v[72:75], v[36:39]\n"
"v_mfma_f32_16x16x32_bf16 v[40:43], v[136:139], v[72:75], v[40:43]\n"
"buffer_load_dwordx4 v[72:75], v30, %[sWS], s64 offen offset:2048 sc1\n"
"s_waitcnt vmcnt(15) lgkmcnt(2)\n"
"v_mfma_f32_16x16x32_bf16 v[44:47], v[60:63], v[76:79], v[44:47]\n"
"v_mfma_f32_16x16x32_bf16 v[32:35], a[24:27], v[76:79], v[32:35]\n"
"ds_read_b128 v[56:59], v31 offset:6144\n"
"v_mfma_f32_16x16x32_bf16 v[36:39], a[28:31], v[76:79], v[36:39]\n"
"v_mfma_f32_16x16x32_bf16 v[40:43], v[140:143], v[76:79], v[40:43]\n"
"buffer_load_dwordx4 v[76:79], v30, %[sWS], s64 offen offset:3072 sc1\n"
"s_waitcnt vmcnt(15) lgkmcnt(2)\n"
"v_mfma_f32_16x16x32_bf16 v[44:47], v[48:51], v[80:83], v[44:47]\n"
"v_mfma_f32_16x16x32_bf16 v[32:35], a[32:35], v[80:83], v[32:35]\n"
"ds_read_b128 v[60:63], v31 offset:7168\n"
"v_mfma_f32_16x16x32_bf16 v[36:39], a[36:39], v[80:83], v[36:39]\n"
"v_mfma_f32_16x16x32_bf16 v[40:43], v[144:147], v[80:83], v[40:43]\n"
"buffer_load_dwordx4 v[80:83], v30, %[sWS], s65 offen offset:0 sc1\n"
"s_waitcnt vmcnt(15) lgkmcnt(2)\n"
"v_mfma_f32_16x16x32_bf16 v[44:47], v[52:55], v[84:87], v[44:47]\n"
"v_mfma_f32_16x16x32_bf16 v[32:35], a[40:43], v[84:87], v[32:35]\n"
"ds_read_b128 v[48:51], v31 offset:8192\n"
"v_mfma_f32_16x16x32_bf16 v[36:39], a[44:47], v[84:87], v[36:39]\n"
"v_mfma_f32_16x16x32_bf16 v[40:43], v[148:151], v[84:87], v[40:43]\n"
"buffer_load_dwordx4 v[84:87], v30, %[sWS], s65 offen offset:1024 sc1\n"
"s_waitcnt vmcnt(15) lgkmcnt(2)\n"
"v_mfma_f32_16x16x32_bf16 v[44:47], v[56:59], v[88:91], v[44:47]\n"
"v_mfma_f32_16x16x32_bf16 v[32:35], a[48:51], v[88:91], v[32:35]\n"
"ds_read_b128 v[52:55], v31 offset:9216\n"
"v_mfma_f32_16x16x32_bf16 v[36:39], a[52:55], v[88:91], v[36:39]\n"
"v_mfma_f32_16x16x32_bf16 v[40:43], v[152:155], v[88:91], v[40:43]\n"
"buffer_load_dwordx4 v[88:91], v30, %[sWS], s65 offen offset:2048 sc1\n"
"s_waitcnt vmcnt(15) lgkmcnt(2)\n"
"v_mfma_f32_16x16x32_bf16 v[44:47], v[60:63], v[92:95], v[44:47]\n"
"v_mfma_f32_16x16x32_bf16 v[32:35], a[56:59], v[92:95], v[32:35]\n"
"ds_read_b128 v[56:59], v31 offset:10240\n"
"v_mfma_f32_16x16x32_bf16 v[36:39], a[60:63], v[92:95], v[36:39]\n"
"v_mfma_f32_16x16x32_bf16 v[40:43], v[156:159], v[92:95], v[40:43]\n"
"buffer_load_dwordx4 v[92:95], v30, %[sWS], s65 offen offset:3072 sc1\n"
"s_waitcnt vmcnt(15) lgkmcnt(2)\n"
"v_mfma_f32_16x16x32_bf16 v[44:47], v[48:51], v[96:99], v[44:47]\n"
"v_mfma_f32_16x16x32_bf16 v[32:35], a[64:67], v[96:99], v[32:35]\n"
"ds_read_b128 v[60:63], v31 offset:11264\n"
"v_mfma_f32_16x16x32_bf16 v[36:39], a[68:71], v[96:99], v[36:39]\n"
"v_mfma_f32_16x16x32_bf16 v[40:43], v[160:163], v[96:99], v[40:43]\n"
"buffer_load_dwordx4 v[96:99], v30, %[sWS], s66 offen offset:0 sc1\n"
"s_waitcnt vmcnt(15) lgkmcnt(2)\n"
"v_mfma_f32_16x16x32_bf16 v[44:47], v[52:55], v[100:103], v[44:47]\n"
"v_mfma_f32_16x16x32_bf16 v[32:35], a[72:75], v[100:103], v[32:35]\n"
"ds_read_b128 v[48:51], v31 offset:12288\n"
"v_mfma_f32_16x16x32_bf16 v[36:39], a[76:79], v[100:103], v[36:39]\n"
"v_mfma_f32_16x16x32_bf16 v[40:43], v[164:167], v[100:103], v[40:43]\n"
"buffer_load_dwordx4 v[100:103], v30, %[sWS], s66 offen offset:1024 sc1\n"
"s_waitcnt vmcnt(15) lgkmcnt(2)\n"
"v_mfma_f32_16x16x32_bf16 v[44:47], v[56:59], v[104:107], v[44:47]\n"
"v_mfma_f32_16x16x32_bf16 v[32:35], a[80:83], v[104:107], v[32:35]\n"
"ds_read_b128 v[52:55], v31 offset:13312\n"
"v_mfma_f32_16x16x32_bf16 v[36:39], a[84:87], v[104:107], v[36:39]\n"
"v_mfma_f32_16x16x32_bf16 v[40:43], v[168:171], v[104:107], v[40:43]\n"
"buffer_load_dwordx4 v[104:107], v30, %[sWS], s66 offen offset:2048 sc1\n"
"s_waitcnt vmcnt(15) lgkmcnt(2)\n"
"v_mfma_f32_16x16x32_bf16 v[44:47], v[60:63], v[108:111], v[44:47]\n"
"v_mfma_f32_16x16x32_bf16 v[32:35], a[88:91], v[108:111], v[32:35]\n"
"ds_read_b128 v[56:59], v31 offset:14336\n"
"v_mfma_f32_16x16x32_bf16 v[36:39], a[92:95], v[108:111], v[36:39]\n"
"v_mfma_f32_16x16x32_bf16 v[40:43], v[172:175], v[108:111], v[40:43]\n"
"buffer_load_dwordx4 v[108:111], v30, %[sWS], s66 offen offset:3072 sc1\n"
"s_waitcnt vmcnt(15) lgkmcnt(2)\n"
"v_mfma_f32_16x16x32_bf16 v[44:47], v[48:51], v[112:115], v[44:47]\n"
"v_mfma_f32_16x16x32_bf16 v[32:35], a[96:99], v[112:115], v[32:35]\n"
"ds_read_b128 v[60:63], v31 offset:15360\n"
"v_mfma_f32_16x16x32_bf16 v[36:39], a[100:103], v[112:115], v[36:39]\n"
"v_mfma_f32_16x16x32_bf16 v[40:43], v[176:179], v[112:115], v[40:43]\n"
"buffer_load_dwordx4 v[112:115], v30, %[sWS], s67 offen offset:0 sc1\n"
"s_waitcnt vmcnt(15) lgkmcnt(2)\n"
"v_mfma_f32_16x16x32_bf16 v[44:47], v[52:55], v[116:119], v[44:47]\n"
"v_mfma_f32_16x16x32_bf16 v[32:35], a[104:107], v[116:119], v[32:35]\n"
"ds_read_b128 v[48:51], v31 offset:16384\n"
"v_mfma_f32_16x16x32_bf16 v[36:39], a[108:111], v[116:119], v[36:39]\n"
"v_mfma_f32_16x16x32_bf16 v[40:43], v[180:183], v[116:119], v[40:43]\n"
"buffer_load_dwordx4 v[116:119], v30, %[sWS], s67 offen offset:1024 sc1\n"
"s_waitcnt vmcnt(15) lgkmcnt(2)\n"
"v_mfma_f32_16x16x32_bf16 v[44:47], v[56:59], v[120:123], v[44:47]\n"
"v_mfma_f32_16x16x32_bf16 v[32:35], a[112:115], v[120:123], v[32:35]\n"
"ds_read_b128 v[52:55], v31 offset:17408\n"
"v_mfma_f32_16x16x32_bf16 v[36:39], a[116:119], v[120:123], v[36:39]\n"
"v_mfma_f32_16x16x32_bf16 v[40:43], v[184:187], v[120:123], v[40:43]\n"
"buffer_load_dwordx4 v[120:123], v30, %[sWS], s67 offen offset:2048 sc1\n"
"s_waitcnt vmcnt(15) lgkmcnt(2)\n"
"v_mfma_f32_16x16x32_bf16 v[44:47], v[60:63], v[124:127], v[44:47]\n"
"v_mfma_f32_16x16x32_bf16 v[32:35], a[120:123], v[124:127], v[32:35]\n"
"ds_read_b128 v[56:59], v31 offset:18432\n"
"v_mfma_f32_16x16x32_bf16 v[36:39], a[124:127], v[124:127], v[36:39]\n"
"v_mfma_f32_16x16x32_bf16 v[40:43], v[188:191], v[124:127], v[40:43]\n"
"buffer_load_dwordx4 v[124:127], v30, %[sWS], s67 offen offset:3072 sc1\n"
"s_waitcnt vmcnt(15) lgkmcnt(2)\n"
"v_mfma_f32_16x16x32_bf16 v[44:47], v[48:51], v[64:67], v[44:47]\n"
"v_mfma_f32_16x16x32_bf16 v[32:35], a[128:131], v[64:67], v[32:35]\n"
"ds_read_b128 v[60:63], v31 offset:19456\n"
"v_mfma_f32_16x16x32_bf16 v[36:39], a[132:135], v[64:67], v[36:39]\n"
"v_mfma_f32_16x16x32_bf16 v[40:43], v[192:195], v[64:67], v[40:43]\n"
"s_waitcnt vmcnt(14) lgkmcnt(2)\n"
"v_mfma_f32_16x16x32_bf16 v[44:47], v[52:55], v[68:71], v[44:47]\n"
"v_mfma_f32_16x16x32_bf16 v[32:35], a[136:139], v[68:71], v[32:35]\n"
"ds_read_b128 v[48:51], v31 offset:20480\n"
"v_mfma_f32_16x16x32_bf16 v[36:39], a[140:143], v[68:71], v[36:39]\n"
"v_mfma_f32_16x16x32_bf16 v[40:43], v[196:199], v[68:71], v[40:43]\n"
"s_waitcnt vmcnt(13) lgkmcnt(2)\n"
"v_mfma_f32_16x16x32_bf16 v[44:47], v[56:59], v[72:75], v[44:47]\n"
"v_mfma_f32_16x16x32_bf16 v[32:35], a[144:147], v[72:75], v[32:35]\n"
"ds_read_b128 v[52:55], v31 offset:21504\n"
"v_mfma_f32_16x16x32_bf16 v[36:39], a[148:151], v[72:75], v[36:39]\n"
"v_mfma_f32_16x16x32_bf16 v[40:43], v[200:203], v[72:75], v[40:43]\n"
"s_waitcnt vmcnt(12) lgkmcnt(2)\n"
"v_mfma_f32_16x16x32_bf16 v[44:47], v[60:63], v[76:79], v[44:47]\n"
"v_mfma_f32_16x16x32_bf16 v[32:35], a[152:155], v[76:79], v[32:35]\n"
"ds_read_b128 v[56:59], v31 offset:22528\n"
"v_mfma_f32_16x16x32_bf16 v[36:39], a[156:159], v[76:79], v[36:39]\n"
"v_mfma_f32_16x16x32_bf16 v[40:43], v[204:207], v[76:79], v[40:43]\n"
"s_waitcnt vmcnt(11) lgkmcnt(2)\n"
"v_mfma_f32_16x16x32_bf16 v[44:47], v[48:51], v[80:83], v[44:47]\n"
"v_mfma_f32_16x16x32_bf16 v[32:35], a[160:163], v[80:83], v[32:35]\n"
"ds_read_b128 v[60:63], v31 offset:23552\n"
"v_mfma_f32_16x16x32_bf16 v[36:39], a[164:167], v[80:83], v[36:39]\n"
"v_mfma_f32_16x16x32_bf16 v[40:43], v[208:211], v[80:83], v[40:43]\n"
"s_waitcnt vmcnt(10) lgkmcnt(2)\n"
"v_mfma_f32_16x16x32_bf16 v[44:47], v[52:55], v[84:87], v[44:47]\n"
"v_mfma_f32_16x16x32_bf16 v[32:35], a[168:171], v[84:87], v[32:35]\n"
"ds_read_b128 v[48:51], v31 offset:24576\n"
"v_mfma_f32_16x16x32_bf16 v[36:39], a[172:175], v[84:87], v[36:39]\n"
"v_mfma_f32_16x16x32_bf16 v[40:43], v[212:215], v[84:87], v[40:43]\n"
"s_waitcnt vmcnt(9) lgkmcnt(2)\n"
"v_mfma_f32_16x16x32_bf16 v[44:47], v[56:59], v[88:91], v[44:47]\n"
"v_mfma_f32_16x16x32_bf16 v[32:35], a[176:179], v[88:91], v[32:35]\n"
"ds_read_b128 v[52:55], v31 offset:25600\n"
"v_mfma_f32_16x16x32_bf16 v[36:39], a[180:183], v[88:91], v[36:39]\n"
"v_mfma_f32_16x16x32_bf16 v[40:43], v[216:219], v[88:91], v[40:43]\n"
"s_waitcnt vmcnt(8) lgkmcnt(2)\n"
"v_mfma_f32_16x16x32_bf16 v[44:47], v[60:63], v[92:95], v[44:47]\n"
"v_mfma_f32_16x16x32_bf16 v[32:35], a[184:187], v[92:95], v[32:35]\n"
"ds_read_b128 v[56:59], v31 offset:26624\n"
"v_mfma_f32_16x16x32_bf16 v[36:39], a[188:191], v[92:95], v[36:39]\n"
"v_mfma_f32_16x16x32_bf16 v[40:43], v[220:223], v[92:95], v[40:43]\n"
"s_waitcnt vmcnt(7) lgkmcnt(2)\n"
"v_mfma_f32_16x16x32_bf16 v[44:47], v[48:51], v[96:99], v[44:47]\n"
"v_mfma_f32_16x16x32_bf16 v[32:35], a[192:195], v[96:99], v[32:35]\n"
"ds_read_b128 v[60:63], v31 offset:27648\n"
"v_mfma_f32_16x16x32_bf16 v[36:39], a[196:199], v[96:99], v[36:39]\n"
"v_mfma_f32_16x16x32_bf16 v[40:43], v[224:227], v[96:99], v[40:43]\n"
"s_waitcnt vmcnt(6) lgkmcnt(2)\n"
"v_mfma_f32_16x16x32_bf16 v[44:47], v[52:55], v[100:103], v[44:47]\n"
"v_mfma_f32_16x16x32_bf16 v[32:35], a[200:203], v[100:103], v[32:35]\n"
"ds_read_b128 v[48:51], v31 offset:28672\n"
"v_mfma_f32_16x16x32_bf16 v[36:39], a[204:207], v[100:103], v[36:39]\n"
"v_mfma_f32_16x16x32_bf16 v[40:43], v[228:231], v[100:103], v[40:43]\n"
"s_waitcnt vmcnt(5) lgkmcnt(2)\n"
"v_mfma_f32_16x16x32_bf16 v[44:47], v[56:59], v[104:107], v[44:47]\n"
"v_mfma_f32_16x16x32_bf16 v[32:35], a[208:211], v[104:107], v[32:35]\n"
"ds_read_b128 v[52:55], v31 offset:29696\n"
"v_mfma_f32_16x16x32_bf16 v[36:39], a[212:215], v[104:107], v[36:39]\n"
"v_mfma_f32_16x16x32_bf16 v[40:43], v[232:235], v[104:107], v[40:43]\n"
"s_waitcnt vmcnt(4) lgkmcnt(2)\n"
"v_mfma_f32_16x16x32_bf16 v[44:47], v[60:63], v[108:111], v[44:47]\n"
"v_mfma_f32_16x16x32_bf16 v[32:35], a[216:219], v[108:111], v[32:35]\n"
"ds_read_b128 v[56:59], v31 offset:30720\n"
"v_mfma_f32_16x16x32_bf16 v[36:39], a[220:223], v[108:111], v[36:39]\n"
"v_mfma_f32_16x16x32_bf16 v[40:43], v[236:239], v[108:111], v[40:43]\n"
"s_waitcnt vmcnt(3) lgkmcnt(2)\n"
"v_mfma_f32_16x16x32_bf16 v[44:47], v[48:51], v[112:115], v[44:47]\n"
"v_mfma_f32_16x16x32_bf16 v[32:35], a[224:227], v[112:115], v[32:35]\n"
"ds_read_b128 v[60:63], v31 offset:31744\n"
"v_mfma_f32_16x16x32_bf16 v[36:39], a[228:231], v[112:115], v[36:39]\n"
"v_mfma_f32_16x16x32_bf16 v[40:43], v[240:243], v[112:115], v[40:43]\n"
"s_waitcnt vmcnt(2) lgkmcnt(2)\n"
"v_mfma_f32_16x16x32_bf16 v[44:47], v[52:55], v[116:119], v[44:47]\n"
"v_mfma_f32_16x16x32_bf16 v[32:35], a[232:235], v[116:119], v[32:35]\n"
"v_mfma_f32_16x16x32_bf16 v[36:39], a[236:239], v[116:119], v[36:39]\n"
"v_mfma_f32_16x16x32_bf16 v[40:43], v[244:247], v[116:119], v[40:43]\n"
"s_waitcnt vmcnt(1) lgkmcnt(1)\n"
"v_mfma_f32_16x16x32_bf16 v[44:47], v[56:59], v[120:123], v[44:47]\n"
"v_mfma_f32_16x16x32_bf16 v[32:35], a[240:243], v[120:123], v[32:35]\n"
"v_mfma_f32_16x16x32_bf16 v[36:39], a[244:247], v[120:123], v[36:39]\n"
"v_mfma_f32_16x16x32_bf16 v[40:43], v[248:251], v[120:123], v[40:43]\n"
"s_waitcnt vmcnt(0) lgkmcnt(0)\n"
"v_mfma_f32_16x16x32_bf16 v[44:47], v[60:63], v[124:127], v[44:47]\n"
"v_mfma_f32_16x16x32_bf16 v[32:35], a[248:251], v[124:127], v[32:35]\n"
"v_mfma_f32_16x16x32_bf16 v[36:39], a[252:255], v[124:127], v[36:39]\n"
"v_mfma_f32_16x16x32_bf16 v[40:43], v[252:255], v[124:127], v[40:43]\n"
"s_barrier\n"
"s_nop 4\n"
"ds_write_b128 %[vRW], v[32:35] offset:0\n"
"ds_write_b128 %[vRW], v[36:39] offset:1024\n"
"ds_write_b128 %[vRW], v[40:43] offset:2048\n"
"ds_write_b128 %[vRW], v[44:47] offset:3072\n"
"s_cmp_gt_u32 %[sW32K], 32768\n"
"s_waitcnt lgkmcnt(0)\n"
"s_barrier\n"
"s_cbranch_scc1 LO_f2\n"
"ds_read_b128 v[32:35], %[vRR] offset:0\n"
"ds_read_b128 v[36:39], %[vRR] offset:1024\n"
"ds_read_b128 v[40:43], %[vRR] offset:4096\n"
"ds_read_b128 v[44:47], %[vRR] offset:5120\n"
"ds_read_b128 v[48:51], %[vRR] offset:8192\n"
"ds_read_b128 v[52:55], %[vRR] offset:9216\n"
"ds_read_b128 v[56:59], %[vRR] offset:12288\n"
"ds_read_b128 v[60:63], %[vRR] offset:13312\n"
"s_waitcnt lgkmcnt(0)\n"
"v_pk_add_f32 v[32:33], v[32:33], v[40:41]\n"
"v_pk_add_f32 v[34:35], v[34:35], v[42:43]\n"
"v_pk_add_f32 v[36:37], v[36:37], v[44:45]\n"
"v_pk_add_f32 v[38:39], v[38:39], v[46:47]\n"
"v_pk_add_f32 v[32:33], v[32:33], v[48:49]\n"
"v_pk_add_f32 v[34:35], v[34:35], v[50:51]\n"
"v_pk_add_f32 v[36:37], v[36:37], v[52:53]\n"
"v_pk_add_f32 v[38:39], v[38:39], v[54:55]\n"
"v_pk_add_f32 v[32:33], v[32:33], v[56:57]\n"
"v_pk_add_f32 v[34:35], v[34:35], v[58:59]\n"
"v_pk_add_f32 v[36:37], v[36:37], v[60:61]\n"
"v_pk_add_f32 v[38:39], v[38:39], v[62:63]\n"
"v_cvt_pk_bf16_f32 v16, v32, v33\n"
"v_cvt_pk_bf16_f32 v17, v34, v35\n"
"v_cvt_pk_bf16_f32 v18, v36, v37\n"
"v_cvt_pk_bf16_f32 v19, v38, v39\n"
"LO_f2:\n"
"buffer_store_dwordx4 v[16:19], %[vOUT], %[sOUT], s45 offen\n"
"s_mov_b32 s43, s44\n"
"s_cmp_lt_u32 s43, 64\n"
"s_cbranch_scc1 LO_task\n"
"s_branch LO_done\n"
"LO_tmo:\n"
"s_mov_b64 exec, 1\n"
"v_mov_b32 v20, 5\n"
"s_mov_b32 s40, 1065088\n"
"buffer_store_dword v20, off, %[sWS], s40 sc1\n"
"s_endpgm\n"
"LO_done:\n"
        :
        : [sWS] "s"(sWS), [sSRC] "s"(wt_off(par, J * 4 + w)), [sSRC2] "s"(wt_off(par, J * 4 + 4 + w)), [sAD] "s"(sAD), [sB2] "s"(sB2), [sOUT] "s"(sOUT),
          [sW32K] "s"(sW32K), [sSEG] "s"(xg), [sFAR] "s"(par), [sYB] "s"(rfl(WS_YB + w * 6291456 + kq * 49152 + (J / 6) * 16384)), [sROT] "s"(rfl(r % 9 + 2 * xg + 4 * kq)),
          [vPW] "v"(vPW), [vOUT] "v"(vOUT), [vAD] "v"(vAD), [vAD2] "v"(vAD2), [vB2] "v"(vB2), [vOL] "v"(vOL), [vRW] "v"(vRW), [vRR] "v"(vRR), [vPX] "v"(vPX), [vL16] "v"(vL16), [vL128] "v"(vL128)
        : "v16", "v17", "v18", "v19", "v20", "v21", "v22", "v23", "v24", "v25", "v26", "v27", "v28", "v29", "v30", "v31", "v32", "v33", "v34", "v35", "v36", "v37", "v38", "v39",
"v40", "v41", "v42", "v43", "v44", "v45", "v46", "v47", "v48", "v49", "v50", "v51", "v52", "v53", "v54", "v55", "v56", "v57", "v58", "v59", "v60", "v61", "v62", "v63",
"v64", "v65", "v66", "v67", "v68", "v69", "v70", "v71", "v72", "v73", "v74", "v75", "v76", "v77", "v78", "v79", "v80", "v81", "v82", "v83", "v84", "v85", "v86", "v87",
"v88", "v89", "v90", "v91", "v92", "v93", "v94", "v95", "v96", "v97", "v98", "v99", "v100", "v101", "v102", "v103", "v104", "v105", "v106", "v107", "v108", "v109", "v110", "v111",
"v112", "v113", "v114", "v115", "v116", "v117", "v118", "v119", "v120", "v121", "v122", "v123", "v124", "v125", "v126", "v127", "v128", "v129", "v130", "v131", "v132", "v133", "v134", "v135",
"v136", "v137", "v138", "v139", "v140", "v141", "v142", "v143", "v144", "v145", "v146", "v147", "v148", "v149", "v150", "v151", "v152", "v153", "v154", "v155", "v156", "v157", "v158", "v159",
"v160", "v161", "v162", "v163", "v164", "v165", "v166", "v167", "v168", "v169", "v170", "v171", "v172", "v173", "v174", "v175", "v176", "v177", "v178", "v179", "v180", "v181", "v182", "v183",
"v184", "v185", "v186", "v187", "v188", "v189", "v190", "v191", "v192", "v193", "v194", "v195", "v196", "v197", "v198", "v199", "v200", "v201", "v202", "v203", "v204", "v205", "v206", "v207",
"v208", "v209", "v210", "v211", "v212", "v213", "v214", "v215", "v216", "v217", "v218", "v219", "v220", "v221", "v222", "v223", "v224", "v225", "v226", "v227", "v228", "v229", "v230", "v231",
"v232", "v233", "v234", "v235", "v236", "v237", "v238", "v239", "v240", "v241", "v242", "v243", "v244", "v245", "v246", "v247", "v248", "v249", "v250", "v251", "v252", "v253", "v254", "v255",
"a0", "a1", "a2", "a3", "a4", "a5", "a6", "a7", "a8", "a9", "a10", "a11", "a12", "a13", "a14", "a15", "a16", "a17", "a18", "a19", "a20", "a21", "a22", "a23",
"a24", "a25", "a26", "a27", "a28", "a29", "a30", "a31", "a32", "a33", "a34", "a35", "a36", "a37", "a38", "a39", "a40", "a41", "a42", "a43", "a44", "a45", "a46", "a47",
"a48", "a49", "a50", "a51", "a52", "a53", "a54", "a55", "a56", "a57", "a58", "a59", "a60", "a61", "a62", "a63", "a64", "a65", "a66", "a67", "a68", "a69", "a70", "a71",
"a72", "a73", "a74", "a75", "a76", "a77", "a78", "a79", "a80", "a81", "a82", "a83", "a84", "a85", "a86", "a87", "a88", "a89", "a90", "a91", "a92", "a93", "a94", "a95",
"a96", "a97", "a98", "a99", "a100", "a101", "a102", "a103", "a104", "a105", "a106", "a107", "a108", "a109", "a110", "a111", "a112", "a113", "a114", "a115", "a116", "a117", "a118", "a119",
"a120", "a121", "a122", "a123", "a124", "a125", "a126", "a127", "a128", "a129", "a130", "a131", "a132", "a133", "a134", "a135", "a136", "a137", "a138", "a139", "a140", "a141", "a142", "a143",
"a144", "a145", "a146", "a147", "a148", "a149", "a150", "a151", "a152", "a153", "a154", "a155", "a156", "a157", "a158", "a159", "a160", "a161", "a162", "a163", "a164", "a165", "a166", "a167",
"a168", "a169", "a170", "a171", "a172", "a173", "a174", "a175", "a176", "a177", "a178", "a179", "a180", "a181", "a182", "a183", "a184", "a185", "a186", "a187", "a188", "a189", "a190", "a191",
"a192", "a193", "a194", "a195", "a196", "a197", "a198", "a199", "a200", "a201", "a202", "a203", "a204", "a205", "a206", "a207", "a208", "a209", "a210", "a211", "a212", "a213", "a214", "a215",
"a216", "a217", "a218", "a219", "a220", "a221", "a222", "a223", "a224", "a225", "a226", "a227", "a228", "a229", "a230", "a231", "a232", "a233", "a234", "a235", "a236", "a237", "a238", "a239",
"a240", "a241", "a242", "a243", "a244", "a245", "a246", "a247", "a248", "a249", "a250", "a251", "a252", "a253", "a254", "a255", "s40", "s41", "s42", "s43", "s44", "s45", "s46", "s47",
"s48", "s49", "s50", "s51", "s52", "s53", "s54", "s55", "s56", "s57", "s58", "s59", "s60", "s61", "s62", "s63", "s64", "s65", "s66", "s67", "vcc", "scc", "memory");
}

__device__ __forceinline__ const char* slow_line(const void* m, int kb) { return (const char*)m + ((384 - (unsigned)(size_t)m) & 896) + (size_t)kb * 1024; }
__device__ __forceinline__ void role_touch(const void* wq, const void* wk, const void* wv, const void* wo, int xg, int r) {
    const int i = r * 256 + threadIdx.x, row = i / 48, qi = (i % 48) >> 2, q = qi * 4 + ((xg + qi + (qi >> 2)) & 3);
    const char* base = (const char*)(q < 16 ? wq : (q < 32 ? wk : wv)) + (size_t)(192 + row) * 8192 + (q & 15) * 512 + (i & 3) * 128;
    const char* s0 = TS0 ? slow_line(xg == 0 ? wq : (xg == 1 ? wk : (xg == 2 ? wv : wo)), i) : base;
    unsigned d0, d1, d2, d3, d4;
    asm volatile("global_load_dword %4, %9, off\n\tglobal_load_dword %0, %5, off\n\tglobal_load_dword %1, %6, off\n\tglobal_load_dword %2, %7, off\n\tglobal_load_dword %3, %8, off\n\ts_waitcnt vmcnt(0)"
                 : "=&v"(d0), "=&v"(d1), "=&v"(d2), "=&v"(d3), "=&v"(d4) : "v"(base), "v"(base + 96 * 8192), "v"(base + 192 * 8192), "v"(base + 288 * 8192), "v"(s0) : "memory");
}

__device__ __forceinline__ unsigned long long clk() { unsigned long long t; asm volatile("s_memtime %0\n\ts_waitcnt lgkmcnt(0)" : "=s"(t) :: "memory"); return t; }
constexpr int NPAIR = 2, NLH = 3, XMAP = 1, QMAP = 1, LEAD_Q = 110, LEAD_X = 80, LEAD_Y = 60;
__device__ __forceinline__ void code_pull() {
    unsigned d0, d1;
    asm volatile("s_getpc_b64 s[40:41]\nLH_pc:\n\ts_sub_u32 s40, s40, 0x40000\n\ts_subb_u32 s41, s41, 0\n\t"
                 "v_min_u32 %0, LX_done-LX_top0, %2\n\tv_min_u32 %1, LY_done-LY_top, %2\n\t"
                 "v_add_u32 %0, LX_top0-LH_pc+0x40000, %0\n\tv_add_u32 %1, LY_top-LH_pc+0x40000, %1\n\t"
                 "global_load_dword %0, %0, s[40:41]\n\tglobal_load_dword %1, %1, s[40:41]\n\ts_waitcnt vmcnt(0)"
                 : "=&v"(d0), "=&v"(d1) : "v"(threadIdx.x * 128) : "s40", "s41", "scc", "memory");
}
__device__ __forceinline__ void code_pull2() {
    unsigned d0, d1;
    asm volatile("s_getpc_b64 s[40:41]\nLG_pc:\n\ts_sub_u32 s40, s40, 0x40000\n\ts_subb_u32 s41, s41, 0\n\t"
                 "v_min_u32 %0, LQ_done-LQ_top, %2\n\tv_min_u32 %1, LB_done-LB_top, %2\n\t"
                 "v_add_u32 %0, LQ_top-LG_pc+0x40000, %0\n\tv_add_u32 %1, LB_top-LG_pc+0x40000, %1\n\t"
                 "global_load_dword %0, %0, s[40:41]\n\tglobal_load_dword %1, %1, s[40:41]\n\ts_waitcnt vmcnt(0)"
                 : "=&v"(d0), "=&v"(d1) : "v"(threadIdx.x * 128) : "s40", "s41", "scc", "memory");
}
__device__ __forceinline__ void role_lead(char* ws, int n) {
    const int tid = threadIdx.x;
    char* hb = ws + 1146880 + n * 1024 + (tid & 7) * 128;
    if (n || tid >= 128) st_dev(ws + 1024 * 1024 + (n * 256 + tid) * 128, ~0u);
    else if (tid < 16) st_dev(ws + 89165824 + (tid >> 3) * 8192 + (tid & 7) * 128, ~0u);
    unsigned h = ld_dev(hb);
    const unsigned h0 = hb_read(ws);
    EVC(1);
    asm volatile("s_waitcnt vmcnt(0)" ::: "memory");
    __syncthreads();
    EVC(2);
    if (tid >= 64) return;
    bool armed = false;
    for (int i = 0; i < 60000; ++i) {
        if (tid < 8) st_dev(hb, ++h);
        if (!armed) { armed = __all(hb_read(ws) != h0); if (armed && tid == 0) st_dev(ws + 1064960 + (n + 1) * 256, 0u); continue; }
        int m = (int)ld_dev(ws + 1064960 + ((tid & 3) ? (tid & 3) * 256 : 1280));
        if (__all(m >= 0)) m = min((int)ld_dev(ws + 1111040 + tid * 128), (int)ld_dev(ws + 1111040 + min(64 + tid, 103) * 128));
        if (__all(m >= 0)) break;
        __builtin_amdgcn_s_sleep(16);
    }
    if (tid < 8) st_dev(hb, ++h);
}
__device__ __forceinline__ void role_map(char* ws) {
    extern __shared__ __attribute__((aligned(16))) char lds[];
    const int tid = threadIdx.x, w = rfl(tid >> 6), l = tid & 63;
    unsigned* msk = (unsigned*)lds;
    if (tid == 0) *msk = 0;
    __syncthreads();
#pragma unroll 1
    for (int rg = w; rg < 21; rg += 4) {
        char* p = ws + WS_UF + ((size_t)rg << 21);
        char* q = p + (l >> 3) * 768 + (l & 7) * 16;
        asm volatile("global_store_dwordx4 %0, %2, off sc1\n\tglobal_store_dwordx4 %1, %2, off sc1\n\ts_waitcnt vmcnt(0)" :: "v"(q), "v"(q + 8192), "v"((v4u){0u, 0u, 0u, 0u}) : "memory");
        unsigned m[2] = {1u << 30, 1u << 30};
#pragma unroll 1
        for (int i = 0; i < 8; ++i) {
#pragma unroll
            for (int k = 0; k < 2; ++k) {
                const unsigned long long c0 = clk(); unsigned v;
                asm volatile("global_load_dword %0, %1, off sc1\n\ts_waitcnt vmcnt(0)" : "=v"(v) : "v"(p + k * 8192 + i * 768) : "memory");
                m[k] = min(m[k], (unsigned)(clk() - c0));
            }
            if (i >= 1 && min(m[0], m[1]) < 580 && max(m[0], m[1]) > min(m[0], m[1]) + 120) break;
        }
        if (l == 0 && (m[0] < m[1]) != (WTX == 0)) atomicOr(msk, 1u << rg);
    }
    __syncthreads();
    if (tid == 0) st_dev(ws + 1065984, *msk);
    EVC(5);
}
__device__ __forceinline__ void role_probe(char* ws) {
    extern __shared__ __attribute__((aligned(16))) char lds[];
    const int tid = threadIdx.x, w = tid >> 6, l = tid & 63;
    const unsigned h0 = hb_read(ws);
    char* p = ws + WS_RING + 1048576 + 3 * 262144 + w * 65536 + 32768 + 1024;
    unsigned* lat = (unsigned*)lds;
    if (tid < 4) lat[8 + tid] = 1u << 30;
#pragma unroll
    for (int v = 0; v < 4; ++v) {
        char* q = p + v * 8192 + (l >> 3) * 384 + (l & 7) * 16;
        asm volatile("global_store_dwordx4 %0, %1, off sc1\n\tglobal_store_dwordx4 %0, %1, off offset:3072 sc1\n\ts_nop 1" :: "v"(q), "v"((v4u){0u, 0u, 0u, 0u}) : "memory");
    }
    asm volatile("s_waitcnt vmcnt(0)" ::: "memory");
    unsigned a = 0, b = 0, v0 = (w & 1) * 2, dv = 1, left = 16;
#pragma unroll 1
    for (int i = 0; i < 16 && left; ++i, --left) {
        __syncthreads();
#pragma unroll
        for (int k = 0; k < 2; ++k) {
            const unsigned long long c0 = clk(); unsigned v;
            asm volatile("global_load_dword %0, %1, off sc1\n\ts_waitcnt vmcnt(0)" : "=v"(v) : "v"(p + (v0 + k * dv) * 8192 + (i >> 1) * 384 + (i & 1) * 3072) : "memory");
            const unsigned d = (unsigned)(clk() - c0);
            if (l == 0) atomicMin(lat + 8 + v0 + k * dv, d);
        }
        __syncthreads();
        if (dv == 1) {
            a = min(lat[8], lat[10]); b = min(lat[9], lat[11]);
            if (i == 8 || (i >= 2 && min(a, b) < 580 && max(a, b) > min(a, b) + 120)) { dv = 2; v0 = a < b ? 0 : 1; left = NPAIR + 1; }
        }
    }
    const unsigned flip = lat[8 + v0] < lat[10 + v0] ? 0u : 1u;
    if (!hb_wait(ws, h0)) return;
    const unsigned par = a < b ? 0u : 8192u;
    if (tid == 0) { lat[2] = 0; lat[3] = 0; lat[4] = (unsigned)__builtin_amdgcn_s_memrealtime(); }
    __syncthreads();
    unsigned cfgw = par;
    for (int k = 0; k < 4; ++k) cfgw |= ((par >> 13) | (((k ^ (k >> 1) ^ flip) & 1) << 1)) << (16 + 2 * k);
    for (int k = 0; k < 2; ++k) cfgw |= ((par >> 13) | (flip << 1)) << (24 + 2 * k);
    if (tid == 0) st_dev(ws + 1064960, cfgw);
    EVC(3);
    const unsigned t0 = lat[4];
    unsigned seen = w ? 2 : 0;
    const char* f0 = ws + (w ? 1073152 + (w - 1) * 8192 : 1097728) + l * 128;
    const char* f1 = w ? f0 : ws + 1105920 + min(l, 35) * 128;
    for (int i = 0; i < 60000 && seen != 3; ++i) {
        const int m0 = (int)ld_dev(f0), m1 = (int)ld_dev(f1);
        const unsigned fresh = ((__all(m0 >= (w ? 1 : 0)) ? 1u : 0u) | (__all(m1 >= 0) ? 2u : 0u)) & ~seen;
        if (fresh && l == 0) atomicMax(lat + 2, (unsigned)__builtin_amdgcn_s_memrealtime() - t0 + (w ? LEAD_Q : (fresh & 1 ? LEAD_X : LEAD_Y)));
        seen |= fresh;
    }
    if (seen != 3) return;
    unsigned n = 0;
    if (l == 0) n = atomicAdd(lat + 3, 1u);
    if (rfl(n) != 3) return;
    const unsigned t = t0 + atomicMax(lat + 2, 0u);
    if (l < 4) __hip_atomic_store((unsigned long long*)(ws + 89165824 + par + l * 128), (unsigned long long)t << 32, __ATOMIC_RELAXED, __HIP_MEMORY_SCOPE_AGENT);
    if (l == 4) st_dev(ws + 1064960 + 1280, 0u);

}
struct Params {
    const void *x, *mem0, *wq, *wk, *wv, *wo, *w1, *b1, *w2, *b2;
    void* out; char* ws;
    int dx, dy;
};

typedef const __attribute__((address_space(4))) Params* KP;
__device__ __forceinline__ KP args() { KP k = (KP)__builtin_amdgcn_kernarg_segment_ptr(); asm volatile("" : "+s"(k)); return k; }

__global__ void __launch_bounds__(256, 1) rmt_mixer_kernel(Params p) {
    const int b = blockIdx.x, xc = (b & 7) ^ 0, r = b >> 3;
    char* ws = p.ws;
    const int xg = xc & 3;
    if (r >= (xc < 4 ? 25 : 30)) {
        const int k = xc < 4 ? 8 + (r - 25) * 4 + xg : (r - 30) * 4 + xg;
        if (k == 35) role_probe(ws);
        else if (k >= 32) role_lead(ws, k - 32);
        else {
            const unsigned h0 = hb_read(ws);
            if (k >= 28) code_pull();
            if (k < 4) code_pull2();
            char* fl = ws + 1111040 + k * 128;
#pragma unroll 1
            for (int ph = 0; ph < 2; ++ph) {
                ln_groups(ws, p.x, k + 32 * ph, 32, ph ? NLH : 1);
                if (threadIdx.x < 64 && (ph || hb_wait(ws, h0)) && threadIdx.x == 0) st_dev(fl, 3 * ph);
                if (ph == 0) EVC(3);
            }
            if (k < 8) {
                EVC(4);
                const int par = wt_par(ws);
#pragma unroll 1
                for (int Jq = 0; Jq < 9; ++Jq) { KP kp = args(); role_wt(kp->ws, kp->wo, par, Jq, k); }
                asm volatile("s_waitcnt vmcnt(0)\n\ts_barrier" ::: "memory");
                if (threadIdx.x == 0) st_dev(ws + 1067008 + k * 128, 1u);
                EVC(5);
            }
            if (k == 31) { __builtin_amdgcn_s_sleep(60); role_map(ws); }
        }
    } else if (xc < 4) {
        if (r < 16) { KP k = args(); role_x(k->ws, k->wq, k->wk, k->wv, (XMAP ? ((xc + (r >> 1)) & 3) : xc) * 16 + r, k->dx, k->dy, xc); }
        else { KP k = args(); role_y(k->ws, k->wo, k->mem0, ((xc >> 1) * 9 + r - 16) * 2 + (xc & 1)); }
    } else if (r < 18) {
        const unsigned h0 = hb_read(ws);
        role_touch(p.wq, p.wk, p.wv, p.wo, xg, r);
        __builtin_amdgcn_s_sleep(T1_SLEEP);
        if (r < 16) ln_groups(ws, p.x, 32 + 32 * NLH + r * 4 + xg, 64, (7 - NLH) / 2);
        if (!hb_wait(ws, h0)) return;
        __syncthreads();
        if (threadIdx.x == 0) st_dev(ws + 1111040 + (32 + xg * 18 + r) * 128, 3u);
        { KP k = args(); role_b1(k->ws, k->w1, k->b1, xg, r); }
        { KP k = args(); role_b2(k->ws, k->w2, xg, r); }
        { KP k = args(); role_o(k->ws, k->x, k->b2, k->out, xg, r); }
    } else {
        const int q = (r - 18) * 4 + (QMAP ? ((xg + (r - 18) + ((r - 18) >> 2)) & 3) : xg);
        KP k = args();
        role_qkv(k->ws, q < 16 ? k->wq : (q < 32 ? k->wk : k->wv), q);
    }
}

static void launch(void* const* d_in, void* d_out, void* d_ws, hipStream_t stream, int dx, int dy) {
    constexpr int kLds = 147456;
    Params p;
    p.x = d_in[0]; p.mem0 = d_in[1]; p.wq = d_in[2]; p.wk = d_in[3]; p.wv = d_in[4]; p.wo = d_in[5];
    p.w1 = d_in[8]; p.b1 = d_in[9]; p.w2 = d_in[10]; p.b2 = d_in[11];
    p.out = d_out; p.ws = (char*)d_ws; p.dx = dx; p.dy = dy;
    (void)hipFuncSetAttribute((const void*)rmt_mixer_kernel, hipFuncAttributeMaxDynamicSharedMemorySize, kLds);
    rmt_mixer_kernel<<<256, 256, kLds, stream>>>(p);
}
extern "C" void kernel_launch(void* const* d_in, const int* in_sizes, int n_in, void* d_out, int out_size, void* d_ws, size_t ws_size, hipStream_t stream) {
    launch(d_in, d_out, d_ws, stream, 168, 152);
}
```
